```python
import torch

def _softmax(x, axis=-1):
    return torch.softmax(x, dim=axis)
def _gelu(x):
    return torch.nn.functional.gelu(x, approximate="tanh")
def _silu(x):
    return torch.nn.functional.silu(x)
def _sigmoid(x):
    return torch.sigmoid(x)
def _layernorm(x, axis=-1, eps=1e-5):
    m = x.mean(dim=axis, keepdim=True)
    v = x.var(dim=axis, keepdim=True, unbiased=False)
    return (x - m) / torch.sqrt(v + eps)
def _scatter_add(tgt, idx, src, axis=0):
    return tgt.clone().index_add_(axis, idx, src)
def _segment_sum(src, seg, n):
    out = torch.zeros((n,) + src.shape[1:], dtype=src.dtype)
    return out.index_add_(0, seg, src)
def _pad(x, pw):
    flat = tuple(v for pair in reversed(pw) for v in pair)
    return torch.nn.functional.pad(x, flat)

kernel_name = 'standalone__gqa_block_cached__near_ridge'

def setup_inputs():

    _w = (lambda a: (lambda t: t.to(torch.bfloat16) if t.is_floating_point() else t)(torch.as_tensor(a)))
    B, Sq, D = 8, 128, 512
    Hq, Hkv, Sk, Dh = 32, 32, 2048, 128
    assert Hq % Hkv == 0, (Hq, Hkv)
    return {
        "x": _w((lambda *s: torch.randn(*s, dtype=torch.bfloat16) * 0.8)(B, Sq, D)),
        "k_cache": _w((lambda *s: torch.randn(*s, dtype=torch.bfloat16) * 0.8)(B, Hkv, Sk, Dh)),
        "v_cache": _w((lambda *s: torch.randn(*s, dtype=torch.bfloat16) * 0.05)(B, Hkv, Sk, Dh)),
        "wq": _w((lambda *s: torch.randn(*s, dtype=torch.bfloat16) * 0.8)(D, Hq * Dh) * (D ** -0.5)),
        "wo": _w((lambda *s: torch.randn(*s, dtype=torch.bfloat16) * 0.05)(Hq * Dh, D) * ((Hq * Dh) ** -0.5)),
    }

def reference(x, k_cache, v_cache, wq, wo):
    B, Sq, D = x.shape
    Hq, Dh = 32, 128
    Hkv = k_cache.shape[1]
    rep = Hq // Hkv
    q = (lambda x, *d: x.permute(*d))((x @ wq).reshape(B, Sq, Hq, Dh), 0, 2, 1, 3)
    k = (lambda x, n, axis: torch.repeat_interleave(x, n, dim=axis))(k_cache, rep, axis=1)
    v = (lambda x, n, axis: torch.repeat_interleave(x, n, dim=axis))(v_cache, rep, axis=1)
    scores = torch.einsum("bhqd,bhkd->bhqk", q, k) * (Dh ** -0.5)
    p = _softmax(scores, axis=-1)
    o = torch.einsum("bhqk,bhkd->bhqd", p, v)
    return (lambda x, *d: x.permute(*d))(o, 0, 2, 1, 3).reshape(B, Sq, Hq * Dh) @ wo
```

```cpp
#include <hip/hip_runtime.h>
#include <hip/hip_bf16.h>
#include <stdint.h>


typedef __attribute__((ext_vector_type(8))) short bf16x8;
typedef __attribute__((ext_vector_type(4))) short s16x4;
typedef __attribute__((ext_vector_type(16))) float f32x16;
typedef __attribute__((ext_vector_type(4))) float f32x4;
typedef __attribute__((ext_vector_type(4))) unsigned u32x4;
typedef __attribute__((ext_vector_type(2))) unsigned u32x2;
typedef __attribute__((address_space(3))) char lds_char;
typedef short v4i16_t __attribute__((ext_vector_type(4)));

constexpr int NB = 8, SQ = 128, DM = 512, NH = 32, SK = 2048, DH = 128;
constexpr int NT = SK / 64;
constexpr int STAGE = 32768;
constexpr int LDS1 = 163840;
constexpr int WQ_OFF = 98304;
constexpr int XCH_OFF = 131072;
constexpr int LDS2 = 131072;
constexpr float CSC = 0.08838834764831845f * 1.4426950408889634f;

#define MFMA32(a, b, c) __builtin_amdgcn_mfma_f32_32x32x16_bf16(a, b, c, 0, 0, 0)

typedef __attribute__((ext_vector_type(2))) float f32x2;
typedef __attribute__((ext_vector_type(2))) __bf16 bf16x2_t;
__device__ __forceinline__ unsigned cvtpk(float lo, float hi) {
  f32x2 v = {lo, hi};
  return __builtin_bit_cast(unsigned, __builtin_convertvector(v, bf16x2_t));
}
__device__ __forceinline__ bf16x8 pack8(float a0, float a1, float a2, float a3, float a4, float a5, float a6, float a7) {
  u32x4 w = {cvtpk(a0, a1), cvtpk(a2, a3), cvtpk(a4, a5), cvtpk(a6, a7)};
  return __builtin_bit_cast(bf16x8, w);
}
__device__ __forceinline__ bf16x8 lds_r128(lds_char* p) { return *(__attribute__((address_space(3))) bf16x8*)p; }
__device__ __forceinline__ s16x4 lds_tr(lds_char* p) {
  return __builtin_bit_cast(s16x4, __builtin_amdgcn_ds_read_tr16_b64_v4i16((__attribute__((address_space(3))) v4i16_t*)p));
}
__device__ __forceinline__ bf16x8 cat8(s16x4 a, s16x4 b) { return __builtin_shufflevector(a, b, 0, 1, 2, 3, 4, 5, 6, 7); }

__device__ __forceinline__ void dma4(unsigned o0, unsigned o1, unsigned o2, unsigned o3, const void* sbase, unsigned ldsaddr) {
  unsigned keep;
  asm volatile(
      "s_mov_b32 %0, m0\n\t"
      "s_mov_b32 m0, %6\n\t"
      "s_nop 0\n\t"
      "global_load_lds_dwordx4 %1, %5\n\t"
      "global_load_lds_dwordx4 %2, %5 offset:1024\n\t"
      "global_load_lds_dwordx4 %3, %5 offset:2048\n\t"
      "global_load_lds_dwordx4 %4, %5 offset:3072\n\t"
      "s_mov_b32 m0, %0"
      : "=&s"(keep) : "v"(o0), "v"(o1), "v"(o2), "v"(o3), "s"(sbase), "s"(ldsaddr) : "memory");
}
__device__ __forceinline__ void dma1(unsigned o0, const void* sbase, unsigned ldsaddr) {
  unsigned keep;
  asm volatile(
      "s_mov_b32 %0, m0\n\t"
      "s_mov_b32 m0, %3\n\t"
      "s_nop 0\n\t"
      "global_load_lds_dwordx4 %1, %2\n\t"
      "s_mov_b32 m0, %0"
      : "=&s"(keep) : "v"(o0), "s"(sbase), "s"(ldsaddr) : "memory");
}
#define WAITVM(n) asm volatile("s_waitcnt vmcnt(" #n ")" ::: "memory")
#define BARRIER() asm volatile("s_waitcnt lgkmcnt(0)\n\ts_barrier" ::: "memory")
#define PIN(x) asm volatile("" : "+v"(x))

__device__ __forceinline__ float swapmax(float v) {
  auto rr = __builtin_amdgcn_permlane32_swap(__float_as_uint(v), __float_as_uint(v), false, false);
  return fmaxf(__uint_as_float(rr[0]), __uint_as_float(rr[1]));
}
__device__ __forceinline__ float swapadd(float v) {
  auto rr = __builtin_amdgcn_permlane32_swap(__float_as_uint(v), __float_as_uint(v), false, false);
  return __uint_as_float(rr[0]) + __uint_as_float(rr[1]);
}

__global__ __launch_bounds__(512, 2) void attn_kernel(const __hip_bfloat16* __restrict__ x, const __hip_bfloat16* __restrict__ kc,
                                                      const __hip_bfloat16* __restrict__ vc, const __hip_bfloat16* __restrict__ wq,
                                                      __hip_bfloat16* __restrict__ ows) {
  extern __shared__ __attribute__((aligned(16))) char smem[];
  lds_char* L = (lds_char*)smem;
  const unsigned L0 = (unsigned)(uintptr_t)smem;
  const int tid = threadIdx.x, lane = tid & 63;
  const int w = __builtin_amdgcn_readfirstlane(tid >> 6);
  const int qg = w & 3, kh = w >> 2;
  const int r32 = lane & 31, hi = lane >> 5;
  const int q4 = (lane & 15) >> 2, p4 = lane & 3, g1 = (lane >> 4) & 1;
  const int bid = blockIdx.x;
  const int head = 4 * (bid & 7) + ((bid >> 3) & 3), b = bid >> 5;
  const char* kbase = (const char*)kc + ((size_t)(b * NH + head) * SK) * 256;
  const char* vbase = (const char*)vc + ((size_t)(b * NH + head) * SK) * 256;
  const char* wqbase = (const char*)wq + head * 256;

  bf16x8 xf[32];
  {
    const char* xb = (const char*)x + (size_t)(b * SQ + qg * 32) * 1024;
    const unsigned xo = r32 * 1024 + hi * 16;
#pragma unroll
    for (int s = 0; s < 32; ++s)
      asm volatile("global_load_dwordx4 %0, %1, %2 offset:%3" : "=v"(xf[s]) : "v"(xo), "s"(xb), "i"(s * 32) : "memory");
  }
  const int drow = lane >> 4, dpos = lane & 15;
  unsigned ko[4];
#pragma unroll
  for (int j = 0; j < 4; ++j) ko[j] = (unsigned)(((w & 3) * 16 + drow) * 256 + ((dpos ^ (4 * j + drow)) << 4));
  const unsigned vo = (unsigned)(((w & 3) * 16 + drow) * 256 + ((dpos ^ (drow << 2)) << 4));
  unsigned wo4[4];
#pragma unroll
  for (int j = 0; j < 4; ++j) wo4[j] = (unsigned)(((w & 3) * 16 + 4 * j + drow) * 8192 + ((dpos ^ (drow << 2)) << 4) - j * 1024);

#define ISSUE_STAGE(t) do { const unsigned la_ = L0 + ((t) & 3) * STAGE + (w & 3) * 4096; \
    dma4(ko[0], ko[1], ko[2], ko[3], kbase + (size_t)(t) * 16384, la_); \
    dma4(vo, vo, vo, vo, vbase + (size_t)(t) * 16384, la_ + 16384); } while (0)
#define ISSUE_WQ(c) dma4(wo4[0], wo4[1], wo4[2], wo4[3], wqbase + (size_t)(c) * 64 * 8192, L0 + WQ_OFF + ((c) & 3) * 16384 + (w & 3) * 4096)

  if (w < 4) { ISSUE_STAGE(0); ISSUE_STAGE(1); ISSUE_STAGE(2); WAITVM(24); }
  else { ISSUE_WQ(0); ISSUE_WQ(1); ISSUE_WQ(2); }

  f32x16 qa[2] = {};
  {
    const int ps = ((p4 & 1) << 1) | (p4 >> 1);
    int wa[2];
#pragma unroll
    for (int dd = 0; dd < 2; ++dd) {
      const int dblk = 2 * kh + dd;
      wa[dd] = (8 * hi + q4) * 256 + (((4 * dblk + 2 * g1 + (ps >> 1)) ^ (q4 << 2)) << 4) + 8 * (ps & 1);
    }
#pragma unroll
    for (int c = 0; c < 8; ++c) {
      if (w >= 4) { if (c < 6) WAITVM(8); else if (c == 6) WAITVM(4); else WAITVM(0); }
      BARRIER();
      if (c == 0) {
#pragma unroll
        for (int s = 0; s < 32; ++s) PIN(xf[s]);
      }
      if (w >= 4 && c + 3 < 8) ISSUE_WQ(c + 3);
      lds_char* wb = L + WQ_OFF + (c & 3) * 16384;
#pragma unroll
      for (int s = 0; s < 4; ++s) {
#pragma unroll
        for (int dd = 0; dd < 2; ++dd) {
          s16x4 a0 = lds_tr(wb + wa[dd] + s * 4096);
          s16x4 a1 = lds_tr(wb + wa[dd] + s * 4096 + 1024);
          qa[dd] = MFMA32(cat8(a0, a1), xf[c * 4 + s], qa[dd]);
        }
      }
    }
  }
  bf16x8 qf[8];
  {
    bf16x8 own[4];
#pragma unroll
    for (int dd = 0; dd < 2; ++dd)
#pragma unroll
      for (int sp = 0; sp < 2; ++sp)
        own[dd * 2 + sp] = pack8(qa[dd][8 * sp + 0], qa[dd][8 * sp + 1], qa[dd][8 * sp + 2], qa[dd][8 * sp + 3],
                                 qa[dd][8 * sp + 4], qa[dd][8 * sp + 5], qa[dd][8 * sp + 6], qa[dd][8 * sp + 7]);
    BARRIER();
#pragma unroll
    for (int i = 0; i < 4; ++i) *(__attribute__((address_space(3))) bf16x8*)(L + XCH_OFF + w * 4096 + i * 1024 + lane * 16) = own[i];
    BARRIER();
#pragma unroll
    for (int i = 0; i < 4; ++i) {
      bf16x8 o = lds_r128(L + XCH_OFF + (w ^ 4) * 4096 + i * 1024 + lane * 16);
      if (kh == 0) { qf[i] = own[i]; qf[4 + i] = o; } else { qf[4 + i] = own[i]; qf[i] = o; }
    }
  }

  f32x16 oacc[4] = {};
  float m = -__builtin_inff(), l = 0.f;
  const int krow = kh * 32 + r32;
  const int kaddr0 = krow * 256 + ((hi ^ (krow & 15)) << 4);
  int vaddr0[4];
#pragma unroll
  for (int d = 0; d < 4; ++d) vaddr0[d] = 16384 + (kh * 32 + 4 * hi + q4) * 256 + ((d ^ q4) << 6) + g1 * 32 + p4 * 8;

  for (int it = 0; it < NT; ++it) {
    if (w < 4) { if (it < NT - 2) WAITVM(16); else if (it == NT - 2) WAITVM(8); else WAITVM(0); }
    BARRIER();
    if (w < 4 && it + 3 < NT) ISSUE_STAGE(it + 3);
    lds_char* sb = L + (it & 3) * STAGE;
    bf16x8 kf[8];
#pragma unroll
    for (int s = 0; s < 8; ++s) kf[s] = lds_r128(sb + (kaddr0 ^ (s << 5)));
    f32x16 sc = {};
#pragma unroll
    for (int s = 0; s < 8; ++s) sc = MFMA32(kf[s], qf[s], sc);
    bf16x8 vf[4][2];
#pragma unroll
    for (int d = 0; d < 4; ++d)
#pragma unroll
      for (int s = 0; s < 2; ++s)
        vf[d][s] = cat8(lds_tr(sb + vaddr0[d] + s * 4096), lds_tr(sb + vaddr0[d] + s * 4096 + 2048));
    float tmax = sc[0];
#pragma unroll
    for (int r = 1; r < 16; ++r) tmax = fmaxf(tmax, sc[r]);
    tmax = swapmax(tmax);
    const float mn = fmaxf(m, tmax * CSC);
    const float alpha = __builtin_amdgcn_exp2f(m - mn);
    m = mn;
    float ps = 0.f;
#pragma unroll
    for (int r = 0; r < 16; ++r) { sc[r] = __builtin_amdgcn_exp2f(fmaf(sc[r], CSC, -mn)); ps += sc[r]; }
    l = l * alpha + ps;
    if (__any(alpha < 1.f)) {
#pragma unroll
      for (int d = 0; d < 4; ++d)
#pragma unroll
        for (int r = 0; r < 16; ++r) oacc[d][r] *= alpha;
    }
    bf16x8 pf[2];
#pragma unroll
    for (int s = 0; s < 2; ++s)
      pf[s] = pack8(sc[8 * s + 0], sc[8 * s + 1], sc[8 * s + 2], sc[8 * s + 3], sc[8 * s + 4], sc[8 * s + 5], sc[8 * s + 6], sc[8 * s + 7]);
#pragma unroll
    for (int d = 0; d < 4; ++d)
#pragma unroll
      for (int s = 0; s < 2; ++s) oacc[d] = MFMA32(vf[d][s], pf[s], oacc[d]);
  }

  l = swapadd(l);
  {
    float* ml = (float*)(smem + XCH_OFF);
    if (hi == 0) { ml[w * 64 + r32] = m; ml[w * 64 + 32 + r32] = l; }
    const int dsend = kh ? 0 : 2;
#pragma unroll
    for (int dd = 0; dd < 2; ++dd)
#pragma unroll
      for (int g = 0; g < 4; ++g) {
        f32x4 v;
        if (kh) { v[0] = oacc[dd][4 * g]; v[1] = oacc[dd][4 * g + 1]; v[2] = oacc[dd][4 * g + 2]; v[3] = oacc[dd][4 * g + 3]; }
        else { v[0] = oacc[2 + dd][4 * g]; v[1] = oacc[2 + dd][4 * g + 1]; v[2] = oacc[2 + dd][4 * g + 2]; v[3] = oacc[2 + dd][4 * g + 3]; }
        *(__attribute__((address_space(3))) f32x4*)(L + w * 8192 + (dd * 4 + g) * 1024 + lane * 16) = v;
      }
    (void)dsend;
    BARRIER();
    const float mp = ml[(w ^ 4) * 64 + r32], lp = ml[(w ^ 4) * 64 + 32 + r32];
    const float mf = fmaxf(m, mp);
    const float wa = __builtin_amdgcn_exp2f(m - mf), wb = __builtin_amdgcn_exp2f(mp - mf);
    const float inv = 1.f / (wa * l + wb * lp);
    const float fa = wa * inv, fb = wb * inv;
    const int mblk = b * 4 + qg;
#pragma unroll
    for (int dd = 0; dd < 2; ++dd) {
      float o[16];
#pragma unroll
      for (int g = 0; g < 4; ++g) {
        f32x4 v = *(__attribute__((address_space(3))) f32x4*)(L + (w ^ 4) * 8192 + (dd * 4 + g) * 1024 + lane * 16);
#pragma unroll
        for (int e = 0; e < 4; ++e) {
          const float own = kh ? oacc[2 + dd][4 * g + e] : oacc[dd][4 * g + e];
          o[4 * g + e] = own * fa + v[e] * fb;
        }
      }
      const int dblk = 2 * kh + dd;
#pragma unroll
      for (int sp = 0; sp < 2; ++sp) {
        bf16x8 f = pack8(o[8 * sp + 0], o[8 * sp + 1], o[8 * sp + 2], o[8 * sp + 3], o[8 * sp + 4], o[8 * sp + 5], o[8 * sp + 6], o[8 * sp + 7]);
        const size_t ks = (size_t)mblk * 256 + head * 8 + dblk * 2 + sp;
        *(bf16x8*)((char*)ows + (ks * 64 + lane) * 16) = f;
      }
    }
  }
}

__global__ __launch_bounds__(512, 2) void oproj_kernel(const __hip_bfloat16* __restrict__ ows, const __hip_bfloat16* __restrict__ wo,
                                                       __hip_bfloat16* __restrict__ out) {
  extern __shared__ __attribute__((aligned(16))) char smem[];
  lds_char* L = (lds_char*)smem;
  const unsigned L0 = (unsigned)(uintptr_t)smem;
  const int tid = threadIdx.x, lane = tid & 63;
  const int w = __builtin_amdgcn_readfirstlane(tid >> 6);
  const int r32 = lane & 31, hi = lane >> 5;
  const int q4 = (lane & 15) >> 2, p4 = lane & 3, g1 = (lane >> 4) & 1;
  const int bid = blockIdx.x;
  const int mblk = (bid & 7) * 4 + ((bid >> 3) & 3), nblk = bid >> 5;

  bf16x8 of[32];
  {
    const char* ob = (const char*)ows + ((size_t)mblk * 256 + w * 32) * 1024;
    const unsigned oo = lane * 16;
#pragma unroll
    for (int t = 0; t < 32; ++t) {
      const char* obt = ob + (t >> 2) * 4096;
      asm volatile("global_load_dwordx4 %0, %1, %2 offset:%3" : "=v"(of[t]) : "v"(oo), "s"(obt), "i"((t & 3) * 1024) : "memory");
    }
  }
  const char* wb = (const char*)wo + (size_t)(w * 512) * 1024 + nblk * 128;
  const int drow = lane >> 3, dpos = lane & 7;
  const unsigned wof = (unsigned)(drow * 1024 + ((dpos ^ (((drow >> 1) & 1) << 2)) << 4));
  const unsigned lw = L0 + w * 16384;
#define ISSUE_WO(c) do { _Pragma("unroll") for (int j_ = 0; j_ < 4; ++j_) \
    dma1(wof, wb + (size_t)((c) * 32 + j_ * 8) * 1024, lw + ((c) & 3) * 4096 + j_ * 1024); } while (0)
  ISSUE_WO(0); ISSUE_WO(1); ISSUE_WO(2);
  f32x16 acc[2] = {};
  int ta[2];
#pragma unroll
  for (int nb = 0; nb < 2; ++nb) ta[nb] = w * 16384 + (4 * hi + q4) * 128 + ((nb ^ (q4 >> 1)) << 6) + g1 * 32 + p4 * 8;
#pragma unroll
  for (int c = 0; c < 16; ++c) {
    if (c < 14) WAITVM(8); else if (c == 14) WAITVM(4); else WAITVM(0);
    if (c == 0) {
#pragma unroll
      for (int t = 0; t < 32; ++t) PIN(of[t]);
    }
    bf16x8 af[2][2];
#pragma unroll
    for (int t2 = 0; t2 < 2; ++t2)
#pragma unroll
      for (int nb = 0; nb < 2; ++nb)
        af[t2][nb] = cat8(lds_tr(L + ta[nb] + (c & 3) * 4096 + t2 * 2048), lds_tr(L + ta[nb] + (c & 3) * 4096 + t2 * 2048 + 1024));
    asm volatile("s_waitcnt lgkmcnt(0)" ::: "memory");
    if (c + 3 < 16) ISSUE_WO(c + 3);
#pragma unroll
    for (int t2 = 0; t2 < 2; ++t2)
#pragma unroll
      for (int nb = 0; nb < 2; ++nb) acc[nb] = MFMA32(af[t2][nb], of[c * 2 + t2], acc[nb]);
  }
  BARRIER();
#pragma unroll
  for (int nb = 0; nb < 2; ++nb)
#pragma unroll
    for (int g = 0; g < 4; ++g) {
      f32x4 v = {acc[nb][4 * g], acc[nb][4 * g + 1], acc[nb][4 * g + 2], acc[nb][4 * g + 3]};
      *(__attribute__((address_space(3))) f32x4*)(L + w * 8192 + (nb * 4 + g) * 1024 + lane * 16) = v;
    }
  BARRIER();
  f32x4 s = {0.f, 0.f, 0.f, 0.f};
#pragma unroll
  for (int ww = 0; ww < 8; ++ww) s += *(__attribute__((address_space(3))) f32x4*)(L + ww * 8192 + w * 1024 + lane * 16);
  const int nb = w >> 2, g = w & 3;
  u32x2 pk = {cvtpk(s[0], s[1]), cvtpk(s[2], s[3])};
  *(u32x2*)((char*)out + ((size_t)(mblk * 32 + r32) * 512 + nblk * 64 + nb * 32 + 8 * g + 4 * hi) * 2) = pk;
}

extern "C" void kernel_launch(void* const* d_in, const int* in_sizes, int n_in, void* d_out, int out_size, void* d_ws, size_t ws_size,
                              hipStream_t stream) {
  const __hip_bfloat16* x = (const __hip_bfloat16*)d_in[0];
  const __hip_bfloat16* kc = (const __hip_bfloat16*)d_in[1];
  const __hip_bfloat16* vc = (const __hip_bfloat16*)d_in[2];
  const __hip_bfloat16* wq = (const __hip_bfloat16*)d_in[3];
  const __hip_bfloat16* wo = (const __hip_bfloat16*)d_in[4];
  __hip_bfloat16* out = (__hip_bfloat16*)d_out;
  __hip_bfloat16* ows = (__hip_bfloat16*)d_ws;
  (void)hipFuncSetAttribute((const void*)attn_kernel, hipFuncAttributeMaxDynamicSharedMemorySize, LDS1);
  (void)hipFuncSetAttribute((const void*)oproj_kernel, hipFuncAttributeMaxDynamicSharedMemorySize, LDS2);
  attn_kernel<<<256, 512, LDS1, stream>>>(x, kc, vc, wq, ows);
  oproj_kernel<<<256, 512, LDS2, stream>>>(ows, wo, out);
}
```

```cpp
#include <hip/hip_runtime.h>
#include <hip/hip_bf16.h>
#include <stdint.h>

typedef __attribute__((ext_vector_type(8))) short bf16x8;
typedef __attribute__((ext_vector_type(4))) short s16x4;
typedef __attribute__((ext_vector_type(16))) float f32x16;
typedef __attribute__((ext_vector_type(4))) float f32x4;
typedef __attribute__((ext_vector_type(4))) unsigned u32x4;
typedef __attribute__((ext_vector_type(2))) unsigned u32x2;
typedef __attribute__((address_space(3))) char lds_char;
typedef short v4i16_t __attribute__((ext_vector_type(4)));

constexpr int NB = 8, SQ = 128, DM = 512, NH = 32, SK = 2048, DH = 128;
constexpr int NT = SK / 64;
constexpr int STAGE = 32768;
constexpr int LDS1 = 163840;
constexpr int K0_OFF = 65536;
constexpr int P_OFF = 81920;
constexpr int XCH_OFF = 131072;
constexpr int LDS2 = 163840;
constexpr float CSC = 0.08838834764831845f * 1.4426950408889634f;

#define MFMA32(a, b, c) __builtin_amdgcn_mfma_f32_32x32x16_bf16(a, b, c, 0, 0, 0)

typedef __attribute__((ext_vector_type(2))) float f32x2;
typedef __attribute__((ext_vector_type(2))) __bf16 bf16x2_t;
__device__ __forceinline__ unsigned cvtpk(float lo, float hi) {
  f32x2 v = {lo, hi};
  return __builtin_bit_cast(unsigned, __builtin_convertvector(v, bf16x2_t));
}
__device__ __forceinline__ bf16x8 pack8(float a0, float a1, float a2, float a3, float a4, float a5, float a6, float a7) {
  u32x4 w = {cvtpk(a0, a1), cvtpk(a2, a3), cvtpk(a4, a5), cvtpk(a6, a7)};
  return __builtin_bit_cast(bf16x8, w);
}
__device__ __forceinline__ bf16x8 lds_r128(lds_char* p) { return *(__attribute__((address_space(3))) bf16x8*)p; }
__device__ __forceinline__ s16x4 lds_tr(lds_char* p) {
  return __builtin_bit_cast(s16x4, __builtin_amdgcn_ds_read_tr16_b64_v4i16((__attribute__((address_space(3))) v4i16_t*)p));
}
__device__ __forceinline__ bf16x8 cat8(s16x4 a, s16x4 b) { return __builtin_shufflevector(a, b, 0, 1, 2, 3, 4, 5, 6, 7); }

__device__ __forceinline__ void dma4(unsigned o0, unsigned o1, unsigned o2, unsigned o3, const void* sbase, unsigned ldsaddr) {
  unsigned keep;
  asm volatile(
      "s_mov_b32 %0, m0\n\t"
      "s_mov_b32 m0, %6\n\t"
      "s_nop 0\n\t"
      "global_load_lds_dwordx4 %1, %5\n\t"
      "global_load_lds_dwordx4 %2, %5 offset:1024\n\t"
      "global_load_lds_dwordx4 %3, %5 offset:2048\n\t"
      "global_load_lds_dwordx4 %4, %5 offset:3072\n\t"
      "s_mov_b32 m0, %0"
      : "=&s"(keep) : "v"(o0), "v"(o1), "v"(o2), "v"(o3), "s"(sbase), "s"(ldsaddr) : "memory");
}
__device__ __forceinline__ void dma2(unsigned o0, unsigned o1, const void* sbase, unsigned ldsaddr) {
  unsigned keep;
  asm volatile(
      "s_mov_b32 %0, m0\n\t"
      "s_mov_b32 m0, %4\n\t"
      "s_nop 0\n\t"
      "global_load_lds_dwordx4 %1, %3\n\t"
      "global_load_lds_dwordx4 %2, %3 offset:1024\n\t"
      "s_mov_b32 m0, %0"
      : "=&s"(keep) : "v"(o0), "v"(o1), "s"(sbase), "s"(ldsaddr) : "memory");
}
__device__ __forceinline__ void dma1(unsigned o0, const void* sbase, unsigned ldsaddr) {
  unsigned keep;
  asm volatile(
      "s_mov_b32 %0, m0\n\t"
      "s_mov_b32 m0, %3\n\t"
      "s_nop 0\n\t"
      "global_load_lds_dwordx4 %1, %2\n\t"
      "s_mov_b32 m0, %0"
      : "=&s"(keep) : "v"(o0), "s"(sbase), "s"(ldsaddr) : "memory");
}
#define WAITVM(n) asm volatile("s_waitcnt vmcnt(" #n ")" ::: "memory")
#define WAITVML(n) asm volatile("s_waitcnt vmcnt(" #n ") lgkmcnt(0)" ::: "memory")
#define BARRIER() asm volatile("s_waitcnt lgkmcnt(0)\n\ts_barrier" ::: "memory")
#define PIN(x) asm volatile("" : "+v"(x))

__device__ __forceinline__ float swapmax(float v) {
  auto rr = __builtin_amdgcn_permlane32_swap(__float_as_uint(v), __float_as_uint(v), false, false);
  return fmaxf(__uint_as_float(rr[0]), __uint_as_float(rr[1]));
}
__device__ __forceinline__ float swapadd(float v) {
  auto rr = __builtin_amdgcn_permlane32_swap(__float_as_uint(v), __float_as_uint(v), false, false);
  return __uint_as_float(rr[0]) + __uint_as_float(rr[1]);
}

__global__ __launch_bounds__(512, 2) void attn_kernel(const __hip_bfloat16* __restrict__ x, const __hip_bfloat16* __restrict__ kc,
                                                      const __hip_bfloat16* __restrict__ vc, const __hip_bfloat16* __restrict__ wq,
                                                      __hip_bfloat16* __restrict__ ows) {
  extern __shared__ __attribute__((aligned(16))) char smem[];
  lds_char* L = (lds_char*)smem;
  const unsigned L0 = (unsigned)(uintptr_t)smem;
  const int tid = threadIdx.x, lane = tid & 63;
  const int w = __builtin_amdgcn_readfirstlane(tid >> 6);
  const int qg = w & 3, kh = w >> 2;
  const int r32 = lane & 31, hi = lane >> 5;
  const int q4 = (lane & 15) >> 2, p4 = lane & 3, g1 = (lane >> 4) & 1;
  const int bid = blockIdx.x;
  const int head = 4 * (bid & 7) + ((bid >> 3) & 3), b = bid >> 5;
  const char* kbase = (const char*)kc + ((size_t)(b * NH + head) * SK) * 256;
  const char* vbase = (const char*)vc + ((size_t)(b * NH + head) * SK) * 256;
  const char* wqbase = (const char*)wq + head * 256;

  const int drow = lane >> 4, dpos = lane & 15;
  unsigned ko[4];
#pragma unroll
  for (int j = 0; j < 4; ++j) ko[j] = (unsigned)(((w & 3) * 16 + drow) * 256 + ((dpos ^ (4 * j + drow)) << 4));
  const unsigned vo = (unsigned)(((w & 3) * 16 + drow) * 256 + ((dpos ^ (drow << 2)) << 4));
  unsigned wo2[2], xo2[2];
#pragma unroll
  for (int j = 0; j < 2; ++j) {
    wo2[j] = (unsigned)(((w & 3) * 8 + 4 * j + drow) * 8192 + ((dpos ^ (drow << 2)) << 4) - j * 1024);
    xo2[j] = (unsigned)(((w & 3) * 32 + 16 * j + (lane >> 2)) * 1024 + (((lane & 3) ^ drow) << 4) - j * 1024);
  }
  const char* xbase = (const char*)x + (size_t)b * SQ * 1024;

#define ISSUE_K(t, la) dma4(ko[0], ko[1], ko[2], ko[3], kbase + (size_t)(t) * 16384, (la) + (w & 3) * 4096)
#define ISSUE_V(t, la) dma4(vo, vo, vo, vo, vbase + (size_t)(t) * 16384, (la) + (w & 3) * 4096)
#define ISSUE_P(c) do { dma2(wo2[0], wo2[1], wqbase + (size_t)(c) * 32 * 8192, L0 + P_OFF + ((c) % 5) * 16384 + (w & 3) * 2048); \
    dma2(xo2[0], xo2[1], xbase + (c) * 64, L0 + P_OFF + ((c) % 5) * 16384 + 8192 + (w & 3) * 2048); } while (0)

  if (w < 4) {
    ISSUE_K(0, L0 + K0_OFF);
#pragma unroll
    for (int s = 0; s < 2; ++s) { ISSUE_K(s + 1, L0 + s * STAGE); ISSUE_V(s, L0 + s * STAGE + 16384); }
  } else { ISSUE_P(0); ISSUE_P(1); ISSUE_P(2); ISSUE_P(3); }

  f32x16 qa[2] = {};
  {
    const int ps = ((p4 & 1) << 1) | (p4 >> 1);
    int wa[2], xa[2];
#pragma unroll
    for (int dd = 0; dd < 2; ++dd) {
      const int dblk = 2 * kh + dd;
      wa[dd] = (8 * hi + q4) * 256 + (((4 * dblk + 2 * g1 + (ps >> 1)) ^ (q4 << 2)) << 4) + 8 * (ps & 1);
      xa[dd] = 8192 + (qg * 32 + r32) * 64 + (((2 * dd + hi) ^ ((r32 >> 2) & 3)) << 4);
    }
#pragma unroll
    for (int c = 0; c < 16; ++c) {
      if (w >= 4) { if (c < 13) WAITVM(12); else if (c == 13) WAITVM(8); else if (c == 14) WAITVM(4); else WAITVM(0); }
      BARRIER();
      if (w >= 4 && c + 4 < 16) ISSUE_P(c + 4);
      lds_char* wb = L + P_OFF + (c % 5) * 16384;
#pragma unroll
      for (int s = 0; s < 2; ++s) {
        const bf16x8 xf = lds_r128(wb + xa[s]);
#pragma unroll
        for (int dd = 0; dd < 2; ++dd) {
          s16x4 a0 = lds_tr(wb + wa[dd] + s * 4096);
          s16x4 a1 = lds_tr(wb + wa[dd] + s * 4096 + 1024);
          qa[dd] = MFMA32(cat8(a0, a1), xf, qa[dd]);
        }
      }
    }
  }
  bf16x8 qf[8];
  {
    bf16x8 own[4];
#pragma unroll
    for (int dd = 0; dd < 2; ++dd)
#pragma unroll
      for (int sp = 0; sp < 2; ++sp)
        own[dd * 2 + sp] = pack8(qa[dd][8 * sp + 0], qa[dd][8 * sp + 1], qa[dd][8 * sp + 2], qa[dd][8 * sp + 3],
                                 qa[dd][8 * sp + 4], qa[dd][8 * sp + 5], qa[dd][8 * sp + 6], qa[dd][8 * sp + 7]);
    BARRIER();
#pragma unroll
    for (int i = 0; i < 4; ++i) *(__attribute__((address_space(3))) bf16x8*)(L + XCH_OFF + w * 4096 + i * 1024 + lane * 16) = own[i];
    if (w < 4) WAITVM(16);
    BARRIER();
#pragma unroll
    for (int i = 0; i < 4; ++i) {
      bf16x8 o = lds_r128(L + XCH_OFF + (w ^ 4) * 4096 + i * 1024 + lane * 16);
      if (kh == 0) { qf[i] = own[i]; qf[4 + i] = o; } else { qf[4 + i] = own[i]; qf[i] = o; }
    }
  }

  f32x16 oacc[4] = {};
  const int krow = kh * 32 + r32;
  const int kaddr0 = krow * 256 + ((hi ^ (krow & 15)) << 4);
  int vaddr0[4];
#pragma unroll
  for (int d = 0; d < 4; ++d) vaddr0[d] = 16384 + (kh * 32 + 4 * hi + q4) * 256 + ((d ^ q4) << 6) + g1 * 32 + p4 * 8;

  f32x16 sc = {};
  {
    bf16x8 kf[8];
#pragma unroll
    for (int s = 0; s < 8; ++s) kf[s] = lds_r128(L + K0_OFF + (kaddr0 ^ (s << 5)));
#pragma unroll
    for (int s = 0; s < 8; ++s) sc = MFMA32(kf[s], qf[s], sc);
  }
  float m, l = 0.f, alpha = 0.f;
  {
    float tmax = sc[0];
#pragma unroll
    for (int r = 1; r < 16; ++r) tmax = fmaxf(tmax, sc[r]);
    m = swapmax(tmax) * CSC;
  }
  int slot = 0;
  bf16x8 kf[8], vf[4][2], pf[2];
  f32x16 sn;
#define READ_K(sb) _Pragma("unroll") for (int s = 0; s < 8; ++s) kf[s] = lds_r128((sb) + (kaddr0 ^ (s << 5)))
#define READ_V(sb) _Pragma("unroll") for (int d = 0; d < 4; ++d) _Pragma("unroll") for (int s = 0; s < 2; ++s) \
        vf[d][s] = cat8(lds_tr((sb) + vaddr0[d] + s * 4096), lds_tr((sb) + vaddr0[d] + s * 4096 + 2048))
#define QK_NEXT() do { sn = (f32x16){}; _Pragma("unroll") for (int s = 0; s < 8; ++s) sn = MFMA32(kf[s], qf[s], sn); } while (0)
#define SOFTMAX() do { \
    const f32x2 al2 = {alpha, alpha}, cs2 = {CSC, CSC}, nm2 = {-m, -m}; \
    _Pragma("unroll") for (int d = 0; d < 4; ++d) _Pragma("unroll") for (int r = 0; r < 16; r += 2) { \
      f32x2 t = {oacc[d][r], oacc[d][r + 1]}; t *= al2; oacc[d][r] = t[0]; oacc[d][r + 1] = t[1]; } \
    f32x2 ps2 = {0.f, 0.f}; \
    _Pragma("unroll") for (int r = 0; r < 16; r += 2) { \
      f32x2 t = {sc[r], sc[r + 1]}; t = __builtin_elementwise_fma(t, cs2, nm2); \
      t[0] = __builtin_amdgcn_exp2f(t[0]); t[1] = __builtin_amdgcn_exp2f(t[1]); ps2 += t; sc[r] = t[0]; sc[r + 1] = t[1]; } \
    l = l * alpha + (ps2[0] + ps2[1]); \
    _Pragma("unroll") for (int s = 0; s < 2; ++s) \
      pf[s] = pack8(sc[8 * s + 0], sc[8 * s + 1], sc[8 * s + 2], sc[8 * s + 3], sc[8 * s + 4], sc[8 * s + 5], sc[8 * s + 6], sc[8 * s + 7]); \
  } while (0)
#define PV() _Pragma("unroll") for (int d = 0; d < 4; ++d) _Pragma("unroll") for (int s = 0; s < 2; ++s) oacc[d] = MFMA32(vf[d][s], pf[s], oacc[d])
#define MAX_NEXT() do { \
    float tmax = sn[0]; \
    _Pragma("unroll") for (int r = 1; r < 16; ++r) tmax = fmaxf(tmax, sn[r]); \
    tmax = swapmax(tmax); \
    const float mn = fmaxf(m, tmax * CSC); \
    alpha = __builtin_amdgcn_exp2f(m - mn); \
    m = mn; sc = sn; } while (0)

  for (int it = 0; it < NT - 1; ++it) {
    if (it == 0) WAITVM(8);
    else if (it < NT - 4) WAITVM(12);
    else if (w < 4) { if (it == NT - 4) WAITVM(8); else if (it == NT - 3) WAITVM(4); else WAITVM(0); }
    else { if (it == NT - 4) WAITVM(12); else if (it == NT - 3) WAITVM(8); else WAITVM(4); }
    BARRIER();
    lds_char* sb = L + slot * STAGE;
    READ_K(sb);
    if (it == 0) {
      if (w < 4) { ISSUE_K(3, L0 + 2 * STAGE); ISSUE_K(4, L0 + 3 * STAGE); ISSUE_K(5, L0 + 4 * STAGE); }
      else { ISSUE_V(2, L0 + 2 * STAGE + 16384); ISSUE_V(3, L0 + 3 * STAGE + 16384); ISSUE_V(4, L0 + 4 * STAGE + 16384); }
    } else if (it + 4 < NT) {
      const unsigned la = L0 + (slot == 0 ? 4 : slot - 1) * STAGE;
      if (w < 4) { if (it + 5 < NT) ISSUE_K(it + 5, la); }
      else ISSUE_V(it + 4, la + 16384);
    }
    slot = (slot == 4) ? 0 : slot + 1;
    READ_V(sb);
    QK_NEXT();
    SOFTMAX();
    PV();
    MAX_NEXT();
  }
  {
    WAITVM(0);
    BARRIER();
    lds_char* sb = L + slot * STAGE;
    READ_V(sb);
    SOFTMAX();
    PV();
  }

  l = swapadd(l);
  {
    float* ml = (float*)(smem + 4 * STAGE);
    lds_char* xo = L + 2 * STAGE;
    if (hi == 0) { ml[w * 64 + r32] = m; ml[w * 64 + 32 + r32] = l; }
#pragma unroll
    for (int dd = 0; dd < 2; ++dd)
#pragma unroll
      for (int g = 0; g < 4; ++g) {
        f32x4 v;
        if (kh) { v[0] = oacc[dd][4 * g]; v[1] = oacc[dd][4 * g + 1]; v[2] = oacc[dd][4 * g + 2]; v[3] = oacc[dd][4 * g + 3]; }
        else { v[0] = oacc[2 + dd][4 * g]; v[1] = oacc[2 + dd][4 * g + 1]; v[2] = oacc[2 + dd][4 * g + 2]; v[3] = oacc[2 + dd][4 * g + 3]; }
        *(__attribute__((address_space(3))) f32x4*)(xo + w * 8192 + (dd * 4 + g) * 1024 + lane * 16) = v;
      }
    BARRIER();
    const float mp = ml[(w ^ 4) * 64 + r32], lp = ml[(w ^ 4) * 64 + 32 + r32];
    const float mf = fmaxf(m, mp);
    const float wa = __builtin_amdgcn_exp2f(m - mf), wb = __builtin_amdgcn_exp2f(mp - mf);
    const float inv = 1.f / (wa * l + wb * lp);
    const float fa = wa * inv, fb = wb * inv;
    const int mblk = b * 4 + qg;
#pragma unroll
    for (int dd = 0; dd < 2; ++dd) {
      float o[16];
#pragma unroll
      for (int g = 0; g < 4; ++g) {
        f32x4 v = *(__attribute__((address_space(3))) f32x4*)(xo + (w ^ 4) * 8192 + (dd * 4 + g) * 1024 + lane * 16);
#pragma unroll
        for (int e = 0; e < 4; ++e) {
          const float own = kh ? oacc[2 + dd][4 * g + e] : oacc[dd][4 * g + e];
          o[4 * g + e] = own * fa + v[e] * fb;
        }
      }
      const int dblk = 2 * kh + dd;
#pragma unroll
      for (int sp = 0; sp < 2; ++sp) {
        bf16x8 f = pack8(o[8 * sp + 0], o[8 * sp + 1], o[8 * sp + 2], o[8 * sp + 3], o[8 * sp + 4], o[8 * sp + 5], o[8 * sp + 6], o[8 * sp + 7]);
        const size_t ks = (size_t)mblk * 256 + head * 8 + dblk * 2 + sp;
        *(bf16x8*)((char*)ows + (ks * 64 + lane) * 16) = f;
      }
    }
  }
}

__global__ __launch_bounds__(512, 2) void oproj_kernel(const __hip_bfloat16* __restrict__ ows, const __hip_bfloat16* __restrict__ wo,
                                                       __hip_bfloat16* __restrict__ out) {
  extern __shared__ __attribute__((aligned(16))) char smem[];
  lds_char* L = (lds_char*)smem;
  const unsigned L0 = (unsigned)(uintptr_t)smem;
  const int tid = threadIdx.x, lane = tid & 63;
  const int w = __builtin_amdgcn_readfirstlane(tid >> 6);
  const int r32 = lane & 31, hi = lane >> 5;
  const int q4 = (lane & 15) >> 2, p4 = lane & 3, g1 = (lane >> 4) & 1;
  const int bid = blockIdx.x;
  const int mblk = (bid & 7) * 4 + ((bid >> 3) & 3), nblk = bid >> 5;

  bf16x8 of[16][2];
  const char* ob = (const char*)ows + ((size_t)mblk * 256 + w * 32) * 1024;
  const unsigned oo = lane * 16;
  const char* wb = (const char*)wo + (size_t)(w * 512) * 1024 + nblk * 128;
  const int drow = lane >> 3, dpos = lane & 7;
  const unsigned wof = (unsigned)(drow * 1024 + ((dpos ^ (((drow >> 1) & 1) << 2)) << 4));
  const unsigned lw = L0 + w * 20480;
#define ISSUE_STEP(c) do { \
    asm volatile("global_load_dwordx4 %0, %1, %2 offset:0" : "=v"(of[c][0]) : "v"(oo), "s"(ob + (c) * 2048) : "memory"); \
    asm volatile("global_load_dwordx4 %0, %1, %2 offset:1024" : "=v"(of[c][1]) : "v"(oo), "s"(ob + (c) * 2048) : "memory"); \
    _Pragma("unroll") for (int j_ = 0; j_ < 4; ++j_) \
      dma1(wof, wb + (size_t)((c) * 32 + j_ * 8) * 1024, lw + ((c) % 5) * 4096 + j_ * 1024); } while (0)
  ISSUE_STEP(0); ISSUE_STEP(1); ISSUE_STEP(2); ISSUE_STEP(3);
  f32x16 acc[2] = {};
  int ta[2];
#pragma unroll
  for (int nb = 0; nb < 2; ++nb) ta[nb] = w * 20480 + (4 * hi + q4) * 128 + ((nb ^ (q4 >> 1)) << 6) + g1 * 32 + p4 * 8;
#pragma unroll
  for (int c = 0; c < 16; ++c) {
    if (c < 13) WAITVML(18); else if (c == 13) WAITVML(12); else if (c == 14) WAITVML(6); else WAITVML(0);
    PIN(of[c][0]); PIN(of[c][1]);
    bf16x8 af[2][2];
#pragma unroll
    for (int t2 = 0; t2 < 2; ++t2)
#pragma unroll
      for (int nb = 0; nb < 2; ++nb)
        af[t2][nb] = cat8(lds_tr(L + ta[nb] + (c % 5) * 4096 + t2 * 2048), lds_tr(L + ta[nb] + (c % 5) * 4096 + t2 * 2048 + 1024));
    if (c + 4 < 16) ISSUE_STEP(c + 4);
#pragma unroll
    for (int t2 = 0; t2 < 2; ++t2)
#pragma unroll
      for (int nb = 0; nb < 2; ++nb) acc[nb] = MFMA32(af[t2][nb], of[c][t2], acc[nb]);
  }
  BARRIER();
#pragma unroll
  for (int nb = 0; nb < 2; ++nb)
#pragma unroll
    for (int g = 0; g < 4; ++g) {
      f32x4 v = {acc[nb][4 * g], acc[nb][4 * g + 1], acc[nb][4 * g + 2], acc[nb][4 * g + 3]};
      *(__attribute__((address_space(3))) f32x4*)(L + w * 8192 + (nb * 4 + g) * 1024 + lane * 16) = v;
    }
  BARRIER();
  f32x4 s = {0.f, 0.f, 0.f, 0.f};
#pragma unroll
  for (int ww = 0; ww < 8; ++ww) s += *(__attribute__((address_space(3))) f32x4*)(L + ww * 8192 + w * 1024 + lane * 16);
  const int nb = w >> 2, g = w & 3;
  u32x2 pk = {cvtpk(s[0], s[1]), cvtpk(s[2], s[3])};
  *(u32x2*)((char*)out + ((size_t)(mblk * 32 + r32) * 512 + nblk * 64 + nb * 32 + 8 * g + 4 * hi) * 2) = pk;
}

extern "C" void kernel_launch(void* const* d_in, const int* in_sizes, int n_in, void* d_out, int out_size, void* d_ws, size_t ws_size,
                              hipStream_t stream) {
  const __hip_bfloat16* x = (const __hip_bfloat16*)d_in[0];
  const __hip_bfloat16* kc = (const __hip_bfloat16*)d_in[1];
  const __hip_bfloat16* vc = (const __hip_bfloat16*)d_in[2];
  const __hip_bfloat16* wq = (const __hip_bfloat16*)d_in[3];
  const __hip_bfloat16* wo = (const __hip_bfloat16*)d_in[4];
  __hip_bfloat16* out = (__hip_bfloat16*)d_out;
  __hip_bfloat16* ows = (__hip_bfloat16*)d_ws;
  (void)hipFuncSetAttribute((const void*)attn_kernel, hipFuncAttributeMaxDynamicSharedMemorySize, LDS1);
  (void)hipFuncSetAttribute((const void*)oproj_kernel, hipFuncAttributeMaxDynamicSharedMemorySize, LDS2);
  attn_kernel<<<256, 512, LDS1, stream>>>(x, kc, vc, wq, ows);
  oproj_kernel<<<256, 512, LDS2, stream>>>(ows, wo, out);
}
```

```cpp
#include <hip/hip_runtime.h>
#include <hip/hip_bf16.h>
#include <stdint.h>

typedef __attribute__((ext_vector_type(8))) short bf16x8;
typedef __attribute__((ext_vector_type(4))) short s16x4;
typedef __attribute__((ext_vector_type(16))) float f32x16;
typedef __attribute__((ext_vector_type(4))) float f32x4;
typedef __attribute__((ext_vector_type(4))) unsigned u32x4;
typedef __attribute__((ext_vector_type(2))) unsigned u32x2;
typedef __attribute__((address_space(3))) char lds_char;
typedef short v4i16_t __attribute__((ext_vector_type(4)));

constexpr int NB = 8, SQ = 128, DM = 512, NH = 32, SK = 2048, DH = 128;
constexpr int NT = SK / 64;
constexpr int STAGE = 32768;
constexpr int LDS1 = 163840;
constexpr int K0_OFF = 65536;
constexpr int P_OFF = 81920;
constexpr int XCH_OFF = 131072;
constexpr int LDS2 = 163840;
constexpr float CSC = 0.08838834764831845f * 1.4426950408889634f;

#define MFMA32(a, b, c) __builtin_amdgcn_mfma_f32_32x32x16_bf16(a, b, c, 0, 0, 0)

typedef __attribute__((ext_vector_type(2))) float f32x2;
typedef __attribute__((ext_vector_type(2))) __bf16 bf16x2_t;
__device__ __forceinline__ unsigned cvtpk(float lo, float hi) {
  f32x2 v = {lo, hi};
  return __builtin_bit_cast(unsigned, __builtin_convertvector(v, bf16x2_t));
}
__device__ __forceinline__ bf16x8 pack8(float a0, float a1, float a2, float a3, float a4, float a5, float a6, float a7) {
  u32x4 w = {cvtpk(a0, a1), cvtpk(a2, a3), cvtpk(a4, a5), cvtpk(a6, a7)};
  return __builtin_bit_cast(bf16x8, w);
}
__device__ __forceinline__ bf16x8 lds_r128(lds_char* p) { return *(__attribute__((address_space(3))) bf16x8*)p; }
__device__ __forceinline__ s16x4 lds_tr(lds_char* p) {
  return __builtin_bit_cast(s16x4, __builtin_amdgcn_ds_read_tr16_b64_v4i16((__attribute__((address_space(3))) v4i16_t*)p));
}
__device__ __forceinline__ bf16x8 cat8(s16x4 a, s16x4 b) { return __builtin_shufflevector(a, b, 0, 1, 2, 3, 4, 5, 6, 7); }

__device__ __forceinline__ void dma4(unsigned o0, unsigned o1, unsigned o2, unsigned o3, const void* sbase, unsigned ldsaddr) {
  unsigned keep;
  asm volatile(
      "s_mov_b32 %0, m0\n\t"
      "s_mov_b32 m0, %6\n\t"
      "s_nop 0\n\t"
      "global_load_lds_dwordx4 %1, %5 sc0 sc1 nt\n\t"
      "global_load_lds_dwordx4 %2, %5 offset:1024 sc0 sc1 nt\n\t"
      "global_load_lds_dwordx4 %3, %5 offset:2048 sc0 sc1 nt\n\t"
      "global_load_lds_dwordx4 %4, %5 offset:3072 sc0 sc1 nt\n\t"
      "s_mov_b32 m0, %0"
      : "=&s"(keep) : "v"(o0), "v"(o1), "v"(o2), "v"(o3), "s"(sbase), "s"(ldsaddr) : "memory");
}
__device__ __forceinline__ void dma2(unsigned o0, unsigned o1, const void* sbase, unsigned ldsaddr) {
  unsigned keep;
  asm volatile(
      "s_mov_b32 %0, m0\n\t"
      "s_mov_b32 m0, %4\n\t"
      "s_nop 0\n\t"
      "global_load_lds_dwordx4 %1, %3\n\t"
      "global_load_lds_dwordx4 %2, %3 offset:1024\n\t"
      "s_mov_b32 m0, %0"
      : "=&s"(keep) : "v"(o0), "v"(o1), "s"(sbase), "s"(ldsaddr) : "memory");
}
__device__ __forceinline__ void dma1(unsigned o0, const void* sbase, unsigned ldsaddr) {
  unsigned keep;
  asm volatile(
      "s_mov_b32 %0, m0\n\t"
      "s_mov_b32 m0, %3\n\t"
      "s_nop 0\n\t"
      "global_load_lds_dwordx4 %1, %2\n\t"
      "s_mov_b32 m0, %0"
      : "=&s"(keep) : "v"(o0), "s"(sbase), "s"(ldsaddr) : "memory");
}
#define WAITVM(n) asm volatile("s_waitcnt vmcnt(" #n ")" ::: "memory")
#define WAITVML(n) asm volatile("s_waitcnt vmcnt(" #n ") lgkmcnt(0)" ::: "memory")
#define BARRIER() asm volatile("s_waitcnt lgkmcnt(0)\n\ts_barrier" ::: "memory")
#define PIN(x) asm volatile("" : "+v"(x))

__device__ __forceinline__ float swapmax(float v) {
  auto rr = __builtin_amdgcn_permlane32_swap(__float_as_uint(v), __float_as_uint(v), false, false);
  return fmaxf(__uint_as_float(rr[0]), __uint_as_float(rr[1]));
}
__device__ __forceinline__ float swapadd(float v) {
  auto rr = __builtin_amdgcn_permlane32_swap(__float_as_uint(v), __float_as_uint(v), false, false);
  return __uint_as_float(rr[0]) + __uint_as_float(rr[1]);
}

__global__ __launch_bounds__(512, 2) void attn_kernel(const __hip_bfloat16* __restrict__ x, const __hip_bfloat16* __restrict__ kc,
                                                      const __hip_bfloat16* __restrict__ vc, const __hip_bfloat16* __restrict__ wq,
                                                      __hip_bfloat16* __restrict__ ows) {
  extern __shared__ __attribute__((aligned(16))) char smem[];
  lds_char* L = (lds_char*)smem;
  const unsigned L0 = (unsigned)(uintptr_t)smem;
  const int tid = threadIdx.x, lane = tid & 63;
  const int w = __builtin_amdgcn_readfirstlane(tid >> 6);
  const int qg = w & 3, kh = w >> 2;
  const int r32 = lane & 31, hi = lane >> 5;
  const int q4 = (lane & 15) >> 2, p4 = lane & 3, g1 = (lane >> 4) & 1;
  const int bid = blockIdx.x;
  const int head = 4 * (bid & 7) + ((bid >> 3) & 3), b = bid >> 5;
  const char* kbase = (const char*)kc + ((size_t)(b * NH + head) * SK) * 256;
  const char* vbase = (const char*)vc + ((size_t)(b * NH + head) * SK) * 256;
  const char* wqbase = (const char*)wq + head * 256;

  const int drow = lane >> 4, dpos = lane & 15;
  unsigned ko[4];
#pragma unroll
  for (int j = 0; j < 4; ++j) ko[j] = (unsigned)(((w & 3) * 16 + drow) * 256 + ((dpos ^ (4 * j + drow)) << 4));
  const unsigned vo = (unsigned)(((w & 3) * 16 + drow) * 256 + ((dpos ^ (drow << 2)) << 4));
  unsigned wo2[2], xo2[2];
#pragma unroll
  for (int j = 0; j < 2; ++j) {
    wo2[j] = (unsigned)(((w & 3) * 8 + 4 * j + drow) * 8192 + ((dpos ^ (drow << 2)) << 4) - j * 1024);
    xo2[j] = (unsigned)(((w & 3) * 32 + 16 * j + (lane >> 2)) * 1024 + (((lane & 3) ^ drow) << 4) - j * 1024);
  }
  const char* xbase = (const char*)x + (size_t)b * SQ * 1024;

#define ISSUE_K(t, la) dma4(ko[0], ko[1], ko[2], ko[3], kbase + (size_t)(t) * 16384, (la) + (w & 3) * 4096)
#define ISSUE_V(t, la) dma4(vo, vo, vo, vo, vbase + (size_t)(t) * 16384, (la) + (w & 3) * 4096)
#define ISSUE_P(c) do { dma2(wo2[0], wo2[1], wqbase + (size_t)(c) * 32 * 8192, L0 + P_OFF + ((c) % 5) * 16384 + (w & 3) * 2048); \
    dma2(xo2[0], xo2[1], xbase + (c) * 64, L0 + P_OFF + ((c) % 5) * 16384 + 8192 + (w & 3) * 2048); } while (0)

  if (w < 4) {
    ISSUE_K(0, L0 + K0_OFF);
#pragma unroll
    for (int s = 0; s < 2; ++s) { ISSUE_K(s + 1, L0 + s * STAGE); ISSUE_V(s, L0 + s * STAGE + 16384); }
  } else { ISSUE_P(0); ISSUE_P(1); ISSUE_P(2); ISSUE_P(3); }

  f32x16 qa[2] = {};
  {
    const int ps = ((p4 & 1) << 1) | (p4 >> 1);
    int wa[2], xa[2];
#pragma unroll
    for (int dd = 0; dd < 2; ++dd) {
      const int dblk = 2 * kh + dd;
      wa[dd] = (8 * hi + q4) * 256 + (((4 * dblk + 2 * g1 + (ps >> 1)) ^ (q4 << 2)) << 4) + 8 * (ps & 1);
      xa[dd] = 8192 + (qg * 32 + r32) * 64 + (((2 * dd + hi) ^ ((r32 >> 2) & 3)) << 4);
    }
#pragma unroll
    for (int c = 0; c < 16; ++c) {
      if (w >= 4) { if (c < 13) WAITVM(12); else if (c == 13) WAITVM(8); else if (c == 14) WAITVM(4); else WAITVM(0); }
      BARRIER();
      if (w >= 4 && c + 4 < 16) ISSUE_P(c + 4);
      lds_char* wb = L + P_OFF + (c % 5) * 16384;
#pragma unroll
      for (int s = 0; s < 2; ++s) {
        const bf16x8 xf = lds_r128(wb + xa[s]);
#pragma unroll
        for (int dd = 0; dd < 2; ++dd) {
          s16x4 a0 = lds_tr(wb + wa[dd] + s * 4096);
          s16x4 a1 = lds_tr(wb + wa[dd] + s * 4096 + 1024);
          qa[dd] = MFMA32(cat8(a0, a1), xf, qa[dd]);
        }
      }
    }
  }
  bf16x8 qf[8];
  {
    bf16x8 own[4];
#pragma unroll
    for (int dd = 0; dd < 2; ++dd)
#pragma unroll
      for (int sp = 0; sp < 2; ++sp)
        own[dd * 2 + sp] = pack8(qa[dd][8 * sp + 0], qa[dd][8 * sp + 1], qa[dd][8 * sp + 2], qa[dd][8 * sp + 3],
                                 qa[dd][8 * sp + 4], qa[dd][8 * sp + 5], qa[dd][8 * sp + 6], qa[dd][8 * sp + 7]);
    BARRIER();
#pragma unroll
    for (int i = 0; i < 4; ++i) *(__attribute__((address_space(3))) bf16x8*)(L + XCH_OFF + w * 4096 + i * 1024 + lane * 16) = own[i];
    if (w < 4) WAITVM(16);
    BARRIER();
#pragma unroll
    for (int i = 0; i < 4; ++i) {
      bf16x8 o = lds_r128(L + XCH_OFF + (w ^ 4) * 4096 + i * 1024 + lane * 16);
      if (kh == 0) { qf[i] = own[i]; qf[4 + i] = o; } else { qf[4 + i] = own[i]; qf[i] = o; }
    }
  }

  f32x16 oacc[4] = {};
  const int krow = kh * 32 + r32;
  const int kaddr0 = krow * 256 + ((hi ^ (krow & 15)) << 4);
  int vaddr0[4];
#pragma unroll
  for (int d = 0; d < 4; ++d) vaddr0[d] = 16384 + (kh * 32 + 4 * hi + q4) * 256 + ((d ^ q4) << 6) + g1 * 32 + p4 * 8;

  f32x16 sc = {};
  {
    bf16x8 kf[8];
#pragma unroll
    for (int s = 0; s < 8; ++s) kf[s] = lds_r128(L + K0_OFF + (kaddr0 ^ (s << 5)));
#pragma unroll
    for (int s = 0; s < 8; ++s) sc = MFMA32(kf[s], qf[s], sc);
  }
  float m, l = 0.f, alpha = 0.f;
  {
    float tmax = sc[0];
#pragma unroll
    for (int r = 1; r < 16; ++r) tmax = fmaxf(tmax, sc[r]);
    m = swapmax(tmax) * CSC;
  }
  int slot = 0;
  bf16x8 kf[8], vf[4][2], pf[2];
  f32x16 sn;
#define READ_K(sb) _Pragma("unroll") for (int s = 0; s < 8; ++s) kf[s] = lds_r128((sb) + (kaddr0 ^ (s << 5)))
#define READ_V(sb) _Pragma("unroll") for (int d = 0; d < 4; ++d) _Pragma("unroll") for (int s = 0; s < 2; ++s) \
        vf[d][s] = cat8(lds_tr((sb) + vaddr0[d] + s * 4096), lds_tr((sb) + vaddr0[d] + s * 4096 + 2048))
#define QK_NEXT() do { sn = (f32x16){}; _Pragma("unroll") for (int s = 0; s < 8; ++s) sn = MFMA32(kf[s], qf[s], sn); } while (0)
#define SOFTMAX() do { \
    const f32x2 al2 = {alpha, alpha}, cs2 = {CSC, CSC}, nm2 = {-m, -m}; \
    _Pragma("unroll") for (int d = 0; d < 4; ++d) _Pragma("unroll") for (int r = 0; r < 16; r += 2) { \
      f32x2 t = {oacc[d][r], oacc[d][r + 1]}; t *= al2; oacc[d][r] = t[0]; oacc[d][r + 1] = t[1]; } \
    f32x2 ps2 = {0.f, 0.f}; \
    _Pragma("unroll") for (int r = 0; r < 16; r += 2) { \
      f32x2 t = {sc[r], sc[r + 1]}; t = __builtin_elementwise_fma(t, cs2, nm2); \
      t[0] = __builtin_amdgcn_exp2f(t[0]); t[1] = __builtin_amdgcn_exp2f(t[1]); ps2 += t; sc[r] = t[0]; sc[r + 1] = t[1]; } \
    l = l * alpha + (ps2[0] + ps2[1]); \
    _Pragma("unroll") for (int s = 0; s < 2; ++s) \
      pf[s] = pack8(sc[8 * s + 0], sc[8 * s + 1], sc[8 * s + 2], sc[8 * s + 3], sc[8 * s + 4], sc[8 * s + 5], sc[8 * s + 6], sc[8 * s + 7]); \
  } while (0)
#define PV() _Pragma("unroll") for (int d = 0; d < 4; ++d) _Pragma("unroll") for (int s = 0; s < 2; ++s) oacc[d] = MFMA32(vf[d][s], pf[s], oacc[d])
#define MAX_NEXT() do { \
    float tmax = sn[0]; \
    _Pragma("unroll") for (int r = 1; r < 16; ++r) tmax = fmaxf(tmax, sn[r]); \
    tmax = swapmax(tmax); \
    const float mn = fmaxf(m, tmax * CSC); \
    alpha = __builtin_amdgcn_exp2f(m - mn); \
    m = mn; sc = sn; } while (0)

  for (int it = 0; it < NT - 1; ++it) {
    if (it == 0) WAITVM(8);
    else if (it < NT - 4) WAITVM(12);
    else if (w < 4) { if (it == NT - 4) WAITVM(8); else if (it == NT - 3) WAITVM(4); else WAITVM(0); }
    else { if (it == NT - 4) WAITVM(12); else if (it == NT - 3) WAITVM(8); else WAITVM(4); }
    BARRIER();
    lds_char* sb = L + slot * STAGE;
    READ_K(sb);
    if (it == 0) {
      if (w < 4) { ISSUE_K(3, L0 + 2 * STAGE); ISSUE_K(4, L0 + 3 * STAGE); ISSUE_K(5, L0 + 4 * STAGE); }
      else { ISSUE_V(2, L0 + 2 * STAGE + 16384); ISSUE_V(3, L0 + 3 * STAGE + 16384); ISSUE_V(4, L0 + 4 * STAGE + 16384); }
    } else if (it + 4 < NT) {
      const unsigned la = L0 + (slot == 0 ? 4 : slot - 1) * STAGE;
      if (w < 4) { if (it + 5 < NT) ISSUE_K(it + 5, la); }
      else ISSUE_V(it + 4, la + 16384);
    }
    slot = (slot == 4) ? 0 : slot + 1;
    READ_V(sb);
    QK_NEXT();
    SOFTMAX();
    PV();
    MAX_NEXT();
  }
  {
    WAITVM(0);
    BARRIER();
    lds_char* sb = L + slot * STAGE;
    READ_V(sb);
    SOFTMAX();
    PV();
  }

  l = swapadd(l);
  {
    float* ml = (float*)(smem + 4 * STAGE);
    lds_char* xo = L + 2 * STAGE;
    if (hi == 0) { ml[w * 64 + r32] = m; ml[w * 64 + 32 + r32] = l; }
#pragma unroll
    for (int dd = 0; dd < 2; ++dd)
#pragma unroll
      for (int g = 0; g < 4; ++g) {
        f32x4 v;
        if (kh) { v[0] = oacc[dd][4 * g]; v[1] = oacc[dd][4 * g + 1]; v[2] = oacc[dd][4 * g + 2]; v[3] = oacc[dd][4 * g + 3]; }
        else { v[0] = oacc[2 + dd][4 * g]; v[1] = oacc[2 + dd][4 * g + 1]; v[2] = oacc[2 + dd][4 * g + 2]; v[3] = oacc[2 + dd][4 * g + 3]; }
        *(__attribute__((address_space(3))) f32x4*)(xo + w * 8192 + (dd * 4 + g) * 1024 + lane * 16) = v;
      }
    BARRIER();
    const float mp = ml[(w ^ 4) * 64 + r32], lp = ml[(w ^ 4) * 64 + 32 + r32];
    const float mf = fmaxf(m, mp);
    const float wa = __builtin_amdgcn_exp2f(m - mf), wb = __builtin_amdgcn_exp2f(mp - mf);
    const float inv = 1.f / (wa * l + wb * lp);
    const float fa = wa * inv, fb = wb * inv;
    const int mblk = b * 4 + qg;
#pragma unroll
    for (int dd = 0; dd < 2; ++dd) {
      float o[16];
#pragma unroll
      for (int g = 0; g < 4; ++g) {
        f32x4 v = *(__attribute__((address_space(3))) f32x4*)(xo + (w ^ 4) * 8192 + (dd * 4 + g) * 1024 + lane * 16);
#pragma unroll
        for (int e = 0; e < 4; ++e) {
          const float own = kh ? oacc[2 + dd][4 * g + e] : oacc[dd][4 * g + e];
          o[4 * g + e] = own * fa + v[e] * fb;
        }
      }
      const int dblk = 2 * kh + dd;
#pragma unroll
      for (int sp = 0; sp < 2; ++sp) {
        bf16x8 f = pack8(o[8 * sp + 0], o[8 * sp + 1], o[8 * sp + 2], o[8 * sp + 3], o[8 * sp + 4], o[8 * sp + 5], o[8 * sp + 6], o[8 * sp + 7]);
        const size_t ks = (size_t)mblk * 256 + head * 8 + dblk * 2 + sp;
        *(bf16x8*)((char*)ows + (ks * 64 + lane) * 16) = f;
      }
    }
  }
}

__global__ __launch_bounds__(512, 2) void oproj_kernel(const __hip_bfloat16* __restrict__ ows, const __hip_bfloat16* __restrict__ wo,
                                                       __hip_bfloat16* __restrict__ out) {
  extern __shared__ __attribute__((aligned(16))) char smem[];
  lds_char* L = (lds_char*)smem;
  const unsigned L0 = (unsigned)(uintptr_t)smem;
  const int tid = threadIdx.x, lane = tid & 63;
  const int w = __builtin_amdgcn_readfirstlane(tid >> 6);
  const int r32 = lane & 31, hi = lane >> 5;
  const int q4 = (lane & 15) >> 2, p4 = lane & 3, g1 = (lane >> 4) & 1;
  const int bid = blockIdx.x;
  const int mblk = (bid & 7) * 4 + ((bid >> 3) & 3), nblk = bid >> 5;

  bf16x8 of[16][2];
  const char* ob = (const char*)ows + ((size_t)mblk * 256 + w * 32) * 1024;
  const unsigned oo = lane * 16;
  const char* wb = (const char*)wo + (size_t)(w * 512) * 1024 + nblk * 128;
  const int drow = lane >> 3, dpos = lane & 7;
  const unsigned wof = (unsigned)(drow * 1024 + ((dpos ^ (((drow >> 1) & 1) << 2)) << 4));
  const unsigned lw = L0 + w * 20480;
#define ISSUE_STEP(c) do { \
    asm volatile("global_load_dwordx4 %0, %1, %2 offset:0" : "=v"(of[c][0]) : "v"(oo), "s"(ob + (c) * 2048) : "memory"); \
    asm volatile("global_load_dwordx4 %0, %1, %2 offset:1024" : "=v"(of[c][1]) : "v"(oo), "s"(ob + (c) * 2048) : "memory"); \
    _Pragma("unroll") for (int j_ = 0; j_ < 4; ++j_) \
      dma1(wof, wb + (size_t)((c) * 32 + j_ * 8) * 1024, lw + ((c) % 5) * 4096 + j_ * 1024); } while (0)
  ISSUE_STEP(0); ISSUE_STEP(1); ISSUE_STEP(2); ISSUE_STEP(3);
  f32x16 acc[2] = {};
  int ta[2];
#pragma unroll
  for (int nb = 0; nb < 2; ++nb) ta[nb] = w * 20480 + (4 * hi + q4) * 128 + ((nb ^ (q4 >> 1)) << 6) + g1 * 32 + p4 * 8;
#pragma unroll
  for (int c = 0; c < 16; ++c) {
    if (c < 13) WAITVML(18); else if (c == 13) WAITVML(12); else if (c == 14) WAITVML(6); else WAITVML(0);
    PIN(of[c][0]); PIN(of[c][1]);
    bf16x8 af[2][2];
#pragma unroll
    for (int t2 = 0; t2 < 2; ++t2)
#pragma unroll
      for (int nb = 0; nb < 2; ++nb)
        af[t2][nb] = cat8(lds_tr(L + ta[nb] + (c % 5) * 4096 + t2 * 2048), lds_tr(L + ta[nb] + (c % 5) * 4096 + t2 * 2048 + 1024));
    if (c + 4 < 16) ISSUE_STEP(c + 4);
#pragma unroll
    for (int t2 = 0; t2 < 2; ++t2)
#pragma unroll
      for (int nb = 0; nb < 2; ++nb) acc[nb] = MFMA32(af[t2][nb], of[c][t2], acc[nb]);
  }
  BARRIER();
#pragma unroll
  for (int nb = 0; nb < 2; ++nb)
#pragma unroll
    for (int g = 0; g < 4; ++g) {
      f32x4 v = {acc[nb][4 * g], acc[nb][4 * g + 1], acc[nb][4 * g + 2], acc[nb][4 * g + 3]};
      *(__attribute__((address_space(3))) f32x4*)(L + w * 8192 + (nb * 4 + g) * 1024 + lane * 16) = v;
    }
  BARRIER();
  f32x4 s = {0.f, 0.f, 0.f, 0.f};
#pragma unroll
  for (int ww = 0; ww < 8; ++ww) s += *(__attribute__((address_space(3))) f32x4*)(L + ww * 8192 + w * 1024 + lane * 16);
  const int nb = w >> 2, g = w & 3;
  u32x2 pk = {cvtpk(s[0], s[1]), cvtpk(s[2], s[3])};
  *(u32x2*)((char*)out + ((size_t)(mblk * 32 + r32) * 512 + nblk * 64 + nb * 32 + 8 * g + 4 * hi) * 2) = pk;
}

extern "C" void kernel_launch(void* const* d_in, const int* in_sizes, int n_in, void* d_out, int out_size, void* d_ws, size_t ws_size,
                              hipStream_t stream) {
  const __hip_bfloat16* x = (const __hip_bfloat16*)d_in[0];
  const __hip_bfloat16* kc = (const __hip_bfloat16*)d_in[1];
  const __hip_bfloat16* vc = (const __hip_bfloat16*)d_in[2];
  const __hip_bfloat16* wq = (const __hip_bfloat16*)d_in[3];
  const __hip_bfloat16* wo = (const __hip_bfloat16*)d_in[4];
  __hip_bfloat16* out = (__hip_bfloat16*)d_out;
  __hip_bfloat16* ows = (__hip_bfloat16*)d_ws;
  (void)hipFuncSetAttribute((const void*)attn_kernel, hipFuncAttributeMaxDynamicSharedMemorySize, LDS1);
  (void)hipFuncSetAttribute((const void*)oproj_kernel, hipFuncAttributeMaxDynamicSharedMemorySize, LDS2);
  attn_kernel<<<256, 512, LDS1, stream>>>(x, kc, vc, wq, ows);
  oproj_kernel<<<256, 512, LDS2, stream>>>(ows, wo, out);
}
```

```cpp
#include <hip/hip_runtime.h>
#include <hip/hip_bf16.h>
#include <stdint.h>

typedef __attribute__((ext_vector_type(8))) short bf16x8;
typedef __attribute__((ext_vector_type(4))) short s16x4;
typedef __attribute__((ext_vector_type(16))) float f32x16;
typedef __attribute__((ext_vector_type(4))) float f32x4;
typedef __attribute__((ext_vector_type(4))) unsigned u32x4;
typedef __attribute__((ext_vector_type(2))) unsigned u32x2;
typedef __attribute__((address_space(3))) char lds_char;
typedef short v4i16_t __attribute__((ext_vector_type(4)));

constexpr int NB = 8, SQ = 128, DM = 512, NH = 32, SK = 2048, DH = 128;
constexpr int NT = SK / 64;
constexpr int LDS1 = 163840;
constexpr int P_OFF = 81920;
constexpr int XCH_OFF = 131072;
constexpr int LDS2 = 163840;
constexpr float CSC = 0.08838834764831845f * 1.4426950408889634f;

#define MFMA32(a, b, c) __builtin_amdgcn_mfma_f32_32x32x16_bf16(a, b, c, 0, 0, 0)

typedef __attribute__((ext_vector_type(2))) float f32x2;
typedef __attribute__((ext_vector_type(2))) __bf16 bf16x2_t;
__device__ __forceinline__ unsigned cvtpk(float lo, float hi) {
  f32x2 v = {lo, hi};
  return __builtin_bit_cast(unsigned, __builtin_convertvector(v, bf16x2_t));
}
__device__ __forceinline__ bf16x8 pack8(float a0, float a1, float a2, float a3, float a4, float a5, float a6, float a7) {
  u32x4 w = {cvtpk(a0, a1), cvtpk(a2, a3), cvtpk(a4, a5), cvtpk(a6, a7)};
  return __builtin_bit_cast(bf16x8, w);
}
__device__ __forceinline__ bf16x8 lds_r128(lds_char* p) { return *(__attribute__((address_space(3))) bf16x8*)p; }
__device__ __forceinline__ s16x4 lds_tr(lds_char* p) {
  return __builtin_bit_cast(s16x4, __builtin_amdgcn_ds_read_tr16_b64_v4i16((__attribute__((address_space(3))) v4i16_t*)p));
}
__device__ __forceinline__ bf16x8 cat8(s16x4 a, s16x4 b) { return __builtin_shufflevector(a, b, 0, 1, 2, 3, 4, 5, 6, 7); }

__device__ __forceinline__ void dma4(unsigned o0, unsigned o1, unsigned o2, unsigned o3, const void* sbase, unsigned ldsaddr) {
  unsigned keep;
  asm volatile(
      "s_mov_b32 %0, m0\n\t"
      "s_mov_b32 m0, %6\n\t"
      "s_nop 0\n\t"
      "global_load_lds_dwordx4 %1, %5\n\t"
      "global_load_lds_dwordx4 %2, %5 offset:1024\n\t"
      "global_load_lds_dwordx4 %3, %5 offset:2048\n\t"
      "global_load_lds_dwordx4 %4, %5 offset:3072\n\t"
      "s_mov_b32 m0, %0"
      : "=&s"(keep) : "v"(o0), "v"(o1), "v"(o2), "v"(o3), "s"(sbase), "s"(ldsaddr) : "memory");
}
__device__ __forceinline__ void dma2(unsigned o0, unsigned o1, const void* sbase, unsigned ldsaddr) {
  unsigned keep;
  asm volatile(
      "s_mov_b32 %0, m0\n\t"
      "s_mov_b32 m0, %4\n\t"
      "s_nop 0\n\t"
      "global_load_lds_dwordx4 %1, %3\n\t"
      "global_load_lds_dwordx4 %2, %3 offset:1024\n\t"
      "s_mov_b32 m0, %0"
      : "=&s"(keep) : "v"(o0), "v"(o1), "s"(sbase), "s"(ldsaddr) : "memory");
}
__device__ __forceinline__ void dma1(unsigned o0, const void* sbase, unsigned ldsaddr) {
  unsigned keep;
  asm volatile(
      "s_mov_b32 %0, m0\n\t"
      "s_mov_b32 m0, %3\n\t"
      "s_nop 0\n\t"
      "global_load_lds_dwordx4 %1, %2\n\t"
      "s_mov_b32 m0, %0"
      : "=&s"(keep) : "v"(o0), "s"(sbase), "s"(ldsaddr) : "memory");
}
template <int J> __device__ __forceinline__ void dmap(unsigned o, const void* sbase, unsigned ldsaddr) {
  unsigned keep;
  asm volatile("s_mov_b32 %0, m0\n\ts_mov_b32 m0, %3\n\ts_nop 0\n\tglobal_load_lds_dwordx4 %1, %2 offset:%4 nt\n\ts_mov_b32 m0, %0"
               : "=&s"(keep) : "v"(o), "s"(sbase), "s"(ldsaddr), "i"(J * 1024) : "memory");
}
__device__ __forceinline__ float fadd_s(float a, float b) { float r; asm("v_add_f32_e32 %0, %1, %2" : "=v"(r) : "v"(a), "v"(b)); return r; }
#define GAP() __builtin_amdgcn_sched_barrier(0)
__device__ __forceinline__ int kslot(int i) { return i * 16384 + (i >= 3 ? 32768 : 0); }
__device__ __forceinline__ int vslot(int i) { return 49152 + i * 16384 + (i >= 2 ? 32768 : 0); }
#define WAITVM(n) asm volatile("s_waitcnt vmcnt(" #n ")" ::: "memory")
#define WAITVML(n) asm volatile("s_waitcnt vmcnt(" #n ") lgkmcnt(0)" ::: "memory")
#define BARRIER() asm volatile("s_waitcnt lgkmcnt(0)\n\ts_barrier" ::: "memory")
#define PIN(x) asm volatile("" : "+v"(x))

__device__ __forceinline__ float swapmax(float v) {
  auto rr = __builtin_amdgcn_permlane32_swap(__float_as_uint(v), __float_as_uint(v), false, false);
  return fmaxf(__uint_as_float(rr[0]), __uint_as_float(rr[1]));
}
__device__ __forceinline__ float swapadd(float v) {
  auto rr = __builtin_amdgcn_permlane32_swap(__float_as_uint(v), __float_as_uint(v), false, false);
  return __uint_as_float(rr[0]) + __uint_as_float(rr[1]);
}

__global__ __launch_bounds__(512, 2) void attn_kernel(const __hip_bfloat16* __restrict__ x, const __hip_bfloat16* __restrict__ kc,
                                                      const __hip_bfloat16* __restrict__ vc, const __hip_bfloat16* __restrict__ wq,
                                                      __hip_bfloat16* __restrict__ ows) {
  extern __shared__ __attribute__((aligned(16))) char smem[];
  lds_char* L = (lds_char*)smem;
  const unsigned L0 = (unsigned)(uintptr_t)smem;
  const int tid = threadIdx.x, lane = tid & 63;
  const int w = __builtin_amdgcn_readfirstlane(tid >> 6);
  const int qg = w & 3, kh = w >> 2;
  const int r32 = lane & 31, hi = lane >> 5;
  const int q4 = (lane & 15) >> 2, p4 = lane & 3, g1 = (lane >> 4) & 1;
  const int bid = blockIdx.x;
  const int head = 4 * (bid & 7) + ((bid >> 3) & 3), b = bid >> 5;
  const char* kbase = (const char*)kc + ((size_t)(b * NH + head) * SK) * 256;
  const char* vbase = (const char*)vc + ((size_t)(b * NH + head) * SK) * 256;
  const char* wqbase = (const char*)wq + head * 256;

  const int drow = lane >> 4, dpos = lane & 15;
  unsigned ko[4];
#pragma unroll
  for (int j = 0; j < 4; ++j) ko[j] = (unsigned)(((w & 3) * 16 + drow) * 256 + ((dpos ^ (4 * j + drow)) << 4));
  const unsigned vo = (unsigned)(((w & 3) * 16 + drow) * 256 + ((dpos ^ (drow << 2)) << 4));
  unsigned wo2[2], xo2[2];
#pragma unroll
  for (int j = 0; j < 2; ++j) {
    wo2[j] = (unsigned)(((w & 3) * 8 + 4 * j + drow) * 8192 + ((dpos ^ (drow << 2)) << 4) - j * 1024);
    xo2[j] = (unsigned)(((w & 3) * 32 + 16 * j + (lane >> 2)) * 1024 + (((lane & 3) ^ drow) << 4) - j * 1024);
  }
  const char* xbase = (const char*)x + (size_t)b * SQ * 1024;

#define ISSUE_K(t, la) dma4(ko[0], ko[1], ko[2], ko[3], kbase + (size_t)(t) * 16384, (la) + (w & 3) * 4096)
#define ISSUE_V(t, la) dma4(vo, vo, vo, vo, vbase + (size_t)(t) * 16384, (la) + (w & 3) * 4096)
#define ISSUE_P(c) do { dma2(wo2[0], wo2[1], wqbase + (size_t)(c) * 32 * 8192, L0 + P_OFF + ((c) % 5) * 16384 + (w & 3) * 2048); \
    dma2(xo2[0], xo2[1], xbase + (c) * 64, L0 + P_OFF + ((c) % 5) * 16384 + 8192 + (w & 3) * 2048); } while (0)

  if (w < 4) {
    ISSUE_K(0, L0 + kslot(0)); ISSUE_K(1, L0 + kslot(1)); ISSUE_V(0, L0 + vslot(0)); ISSUE_K(2, L0 + kslot(2)); ISSUE_V(1, L0 + vslot(1));
  } else { ISSUE_P(0); ISSUE_P(1); ISSUE_P(2); ISSUE_P(3); }

  f32x16 qa[2] = {};
  {
    const int ps = ((p4 & 1) << 1) | (p4 >> 1);
    int wa[2], xa[2];
#pragma unroll
    for (int dd = 0; dd < 2; ++dd) {
      const int dblk = 2 * kh + dd;
      wa[dd] = (8 * hi + q4) * 256 + (((4 * dblk + 2 * g1 + (ps >> 1)) ^ (q4 << 2)) << 4) + 8 * (ps & 1);
      xa[dd] = 8192 + (qg * 32 + r32) * 64 + (((2 * dd + hi) ^ ((r32 >> 2) & 3)) << 4);
    }
#pragma unroll
    for (int c = 0; c < 16; ++c) {
      if (w >= 4) { if (c < 13) WAITVM(12); else if (c == 13) WAITVM(8); else if (c == 14) WAITVM(4); else WAITVM(0); }
      BARRIER();
      if (w >= 4 && c + 4 < 16) ISSUE_P(c + 4);
      lds_char* wb = L + P_OFF + (c % 5) * 16384;
#pragma unroll
      for (int s = 0; s < 2; ++s) {
        const bf16x8 xf = lds_r128(wb + xa[s]);
#pragma unroll
        for (int dd = 0; dd < 2; ++dd) {
          s16x4 a0 = lds_tr(wb + wa[dd] + s * 4096);
          s16x4 a1 = lds_tr(wb + wa[dd] + s * 4096 + 1024);
          qa[dd] = MFMA32(cat8(a0, a1), xf, qa[dd]);
        }
      }
    }
  }
  bf16x8 qf[8];
  {
    bf16x8 own[4];
#pragma unroll
    for (int dd = 0; dd < 2; ++dd)
#pragma unroll
      for (int sp = 0; sp < 2; ++sp)
      {
        u32x4 t = __builtin_bit_cast(u32x4, pack8(qa[dd][8 * sp + 0], qa[dd][8 * sp + 1], qa[dd][8 * sp + 2], qa[dd][8 * sp + 3],
                                                  qa[dd][8 * sp + 4], qa[dd][8 * sp + 5], qa[dd][8 * sp + 6], qa[dd][8 * sp + 7]));
#pragma unroll
        for (int e = 0; e < 4; ++e) t[e] = cvtpk(__uint_as_float(t[e] << 16) * CSC, __uint_as_float(t[e] & 0xffff0000u) * CSC);
        own[dd * 2 + sp] = __builtin_bit_cast(bf16x8, t);
      }
    BARRIER();
#pragma unroll
    for (int i = 0; i < 4; ++i) *(__attribute__((address_space(3))) bf16x8*)(L + XCH_OFF + w * 4096 + i * 1024 + lane * 16) = own[i];
    if (w < 4) WAITVM(12);
    BARRIER();
#pragma unroll
    for (int i = 0; i < 4; ++i) {
      bf16x8 o = lds_r128(L + XCH_OFF + (w ^ 4) * 4096 + i * 1024 + lane * 16);
      if (kh == 0) { qf[i] = own[i]; qf[4 + i] = o; } else { qf[4 + i] = own[i]; qf[i] = o; }
    }
  }

  f32x16 oacc[4] = {};
  const int krow = kh * 32 + r32;
  const int kaddr0 = krow * 256 + ((hi ^ (krow & 15)) << 4);
  int vaddr0[4];
#pragma unroll
  for (int d = 0; d < 4; ++d) vaddr0[d] = (kh * 32 + 4 * hi + q4) * 256 + ((d ^ q4) << 6) + g1 * 32 + p4 * 8;

  bf16x8 kf[8], vf[4][2], pf[2];
  f32x16 sc = {}, sn, negm;
#pragma unroll
  for (int s = 0; s < 8; ++s) kf[s] = lds_r128(L + kslot(0) + (kaddr0 ^ (s << 5)));
#pragma unroll
  for (int s = 0; s < 8; ++s) sc = MFMA32(kf[s], qf[s], sc);
#pragma unroll
  for (int s = 0; s < 8; ++s) kf[s] = lds_r128(L + kslot(1) + (kaddr0 ^ (s << 5)));
  float m;
  {
    float tmax = sc[0];
#pragma unroll
    for (int r = 1; r < 16; ++r) tmax = fmaxf(tmax, sc[r]);
    m = swapmax(tmax);
#pragma unroll
    for (int r = 0; r < 16; ++r) { sc[r] -= m; negm[r] = -m; }
  }
  float l0 = 0.f, l1 = 0.f;
  const char* dbase = w < 4 ? kbase : vbase;
  const unsigned dof0 = w < 4 ? ko[0] : vo, dof1 = w < 4 ? ko[1] : vo, dof2 = w < 4 ? ko[2] : vo, dof3 = w < 4 ? ko[3] : vo;
  const int dahead = w < 4 ? 6 : 4;
  int ks = kslot(2), vs = vslot(0), ksp = kslot(1), vsp = vslot(4);

#define RDV(d, s) vf[d][s] = cat8(lds_tr(vb + vaddr0[d] + (s) * 4096), lds_tr(vb + vaddr0[d] + (s) * 4096 + 2048))
#define RDK(s) kf[s] = lds_r128(kb + (kaddr0 ^ ((s) << 5)))
#define EXP(SC, r) SC[r] = __builtin_amdgcn_exp2f(SC[r])
#define CVT(SC, s, e) pw[s][e] = cvtpk(SC[8 * (s) + 2 * (e)], SC[8 * (s) + 2 * (e) + 1])
#define ADD(SC, r) do { l0 = fadd_s(l0, SC[r]); l1 = fadd_s(l1, SC[(r) + 1]); } while (0)
#define PVM(d, s) oacc[d] = MFMA32(vf[d][s], __builtin_bit_cast(bf16x8, pw[s]), oacc[d])
#define QKM(SN, s) SN = MFMA32(kf[s], qf[s], SN)
#define DMA(J, o) if (doit) dmap<J>(o, dptr, dla)
#define RESCALE(SN, tmax) do { \
    if (__builtin_amdgcn_ballot_w64(!(tmax <= 8.f)) != 0) { \
      const float mn = m + fmaxf(tmax, 0.f); \
      const float dl = mn - m; \
      const float alpha = __builtin_amdgcn_exp2f(-dl); \
      _Pragma("unroll") for (int d = 0; d < 4; ++d) oacc[d] *= alpha; \
      _Pragma("unroll") for (int r = 0; r < 16; ++r) { SN[r] -= dl; negm[r] = -mn; } \
      l0 *= alpha; l1 *= alpha; m = mn; \
    } } while (0)
#define BODY(SC, SN, HASK) do { \
    lds_char* vb = L + vs; \
    lds_char* kb = L + ks; \
    u32x4 pw[2]; \
    SN = MFMA32(kf[0], qf[0], negm); RDV(0, 0); RDV(1, 0); EXP(SC, 0); EXP(SC, 1); GAP(); \
    QKM(SN, 1); RDV(2, 0); RDV(3, 0); EXP(SC, 2); EXP(SC, 3); CVT(SC, 0, 0); GAP(); \
    QKM(SN, 2); RDV(0, 1); RDV(1, 1); EXP(SC, 4); EXP(SC, 5); CVT(SC, 0, 1); GAP(); \
    QKM(SN, 3); RDV(2, 1); RDV(3, 1); EXP(SC, 6); EXP(SC, 7); CVT(SC, 0, 2); GAP(); \
    QKM(SN, 4); DMA(0, dof0); EXP(SC, 8); EXP(SC, 9); CVT(SC, 0, 3); GAP(); \
    QKM(SN, 5); DMA(1, dof1); EXP(SC, 10); EXP(SC, 11); CVT(SC, 1, 0); GAP(); \
    QKM(SN, 6); DMA(2, dof2); EXP(SC, 12); EXP(SC, 13); CVT(SC, 1, 1); GAP(); \
    QKM(SN, 7); DMA(3, dof3); EXP(SC, 14); EXP(SC, 15); CVT(SC, 1, 2); GAP(); \
    PVM(0, 0); if (HASK) { RDK(0); RDK(1); } CVT(SC, 1, 3); ADD(SC, 0); ADD(SC, 2); GAP(); \
    PVM(1, 0); if (HASK) { RDK(2); RDK(3); } ADD(SC, 4); ADD(SC, 6); GAP(); \
    PVM(2, 0); if (HASK) { RDK(4); RDK(5); } ADD(SC, 8); ADD(SC, 10); GAP(); \
    PVM(3, 0); if (HASK) { RDK(6); RDK(7); } ADD(SC, 12); ADD(SC, 14); GAP(); \
    float t0 = fmaxf(fmaxf(SN[0], SN[1]), SN[2]), t1 = fmaxf(fmaxf(SN[3], SN[4]), SN[5]); \
    PVM(0, 1); t0 = fmaxf(fmaxf(t0, SN[6]), SN[7]); t1 = fmaxf(fmaxf(t1, SN[8]), SN[9]); GAP(); \
    PVM(1, 1); t0 = fmaxf(fmaxf(t0, SN[10]), SN[11]); t1 = fmaxf(fmaxf(t1, SN[12]), SN[13]); GAP(); \
    PVM(2, 1); t0 = fmaxf(fmaxf(t0, SN[14]), SN[15]); t0 = swapmax(fmaxf(t0, t1)); GAP(); \
    PVM(3, 1); GAP(); \
    RESCALE(SN, t0); \
    ksp = ks; vsp = vs; \
    ks = ks == 32768 ? 81920 : (ks == 98304 ? 0 : ks + 16384); \
    vs = vs == 65536 ? 114688 : (vs == 147456 ? 49152 : vs + 16384); } while (0)

  {
    if (w < 4) WAITVM(0);
    BARRIER();
    if (w < 4) { ISSUE_K(3, L0 + kslot(3)); ISSUE_K(4, L0 + kslot(4)); ISSUE_K(5, L0 + kslot(0)); ISSUE_K(6, L0 + kslot(1)); }
    else { ISSUE_V(2, L0 + vslot(2)); ISSUE_V(3, L0 + vslot(3)); ISSUE_V(4, L0 + vslot(4)); }
    const bool doit = false; const char* dptr = dbase; const unsigned dla = 0;
    BODY(sc, sn, true);
  }
#define DSETUP(it) \
    const char* dptr = dbase + (size_t)((it) + dahead) * 16384; \
    const unsigned dla = L0 + (w < 4 ? ksp : vsp) + (w & 3) * 4096;
#pragma unroll 1
  for (int it = 1; it < 25; it += 2) {
    { WAITVM(12); BARRIER(); const bool doit = true; DSETUP(it); BODY(sn, sc, true); }
    { WAITVM(12); BARRIER(); const bool doit = true; DSETUP(it + 1); BODY(sc, sn, true); }
  }
#define TAILWAIT(it) do { \
    const int nout = w < 4 ? 29 - (it) : 31 - (it); \
    if (nout >= 3) WAITVM(12); else if (nout == 2) WAITVM(8); else if (nout == 1) WAITVM(4); else WAITVM(0); } while (0)
#pragma unroll 1
  for (int it = 25; it < 29; it += 2) {
    { TAILWAIT(it); BARRIER(); const bool doit = it + dahead < NT; DSETUP(it); BODY(sn, sc, true); }
    { TAILWAIT(it + 1); BARRIER(); const bool doit = it + 1 + dahead < NT; DSETUP(it + 1); BODY(sc, sn, true); }
  }
  {
    const bool doit = false; const char* dptr = dbase; const unsigned dla = 0;
    TAILWAIT(29); BARRIER(); BODY(sn, sc, true);
    TAILWAIT(30); BARRIER(); BODY(sc, sn, false);
  }
  {
    WAITVM(0);
    BARRIER();
    lds_char* vb = L + vs;
    u32x4 pw[2];
#pragma unroll
    for (int s = 0; s < 2; ++s)
#pragma unroll
      for (int d = 0; d < 4; ++d) RDV(d, s);
#pragma unroll
    for (int r = 0; r < 16; ++r) EXP(sn, r);
#pragma unroll
    for (int r = 0; r < 16; r += 2) ADD(sn, r);
#pragma unroll
    for (int s = 0; s < 2; ++s)
#pragma unroll
      for (int e = 0; e < 4; ++e) CVT(sn, s, e);
#pragma unroll
    for (int s = 0; s < 2; ++s)
#pragma unroll
      for (int d = 0; d < 4; ++d) PVM(d, s);
  }

  const float l = swapadd(l0 + l1);
  {
    int lane;
    asm volatile("v_mbcnt_lo_u32_b32 %0, -1, 0\n\tv_mbcnt_hi_u32_b32 %0, -1, %0" : "=v"(lane));
    const int r32 = lane & 31, hi = lane >> 5;
    float* ml = (float*)(smem + 147456);
    lds_char* xo = L + 81920;
    if (hi == 0) { ml[w * 64 + r32] = m; ml[w * 64 + 32 + r32] = l; }
#pragma unroll
    for (int dd = 0; dd < 2; ++dd)
#pragma unroll
      for (int g = 0; g < 4; ++g) {
        f32x4 v;
        if (kh) { v[0] = oacc[dd][4 * g]; v[1] = oacc[dd][4 * g + 1]; v[2] = oacc[dd][4 * g + 2]; v[3] = oacc[dd][4 * g + 3]; }
        else { v[0] = oacc[2 + dd][4 * g]; v[1] = oacc[2 + dd][4 * g + 1]; v[2] = oacc[2 + dd][4 * g + 2]; v[3] = oacc[2 + dd][4 * g + 3]; }
        *(__attribute__((address_space(3))) f32x4*)(xo + w * 8192 + (dd * 4 + g) * 1024 + lane * 16) = v;
      }
    BARRIER();
    const float mp = ml[(w ^ 4) * 64 + r32], lp = ml[(w ^ 4) * 64 + 32 + r32];
    const float mf = fmaxf(m, mp);
    const float wa = __builtin_amdgcn_exp2f(m - mf), wb = __builtin_amdgcn_exp2f(mp - mf);
    const float inv = 1.f / (wa * l + wb * lp);
    const float fa = wa * inv, fb = wb * inv;
    const int mblk = b * 4 + qg;
#pragma unroll
    for (int dd = 0; dd < 2; ++dd) {
      float o[16];
#pragma unroll
      for (int g = 0; g < 4; ++g) {
        f32x4 v = *(__attribute__((address_space(3))) f32x4*)(xo + (w ^ 4) * 8192 + (dd * 4 + g) * 1024 + lane * 16);
#pragma unroll
        for (int e = 0; e < 4; ++e) {
          const float own = kh ? oacc[2 + dd][4 * g + e] : oacc[dd][4 * g + e];
          o[4 * g + e] = own * fa + v[e] * fb;
        }
      }
      const int dblk = 2 * kh + dd;
#pragma unroll
      for (int sp = 0; sp < 2; ++sp) {
        bf16x8 f = pack8(o[8 * sp + 0], o[8 * sp + 1], o[8 * sp + 2], o[8 * sp + 3], o[8 * sp + 4], o[8 * sp + 5], o[8 * sp + 6], o[8 * sp + 7]);
        const size_t ks = (size_t)mblk * 256 + head * 8 + dblk * 2 + sp;
        *(bf16x8*)((char*)ows + (ks * 64 + lane) * 16) = f;
      }
    }
  }
}

__global__ __launch_bounds__(512, 2) void oproj_kernel(const __hip_bfloat16* __restrict__ ows, const __hip_bfloat16* __restrict__ wo,
                                                       __hip_bfloat16* __restrict__ out) {
  extern __shared__ __attribute__((aligned(16))) char smem[];
  lds_char* L = (lds_char*)smem;
  const unsigned L0 = (unsigned)(uintptr_t)smem;
  const int tid = threadIdx.x, lane = tid & 63;
  const int w = __builtin_amdgcn_readfirstlane(tid >> 6);
  const int r32 = lane & 31, hi = lane >> 5;
  const int q4 = (lane & 15) >> 2, p4 = lane & 3, g1 = (lane >> 4) & 1;
  const int bid = blockIdx.x;
  const int mblk = (bid & 7) * 4 + ((bid >> 3) & 3), nblk = bid >> 5;

  bf16x8 of[16][2];
  const char* ob = (const char*)ows + ((size_t)mblk * 256 + w * 32) * 1024;
  const unsigned oo = lane * 16;
  const char* wb = (const char*)wo + (size_t)(w * 512) * 1024 + nblk * 128;
  const int drow = lane >> 3, dpos = lane & 7;
  const unsigned wof = (unsigned)(drow * 1024 + ((dpos ^ (((drow >> 1) & 1) << 2)) << 4));
  const unsigned lw = L0 + w * 20480;
#define ISSUE_STEP(c) do { \
    asm volatile("global_load_dwordx4 %0, %1, %2 offset:0" : "=v"(of[c][0]) : "v"(oo), "s"(ob + (c) * 2048) : "memory"); \
    asm volatile("global_load_dwordx4 %0, %1, %2 offset:1024" : "=v"(of[c][1]) : "v"(oo), "s"(ob + (c) * 2048) : "memory"); \
    _Pragma("unroll") for (int j_ = 0; j_ < 4; ++j_) \
      dma1(wof, wb + (size_t)((c) * 32 + j_ * 8) * 1024, lw + ((c) % 5) * 4096 + j_ * 1024); } while (0)
  ISSUE_STEP(0); ISSUE_STEP(1); ISSUE_STEP(2); ISSUE_STEP(3);
  f32x16 acc[2] = {};
  int ta[2];
#pragma unroll
  for (int nb = 0; nb < 2; ++nb) ta[nb] = w * 20480 + (4 * hi + q4) * 128 + ((nb ^ (q4 >> 1)) << 6) + g1 * 32 + p4 * 8;
#pragma unroll
  for (int c = 0; c < 16; ++c) {
    if (c < 13) WAITVML(18); else if (c == 13) WAITVML(12); else if (c == 14) WAITVML(6); else WAITVML(0);
    PIN(of[c][0]); PIN(of[c][1]);
    bf16x8 af[2][2];
#pragma unroll
    for (int t2 = 0; t2 < 2; ++t2)
#pragma unroll
      for (int nb = 0; nb < 2; ++nb)
        af[t2][nb] = cat8(lds_tr(L + ta[nb] + (c % 5) * 4096 + t2 * 2048), lds_tr(L + ta[nb] + (c % 5) * 4096 + t2 * 2048 + 1024));
    if (c + 4 < 16) ISSUE_STEP(c + 4);
#pragma unroll
    for (int t2 = 0; t2 < 2; ++t2)
#pragma unroll
      for (int nb = 0; nb < 2; ++nb) acc[nb] = MFMA32(af[t2][nb], of[c][t2], acc[nb]);
  }
  BARRIER();
#pragma unroll
  for (int nb = 0; nb < 2; ++nb)
#pragma unroll
    for (int g = 0; g < 4; ++g) {
      f32x4 v = {acc[nb][4 * g], acc[nb][4 * g + 1], acc[nb][4 * g + 2], acc[nb][4 * g + 3]};
      *(__attribute__((address_space(3))) f32x4*)(L + w * 8192 + (nb * 4 + g) * 1024 + lane * 16) = v;
    }
  BARRIER();
  f32x4 s = {0.f, 0.f, 0.f, 0.f};
#pragma unroll
  for (int ww = 0; ww < 8; ++ww) s += *(__attribute__((address_space(3))) f32x4*)(L + ww * 8192 + w * 1024 + lane * 16);
  const int nb = w >> 2, g = w & 3;
  u32x2 pk = {cvtpk(s[0], s[1]), cvtpk(s[2], s[3])};
  *(u32x2*)((char*)out + ((size_t)(mblk * 32 + r32) * 512 + nblk * 64 + nb * 32 + 8 * g + 4 * hi) * 2) = pk;
}

extern "C" void kernel_launch(void* const* d_in, const int* in_sizes, int n_in, void* d_out, int out_size, void* d_ws, size_t ws_size,
                              hipStream_t stream) {
  const __hip_bfloat16* x = (const __hip_bfloat16*)d_in[0];
  const __hip_bfloat16* kc = (const __hip_bfloat16*)d_in[1];
  const __hip_bfloat16* vc = (const __hip_bfloat16*)d_in[2];
  const __hip_bfloat16* wq = (const __hip_bfloat16*)d_in[3];
  const __hip_bfloat16* wo = (const __hip_bfloat16*)d_in[4];
  __hip_bfloat16* out = (__hip_bfloat16*)d_out;
  __hip_bfloat16* ows = (__hip_bfloat16*)d_ws;
  (void)hipFuncSetAttribute((const void*)attn_kernel, hipFuncAttributeMaxDynamicSharedMemorySize, LDS1);
  (void)hipFuncSetAttribute((const void*)oproj_kernel, hipFuncAttributeMaxDynamicSharedMemorySize, LDS2);
  attn_kernel<<<256, 512, LDS1, stream>>>(x, kc, vc, wq, ows);
  oproj_kernel<<<256, 512, LDS2, stream>>>(ows, wo, out);
}
```

```cpp
#include <hip/hip_runtime.h>
#include <hip/hip_bf16.h>
#include <stdint.h>

typedef __attribute__((ext_vector_type(8))) short bf16x8;
typedef __attribute__((ext_vector_type(4))) short s16x4;
typedef __attribute__((ext_vector_type(16))) float f32x16;
typedef __attribute__((ext_vector_type(4))) float f32x4;
typedef __attribute__((ext_vector_type(4))) unsigned u32x4;
typedef __attribute__((ext_vector_type(2))) unsigned u32x2;
typedef __attribute__((address_space(3))) char lds_char;
typedef short v4i16_t __attribute__((ext_vector_type(4)));

constexpr int NB = 8, SQ = 128, DM = 512, NH = 32, SK = 2048, DH = 128;
constexpr int NT = SK / 64;
constexpr int LDS1 = 163840;
constexpr int P_OFF = 81920;
constexpr int XCH_OFF = 131072;
constexpr int LDS2 = 163840;
constexpr float CSC = 0.08838834764831845f * 1.4426950408889634f;

#define MFMA32(a, b, c) __builtin_amdgcn_mfma_f32_32x32x16_bf16(a, b, c, 0, 0, 0)

typedef __attribute__((ext_vector_type(2))) float f32x2;
typedef __attribute__((ext_vector_type(2))) __bf16 bf16x2_t;
__device__ __forceinline__ unsigned cvtpk(float lo, float hi) {
  f32x2 v = {lo, hi};
  return __builtin_bit_cast(unsigned, __builtin_convertvector(v, bf16x2_t));
}
__device__ __forceinline__ bf16x8 pack8(float a0, float a1, float a2, float a3, float a4, float a5, float a6, float a7) {
  u32x4 w = {cvtpk(a0, a1), cvtpk(a2, a3), cvtpk(a4, a5), cvtpk(a6, a7)};
  return __builtin_bit_cast(bf16x8, w);
}
__device__ __forceinline__ bf16x8 lds_r128(lds_char* p) { return *(__attribute__((address_space(3))) bf16x8*)p; }
__device__ __forceinline__ s16x4 lds_tr(lds_char* p) {
  return __builtin_bit_cast(s16x4, __builtin_amdgcn_ds_read_tr16_b64_v4i16((__attribute__((address_space(3))) v4i16_t*)p));
}
__device__ __forceinline__ bf16x8 cat8(s16x4 a, s16x4 b) { return __builtin_shufflevector(a, b, 0, 1, 2, 3, 4, 5, 6, 7); }

__device__ __forceinline__ void dma4(unsigned o0, unsigned o1, unsigned o2, unsigned o3, const void* sbase, unsigned ldsaddr) {
  unsigned keep;
  asm volatile(
      "s_mov_b32 %0, m0\n\t"
      "s_mov_b32 m0, %6\n\t"
      "s_nop 0\n\t"
      "global_load_lds_dwordx4 %1, %5 nt\n\t"
      "global_load_lds_dwordx4 %2, %5 offset:1024 nt\n\t"
      "global_load_lds_dwordx4 %3, %5 offset:2048 nt\n\t"
      "global_load_lds_dwordx4 %4, %5 offset:3072 nt\n\t"
      "s_mov_b32 m0, %0"
      : "=&s"(keep) : "v"(o0), "v"(o1), "v"(o2), "v"(o3), "s"(sbase), "s"(ldsaddr) : "memory");
}
__device__ __forceinline__ void dma2(unsigned o0, unsigned o1, const void* sbase, unsigned ldsaddr) {
  unsigned keep;
  asm volatile(
      "s_mov_b32 %0, m0\n\t"
      "s_mov_b32 m0, %4\n\t"
      "s_nop 0\n\t"
      "global_load_lds_dwordx4 %1, %3\n\t"
      "global_load_lds_dwordx4 %2, %3 offset:1024\n\t"
      "s_mov_b32 m0, %0"
      : "=&s"(keep) : "v"(o0), "v"(o1), "s"(sbase), "s"(ldsaddr) : "memory");
}
__device__ __forceinline__ void dma1(unsigned o0, const void* sbase, unsigned ldsaddr) {
  unsigned keep;
  asm volatile(
      "s_mov_b32 %0, m0\n\t"
      "s_mov_b32 m0, %3\n\t"
      "s_nop 0\n\t"
      "global_load_lds_dwordx4 %1, %2\n\t"
      "s_mov_b32 m0, %0"
      : "=&s"(keep) : "v"(o0), "s"(sbase), "s"(ldsaddr) : "memory");
}
template <int J> __device__ __forceinline__ void dmap(unsigned o, const void* sbase, unsigned ldsaddr) {
  unsigned keep;
  asm volatile("s_mov_b32 %0, m0\n\ts_mov_b32 m0, %3\n\ts_nop 0\n\tglobal_load_lds_dwordx4 %1, %2 offset:%4 nt\n\ts_mov_b32 m0, %0"
               : "=&s"(keep) : "v"(o), "s"(sbase), "s"(ldsaddr), "i"(J * 1024) : "memory");
}
__device__ __forceinline__ float fadd_s(float a, float b) { float r; asm("v_add_f32_e32 %0, %1, %2" : "=v"(r) : "v"(a), "v"(b)); return r; }
#define GAP() __builtin_amdgcn_sched_barrier(0)
__device__ __forceinline__ int kslot(int i) { return i * 16384 + (i >= 3 ? 32768 : 0); }
__device__ __forceinline__ int vslot(int i) { return 49152 + i * 16384 + (i >= 2 ? 32768 : 0); }
#define WAITVM(n) asm volatile("s_waitcnt vmcnt(" #n ")" ::: "memory")
#define WAITVML(n) asm volatile("s_waitcnt vmcnt(" #n ") lgkmcnt(0)" ::: "memory")
#define BARRIER() asm volatile("s_waitcnt lgkmcnt(0)\n\ts_barrier" ::: "memory")
#define PIN(x) asm volatile("" : "+v"(x))

__device__ __forceinline__ float swapmax(float v) {
  auto rr = __builtin_amdgcn_permlane32_swap(__float_as_uint(v), __float_as_uint(v), false, false);
  return fmaxf(__uint_as_float(rr[0]), __uint_as_float(rr[1]));
}
__device__ __forceinline__ float swapadd(float v) {
  auto rr = __builtin_amdgcn_permlane32_swap(__float_as_uint(v), __float_as_uint(v), false, false);
  return __uint_as_float(rr[0]) + __uint_as_float(rr[1]);
}

__global__ __launch_bounds__(512, 2) void attn_kernel(const __hip_bfloat16* __restrict__ x, const __hip_bfloat16* __restrict__ kc,
                                                      const __hip_bfloat16* __restrict__ vc, const __hip_bfloat16* __restrict__ wq,
                                                      __hip_bfloat16* __restrict__ ows) {
  extern __shared__ __attribute__((aligned(16))) char smem[];
  lds_char* L = (lds_char*)smem;
  const unsigned L0 = (unsigned)(uintptr_t)smem;
  const int tid = threadIdx.x, lane = tid & 63;
  const int w = __builtin_amdgcn_readfirstlane(tid >> 6);
  const int qg = w & 3, kh = w >> 2;
  const int r32 = lane & 31, hi = lane >> 5;
  const int q4 = (lane & 15) >> 2, p4 = lane & 3, g1 = (lane >> 4) & 1;
  const int bid = blockIdx.x;
  const int head = 4 * (bid & 7) + ((bid >> 3) & 3), b = bid >> 5;
  const char* kbase = (const char*)kc + ((size_t)(b * NH + head) * SK) * 256;
  const char* vbase = (const char*)vc + ((size_t)(b * NH + head) * SK) * 256;
  const char* wqbase = (const char*)wq + head * 256;

  const int drow = lane >> 4, dpos = lane & 15;
  unsigned ko[4];
#pragma unroll
  for (int j = 0; j < 4; ++j) ko[j] = (unsigned)(((w & 3) * 16 + drow) * 256 + ((dpos ^ (4 * j + drow)) << 4));
  const unsigned vo = (unsigned)(((w & 3) * 16 + drow) * 256 + ((dpos ^ (drow << 2)) << 4));
  unsigned wo2[2], xo2[2];
#pragma unroll
  for (int j = 0; j < 2; ++j) {
    wo2[j] = (unsigned)(((w & 3) * 8 + 4 * j + drow) * 8192 + ((dpos ^ (drow << 2)) << 4) - j * 1024);
    xo2[j] = (unsigned)(((w & 3) * 32 + 16 * j + (lane >> 2)) * 1024 + (((lane & 3) ^ drow) << 4) - j * 1024);
  }
  const char* xbase = (const char*)x + (size_t)b * SQ * 1024;

#define ISSUE_K(t, la) dma4(ko[0], ko[1], ko[2], ko[3], kbase + (size_t)(t) * 16384, (la) + (w & 3) * 4096)
#define ISSUE_V(t, la) dma4(vo, vo, vo, vo, vbase + (size_t)(t) * 16384, (la) + (w & 3) * 4096)
#define ISSUE_P(c) do { dma2(wo2[0], wo2[1], wqbase + (size_t)(c) * 32 * 8192, L0 + P_OFF + ((c) % 5) * 16384 + (w & 3) * 2048); \
    dma2(xo2[0], xo2[1], xbase + (c) * 64, L0 + P_OFF + ((c) % 5) * 16384 + 8192 + (w & 3) * 2048); } while (0)

  if (w < 4) {
    ISSUE_K(0, L0 + kslot(0)); ISSUE_K(1, L0 + kslot(1)); ISSUE_V(0, L0 + vslot(0)); ISSUE_K(2, L0 + kslot(2)); ISSUE_V(1, L0 + vslot(1));
  } else { ISSUE_P(0); ISSUE_P(1); ISSUE_P(2); ISSUE_P(3); }

  f32x16 qa[2] = {};
  {
    const int ps = ((p4 & 1) << 1) | (p4 >> 1);
    int wa[2], xa[2];
#pragma unroll
    for (int dd = 0; dd < 2; ++dd) {
      const int dblk = 2 * kh + dd;
      wa[dd] = (8 * hi + q4) * 256 + (((4 * dblk + 2 * g1 + (ps >> 1)) ^ (q4 << 2)) << 4) + 8 * (ps & 1);
      xa[dd] = 8192 + (qg * 32 + r32) * 64 + (((2 * dd + hi) ^ ((r32 >> 2) & 3)) << 4);
    }
#pragma unroll
    for (int c = 0; c < 16; ++c) {
      if (w >= 4) { if (c < 13) WAITVM(12); else if (c == 13) WAITVM(8); else if (c == 14) WAITVM(4); else WAITVM(0); }
      BARRIER();
      if (w >= 4 && c + 4 < 16) ISSUE_P(c + 4);
      lds_char* wb = L + P_OFF + (c % 5) * 16384;
#pragma unroll
      for (int s = 0; s < 2; ++s) {
        const bf16x8 xf = lds_r128(wb + xa[s]);
#pragma unroll
        for (int dd = 0; dd < 2; ++dd) {
          s16x4 a0 = lds_tr(wb + wa[dd] + s * 4096);
          s16x4 a1 = lds_tr(wb + wa[dd] + s * 4096 + 1024);
          qa[dd] = MFMA32(cat8(a0, a1), xf, qa[dd]);
        }
      }
    }
  }
  bf16x8 qf[8];
  {
    bf16x8 own[4];
#pragma unroll
    for (int dd = 0; dd < 2; ++dd)
#pragma unroll
      for (int sp = 0; sp < 2; ++sp)
      {
        u32x4 t = __builtin_bit_cast(u32x4, pack8(qa[dd][8 * sp + 0], qa[dd][8 * sp + 1], qa[dd][8 * sp + 2], qa[dd][8 * sp + 3],
                                                  qa[dd][8 * sp + 4], qa[dd][8 * sp + 5], qa[dd][8 * sp + 6], qa[dd][8 * sp + 7]));
#pragma unroll
        for (int e = 0; e < 4; ++e) t[e] = cvtpk(__uint_as_float(t[e] << 16) * CSC, __uint_as_float(t[e] & 0xffff0000u) * CSC);
        own[dd * 2 + sp] = __builtin_bit_cast(bf16x8, t);
      }
    BARRIER();
#pragma unroll
    for (int i = 0; i < 4; ++i) *(__attribute__((address_space(3))) bf16x8*)(L + XCH_OFF + w * 4096 + i * 1024 + lane * 16) = own[i];
    if (w < 4) WAITVM(12);
    BARRIER();
#pragma unroll
    for (int i = 0; i < 4; ++i) {
      bf16x8 o = lds_r128(L + XCH_OFF + (w ^ 4) * 4096 + i * 1024 + lane * 16);
      if (kh == 0) { qf[i] = own[i]; qf[4 + i] = o; } else { qf[4 + i] = own[i]; qf[i] = o; }
    }
  }

  f32x16 oacc[4] = {};
  const int krow = kh * 32 + r32;
  const int kaddr0 = krow * 256 + ((hi ^ (krow & 15)) << 4);
  int vaddr0[4];
#pragma unroll
  for (int d = 0; d < 4; ++d) vaddr0[d] = (kh * 32 + 4 * hi + q4) * 256 + ((d ^ q4) << 6) + g1 * 32 + p4 * 8;

  bf16x8 kf[8], vf[4][2], pf[2];
  f32x16 sc = {}, sn, negm;
#pragma unroll
  for (int s = 0; s < 8; ++s) kf[s] = lds_r128(L + kslot(0) + (kaddr0 ^ (s << 5)));
#pragma unroll
  for (int s = 0; s < 8; ++s) sc = MFMA32(kf[s], qf[s], sc);
#pragma unroll
  for (int s = 0; s < 8; ++s) kf[s] = lds_r128(L + kslot(1) + (kaddr0 ^ (s << 5)));
  float m;
  {
    float tmax = sc[0];
#pragma unroll
    for (int r = 1; r < 16; ++r) tmax = fmaxf(tmax, sc[r]);
    m = swapmax(tmax);
#pragma unroll
    for (int r = 0; r < 16; ++r) { sc[r] -= m; negm[r] = -m; }
  }
  float l0 = 0.f, l1 = 0.f;
  const char* dbase = w < 4 ? kbase : vbase;
  const unsigned dof0 = w < 4 ? ko[0] : vo, dof1 = w < 4 ? ko[1] : vo, dof2 = w < 4 ? ko[2] : vo, dof3 = w < 4 ? ko[3] : vo;
  const int dahead = w < 4 ? 6 : 4;
  int ks = kslot(2), vs = vslot(0), ksp = kslot(1), vsp = vslot(4);

#define RDV(d, s) vf[d][s] = cat8(lds_tr(vb + vaddr0[d] + (s) * 4096), lds_tr(vb + vaddr0[d] + (s) * 4096 + 2048))
#define RDK(s) kf[s] = lds_r128(kb + (kaddr0 ^ ((s) << 5)))
#define EXP(SC, r) SC[r] = __builtin_amdgcn_exp2f(SC[r])
#define CVT(SC, s, e) pw[s][e] = cvtpk(SC[8 * (s) + 2 * (e)], SC[8 * (s) + 2 * (e) + 1])
#define ADD(SC, r) do { l0 = fadd_s(l0, SC[r]); l1 = fadd_s(l1, SC[(r) + 1]); } while (0)
#define PVM(d, s) oacc[d] = MFMA32(vf[d][s], __builtin_bit_cast(bf16x8, pw[s]), oacc[d])
#define QKM(SN, s) SN = MFMA32(kf[s], qf[s], SN)
#define DMA(J, o) if (doit) dmap<J>(o, dptr, dla)
#define RESCALE(SN, tmax) do { \
    if (__builtin_amdgcn_ballot_w64(!(tmax <= 8.f)) != 0) { \
      const float mn = m + fmaxf(tmax, 0.f); \
      const float dl = mn - m; \
      const float alpha = __builtin_amdgcn_exp2f(-dl); \
      _Pragma("unroll") for (int d = 0; d < 4; ++d) oacc[d] *= alpha; \
      _Pragma("unroll") for (int r = 0; r < 16; ++r) { SN[r] -= dl; negm[r] = -mn; } \
      l0 *= alpha; l1 *= alpha; m = mn; \
    } } while (0)
#define BODY(SC, SN, HASK) do { \
    lds_char* vb = L + vs; \
    lds_char* kb = L + ks; \
    u32x4 pw[2]; \
    SN = MFMA32(kf[0], qf[0], negm); RDV(0, 0); RDV(1, 0); EXP(SC, 0); EXP(SC, 1); GAP(); \
    QKM(SN, 1); RDV(2, 0); RDV(3, 0); EXP(SC, 2); EXP(SC, 3); CVT(SC, 0, 0); GAP(); \
    QKM(SN, 2); RDV(0, 1); RDV(1, 1); EXP(SC, 4); EXP(SC, 5); CVT(SC, 0, 1); GAP(); \
    QKM(SN, 3); RDV(2, 1); RDV(3, 1); EXP(SC, 6); EXP(SC, 7); CVT(SC, 0, 2); GAP(); \
    QKM(SN, 4); DMA(0, dof0); EXP(SC, 8); EXP(SC, 9); CVT(SC, 0, 3); GAP(); \
    QKM(SN, 5); DMA(1, dof1); EXP(SC, 10); EXP(SC, 11); CVT(SC, 1, 0); GAP(); \
    QKM(SN, 6); DMA(2, dof2); EXP(SC, 12); EXP(SC, 13); CVT(SC, 1, 1); GAP(); \
    QKM(SN, 7); DMA(3, dof3); EXP(SC, 14); EXP(SC, 15); CVT(SC, 1, 2); GAP(); \
    PVM(0, 0); if (HASK) { RDK(0); RDK(1); } CVT(SC, 1, 3); ADD(SC, 0); ADD(SC, 2); GAP(); \
    PVM(1, 0); if (HASK) { RDK(2); RDK(3); } ADD(SC, 4); ADD(SC, 6); GAP(); \
    PVM(2, 0); if (HASK) { RDK(4); RDK(5); } ADD(SC, 8); ADD(SC, 10); GAP(); \
    PVM(3, 0); if (HASK) { RDK(6); RDK(7); } ADD(SC, 12); ADD(SC, 14); GAP(); \
    float t0 = fmaxf(fmaxf(SN[0], SN[1]), SN[2]), t1 = fmaxf(fmaxf(SN[3], SN[4]), SN[5]); \
    PVM(0, 1); t0 = fmaxf(fmaxf(t0, SN[6]), SN[7]); t1 = fmaxf(fmaxf(t1, SN[8]), SN[9]); GAP(); \
    PVM(1, 1); t0 = fmaxf(fmaxf(t0, SN[10]), SN[11]); t1 = fmaxf(fmaxf(t1, SN[12]), SN[13]); GAP(); \
    PVM(2, 1); t0 = fmaxf(fmaxf(t0, SN[14]), SN[15]); t0 = swapmax(fmaxf(t0, t1)); GAP(); \
    PVM(3, 1); GAP(); \
    RESCALE(SN, t0); \
    ksp = ks; vsp = vs; \
    ks = ks == 32768 ? 81920 : (ks == 98304 ? 0 : ks + 16384); \
    vs = vs == 65536 ? 114688 : (vs == 147456 ? 49152 : vs + 16384); } while (0)

  {
    if (w < 4) WAITVM(0);
    BARRIER();
    if (w < 4) { ISSUE_K(3, L0 + kslot(3)); ISSUE_K(4, L0 + kslot(4)); ISSUE_K(5, L0 + kslot(0)); ISSUE_K(6, L0 + kslot(1)); }
    else { ISSUE_V(2, L0 + vslot(2)); ISSUE_V(3, L0 + vslot(3)); ISSUE_V(4, L0 + vslot(4)); }
    const bool doit = false; const char* dptr = dbase; const unsigned dla = 0;
    BODY(sc, sn, true);
  }
#define DSETUP(it) \
    const char* dptr = dbase + (size_t)((it) + dahead) * 16384; \
    const unsigned dla = L0 + (w < 4 ? ksp : vsp) + (w & 3) * 4096;
#pragma unroll 1
  for (int it = 1; it < 25; it += 2) {
    { WAITVM(12); BARRIER(); const bool doit = true; DSETUP(it); BODY(sn, sc, true); }
    { WAITVM(12); BARRIER(); const bool doit = true; DSETUP(it + 1); BODY(sc, sn, true); }
  }
#define TAILWAIT(it) do { \
    const int nout = w < 4 ? 29 - (it) : 31 - (it); \
    if (nout >= 3) WAITVM(12); else if (nout == 2) WAITVM(8); else if (nout == 1) WAITVM(4); else WAITVM(0); } while (0)
#pragma unroll 1
  for (int it = 25; it < 29; it += 2) {
    { TAILWAIT(it); BARRIER(); const bool doit = it + dahead < NT; DSETUP(it); BODY(sn, sc, true); }
    { TAILWAIT(it + 1); BARRIER(); const bool doit = it + 1 + dahead < NT; DSETUP(it + 1); BODY(sc, sn, true); }
  }
  {
    const bool doit = false; const char* dptr = dbase; const unsigned dla = 0;
    TAILWAIT(29); BARRIER(); BODY(sn, sc, true);
    TAILWAIT(30); BARRIER(); BODY(sc, sn, false);
  }
  {
    WAITVM(0);
    BARRIER();
    lds_char* vb = L + vs;
    u32x4 pw[2];
#pragma unroll
    for (int s = 0; s < 2; ++s)
#pragma unroll
      for (int d = 0; d < 4; ++d) RDV(d, s);
#pragma unroll
    for (int r = 0; r < 16; ++r) EXP(sn, r);
#pragma unroll
    for (int r = 0; r < 16; r += 2) ADD(sn, r);
#pragma unroll
    for (int s = 0; s < 2; ++s)
#pragma unroll
      for (int e = 0; e < 4; ++e) CVT(sn, s, e);
#pragma unroll
    for (int s = 0; s < 2; ++s)
#pragma unroll
      for (int d = 0; d < 4; ++d) PVM(d, s);
  }

  const float l = swapadd(l0 + l1);
  {
    int lane;
    asm volatile("v_mbcnt_lo_u32_b32 %0, -1, 0\n\tv_mbcnt_hi_u32_b32 %0, -1, %0" : "=v"(lane));
    const int r32 = lane & 31, hi = lane >> 5;
    float* ml = (float*)(smem + 147456);
    lds_char* xo = L + 81920;
    if (hi == 0) { ml[w * 64 + r32] = m; ml[w * 64 + 32 + r32] = l; }
#pragma unroll
    for (int dd = 0; dd < 2; ++dd)
#pragma unroll
      for (int g = 0; g < 4; ++g) {
        f32x4 v;
        if (kh) { v[0] = oacc[dd][4 * g]; v[1] = oacc[dd][4 * g + 1]; v[2] = oacc[dd][4 * g + 2]; v[3] = oacc[dd][4 * g + 3]; }
        else { v[0] = oacc[2 + dd][4 * g]; v[1] = oacc[2 + dd][4 * g + 1]; v[2] = oacc[2 + dd][4 * g + 2]; v[3] = oacc[2 + dd][4 * g + 3]; }
        *(__attribute__((address_space(3))) f32x4*)(xo + w * 8192 + (dd * 4 + g) * 1024 + lane * 16) = v;
      }
    BARRIER();
    const float mp = ml[(w ^ 4) * 64 + r32], lp = ml[(w ^ 4) * 64 + 32 + r32];
    const float mf = fmaxf(m, mp);
    const float wa = __builtin_amdgcn_exp2f(m - mf), wb = __builtin_amdgcn_exp2f(mp - mf);
    const float inv = 1.f / (wa * l + wb * lp);
    const float fa = wa * inv, fb = wb * inv;
    const int mblk = b * 4 + qg;
#pragma unroll
    for (int dd = 0; dd < 2; ++dd) {
      float o[16];
#pragma unroll
      for (int g = 0; g < 4; ++g) {
        f32x4 v = *(__attribute__((address_space(3))) f32x4*)(xo + (w ^ 4) * 8192 + (dd * 4 + g) * 1024 + lane * 16);
#pragma unroll
        for (int e = 0; e < 4; ++e) {
          const float own = kh ? oacc[2 + dd][4 * g + e] : oacc[dd][4 * g + e];
          o[4 * g + e] = own * fa + v[e] * fb;
        }
      }
      const int dblk = 2 * kh + dd;
#pragma unroll
      for (int sp = 0; sp < 2; ++sp) {
        bf16x8 f = pack8(o[8 * sp + 0], o[8 * sp + 1], o[8 * sp + 2], o[8 * sp + 3], o[8 * sp + 4], o[8 * sp + 5], o[8 * sp + 6], o[8 * sp + 7]);
        const size_t ks = (size_t)mblk * 256 + head * 8 + dblk * 2 + sp;
        *(bf16x8*)((char*)ows + (ks * 64 + lane) * 16) = f;
      }
    }
  }
}

__global__ __launch_bounds__(512, 2) void oproj_kernel(const __hip_bfloat16* __restrict__ ows, const __hip_bfloat16* __restrict__ wo,
                                                       __hip_bfloat16* __restrict__ out) {
  extern __shared__ __attribute__((aligned(16))) char smem[];
  lds_char* L = (lds_char*)smem;
  const unsigned L0 = (unsigned)(uintptr_t)smem;
  const int tid = threadIdx.x, lane = tid & 63;
  const int w = __builtin_amdgcn_readfirstlane(tid >> 6);
  const int r32 = lane & 31, hi = lane >> 5;
  const int q4 = (lane & 15) >> 2, p4 = lane & 3, g1 = (lane >> 4) & 1;
  const int bid = blockIdx.x;
  const int mblk = (bid & 7) * 4 + ((bid >> 3) & 3), nblk = bid >> 5;

  bf16x8 of[16][2];
  const char* ob = (const char*)ows + ((size_t)mblk * 256 + w * 32) * 1024;
  const unsigned oo = lane * 16;
  const char* wb = (const char*)wo + (size_t)(w * 512) * 1024 + nblk * 128;
  const int drow = lane >> 3, dpos = lane & 7;
  const unsigned wof = (unsigned)(drow * 1024 + ((dpos ^ (((drow >> 1) & 1) << 2)) << 4));
  const unsigned lw = L0 + w * 20480;
#define ISSUE_STEP(c) do { \
    asm volatile("global_load_dwordx4 %0, %1, %2 offset:0" : "=v"(of[c][0]) : "v"(oo), "s"(ob + (c) * 2048) : "memory"); \
    asm volatile("global_load_dwordx4 %0, %1, %2 offset:1024" : "=v"(of[c][1]) : "v"(oo), "s"(ob + (c) * 2048) : "memory"); \
    _Pragma("unroll") for (int j_ = 0; j_ < 4; ++j_) \
      dma1(wof, wb + (size_t)((c) * 32 + j_ * 8) * 1024, lw + ((c) % 5) * 4096 + j_ * 1024); } while (0)
  ISSUE_STEP(0); ISSUE_STEP(1); ISSUE_STEP(2); ISSUE_STEP(3);
  f32x16 acc[2] = {};
  int ta[2];
#pragma unroll
  for (int nb = 0; nb < 2; ++nb) ta[nb] = w * 20480 + (4 * hi + q4) * 128 + ((nb ^ (q4 >> 1)) << 6) + g1 * 32 + p4 * 8;
#pragma unroll
  for (int c = 0; c < 16; ++c) {
    if (c < 13) WAITVML(18); else if (c == 13) WAITVML(12); else if (c == 14) WAITVML(6); else WAITVML(0);
    PIN(of[c][0]); PIN(of[c][1]);
    bf16x8 af[2][2];
#pragma unroll
    for (int t2 = 0; t2 < 2; ++t2)
#pragma unroll
      for (int nb = 0; nb < 2; ++nb)
        af[t2][nb] = cat8(lds_tr(L + ta[nb] + (c % 5) * 4096 + t2 * 2048), lds_tr(L + ta[nb] + (c % 5) * 4096 + t2 * 2048 + 1024));
    if (c + 4 < 16) ISSUE_STEP(c + 4);
#pragma unroll
    for (int t2 = 0; t2 < 2; ++t2)
#pragma unroll
      for (int nb = 0; nb < 2; ++nb) acc[nb] = MFMA32(af[t2][nb], of[c][t2], acc[nb]);
  }
  BARRIER();
#pragma unroll
  for (int nb = 0; nb < 2; ++nb)
#pragma unroll
    for (int g = 0; g < 4; ++g) {
      f32x4 v = {acc[nb][4 * g], acc[nb][4 * g + 1], acc[nb][4 * g + 2], acc[nb][4 * g + 3]};
      *(__attribute__((address_space(3))) f32x4*)(L + w * 8192 + (nb * 4 + g) * 1024 + lane * 16) = v;
    }
  BARRIER();
  f32x4 s = {0.f, 0.f, 0.f, 0.f};
#pragma unroll
  for (int ww = 0; ww < 8; ++ww) s += *(__attribute__((address_space(3))) f32x4*)(L + ww * 8192 + w * 1024 + lane * 16);
  const int nb = w >> 2, g = w & 3;
  u32x2 pk = {cvtpk(s[0], s[1]), cvtpk(s[2], s[3])};
  *(u32x2*)((char*)out + ((size_t)(mblk * 32 + r32) * 512 + nblk * 64 + nb * 32 + 8 * g + 4 * hi) * 2) = pk;
}

extern "C" void kernel_launch(void* const* d_in, const int* in_sizes, int n_in, void* d_out, int out_size, void* d_ws, size_t ws_size,
                              hipStream_t stream) {
  const __hip_bfloat16* x = (const __hip_bfloat16*)d_in[0];
  const __hip_bfloat16* kc = (const __hip_bfloat16*)d_in[1];
  const __hip_bfloat16* vc = (const __hip_bfloat16*)d_in[2];
  const __hip_bfloat16* wq = (const __hip_bfloat16*)d_in[3];
  const __hip_bfloat16* wo = (const __hip_bfloat16*)d_in[4];
  __hip_bfloat16* out = (__hip_bfloat16*)d_out;
  __hip_bfloat16* ows = (__hip_bfloat16*)d_ws;
  (void)hipFuncSetAttribute((const void*)attn_kernel, hipFuncAttributeMaxDynamicSharedMemorySize, LDS1);
  (void)hipFuncSetAttribute((const void*)oproj_kernel, hipFuncAttributeMaxDynamicSharedMemorySize, LDS2);
  attn_kernel<<<256, 512, LDS1, stream>>>(x, kc, vc, wq, ows);
  oproj_kernel<<<256, 512, LDS2, stream>>>(ows, wo, out);
}
```

```cpp
#include <hip/hip_runtime.h>
#include <hip/hip_bf16.h>
#include <stdint.h>

typedef __attribute__((ext_vector_type(8))) short bf16x8;
typedef __attribute__((ext_vector_type(4))) short s16x4;
typedef __attribute__((ext_vector_type(16))) float f32x16;
typedef __attribute__((ext_vector_type(4))) float f32x4;
typedef __attribute__((ext_vector_type(4))) unsigned u32x4;
typedef __attribute__((ext_vector_type(2))) unsigned u32x2;
typedef __attribute__((address_space(3))) char lds_char;
typedef short v4i16_t __attribute__((ext_vector_type(4)));

constexpr int NB = 8, SQ = 128, DM = 512, NH = 32, SK = 2048, DH = 128;
constexpr int NT = SK / 64;
constexpr int LDS1 = 163840;
constexpr int P_OFF = 32768;
constexpr int XCH_OFF = 131072;
constexpr int LDS2 = 163840;
constexpr float CSC = 0.08838834764831845f * 1.4426950408889634f;

#define MFMA32(a, b, c) __builtin_amdgcn_mfma_f32_32x32x16_bf16(a, b, c, 0, 0, 0)

typedef __attribute__((ext_vector_type(2))) float f32x2;
typedef __attribute__((ext_vector_type(2))) __bf16 bf16x2_t;
__device__ __forceinline__ unsigned cvtpk(float lo, float hi) {
  f32x2 v = {lo, hi};
  return __builtin_bit_cast(unsigned, __builtin_convertvector(v, bf16x2_t));
}
__device__ __forceinline__ bf16x8 pack8(float a0, float a1, float a2, float a3, float a4, float a5, float a6, float a7) {
  u32x4 w = {cvtpk(a0, a1), cvtpk(a2, a3), cvtpk(a4, a5), cvtpk(a6, a7)};
  return __builtin_bit_cast(bf16x8, w);
}
__device__ __forceinline__ bf16x8 lds_r128(lds_char* p) { return *(__attribute__((address_space(3))) bf16x8*)p; }
__device__ __forceinline__ s16x4 lds_tr(lds_char* p) {
  return __builtin_bit_cast(s16x4, __builtin_amdgcn_ds_read_tr16_b64_v4i16((__attribute__((address_space(3))) v4i16_t*)p));
}
__device__ __forceinline__ bf16x8 cat8(s16x4 a, s16x4 b) { return __builtin_shufflevector(a, b, 0, 1, 2, 3, 4, 5, 6, 7); }

__device__ __forceinline__ void dma4(unsigned o0, unsigned o1, unsigned o2, unsigned o3, const void* sbase, unsigned ldsaddr) {
  unsigned keep;
  asm volatile(
      "s_mov_b32 %0, m0\n\t"
      "s_mov_b32 m0, %6\n\t"
      "s_nop 0\n\t"
      "global_load_lds_dwordx4 %1, %5 nt\n\t"
      "global_load_lds_dwordx4 %2, %5 offset:1024 nt\n\t"
      "global_load_lds_dwordx4 %3, %5 offset:2048 nt\n\t"
      "global_load_lds_dwordx4 %4, %5 offset:3072 nt\n\t"
      "s_mov_b32 m0, %0"
      : "=&s"(keep) : "v"(o0), "v"(o1), "v"(o2), "v"(o3), "s"(sbase), "s"(ldsaddr) : "memory");
}
__device__ __forceinline__ void dma2(unsigned o0, unsigned o1, const void* sbase, unsigned ldsaddr) {
  unsigned keep;
  asm volatile(
      "s_mov_b32 %0, m0\n\t"
      "s_mov_b32 m0, %4\n\t"
      "s_nop 0\n\t"
      "global_load_lds_dwordx4 %1, %3\n\t"
      "global_load_lds_dwordx4 %2, %3 offset:1024\n\t"
      "s_mov_b32 m0, %0"
      : "=&s"(keep) : "v"(o0), "v"(o1), "s"(sbase), "s"(ldsaddr) : "memory");
}
__device__ __forceinline__ void dma1(unsigned o0, const void* sbase, unsigned ldsaddr) {
  unsigned keep;
  asm volatile(
      "s_mov_b32 %0, m0\n\t"
      "s_mov_b32 m0, %3\n\t"
      "s_nop 0\n\t"
      "global_load_lds_dwordx4 %1, %2\n\t"
      "s_mov_b32 m0, %0"
      : "=&s"(keep) : "v"(o0), "s"(sbase), "s"(ldsaddr) : "memory");
}
template <int J> __device__ __forceinline__ void dmap(unsigned o, const void* sbase, unsigned ldsaddr) {
  unsigned keep;
  asm volatile("s_mov_b32 %0, m0\n\ts_mov_b32 m0, %3\n\ts_nop 0\n\tglobal_load_lds_dwordx4 %1, %2 offset:%4 nt\n\ts_mov_b32 m0, %0"
               : "=&s"(keep) : "v"(o), "s"(sbase), "s"(ldsaddr), "i"(J * 1024) : "memory");
}
__device__ __forceinline__ float fadd_s(float a, float b) { float r; asm("v_add_f32_e32 %0, %1, %2" : "=v"(r) : "v"(a), "v"(b)); return r; }
#define GAP() __builtin_amdgcn_sched_barrier(0)
__device__ __forceinline__ int kslot(int i) { return i == 0 ? 0 : i * 32768 - 16384; }
__device__ __forceinline__ int vslot(int i) { return i == 4 ? 147456 : i * 32768 + 32768; }
#define WAITVM(n) asm volatile("s_waitcnt vmcnt(" #n ")" ::: "memory")
#define WAITVML(n) asm volatile("s_waitcnt vmcnt(" #n ") lgkmcnt(0)" ::: "memory")
#define BARRIER() asm volatile("s_waitcnt lgkmcnt(0)\n\ts_barrier" ::: "memory")
#define PIN(x) asm volatile("" : "+v"(x))

__device__ __forceinline__ float swapmax(float v) {
  auto rr = __builtin_amdgcn_permlane32_swap(__float_as_uint(v), __float_as_uint(v), false, false);
  return fmaxf(__uint_as_float(rr[0]), __uint_as_float(rr[1]));
}
__device__ __forceinline__ float swapadd(float v) {
  auto rr = __builtin_amdgcn_permlane32_swap(__float_as_uint(v), __float_as_uint(v), false, false);
  return __uint_as_float(rr[0]) + __uint_as_float(rr[1]);
}

__global__ __launch_bounds__(512, 2) void attn_kernel(const __hip_bfloat16* __restrict__ x, const __hip_bfloat16* __restrict__ kc,
                                                      const __hip_bfloat16* __restrict__ vc, const __hip_bfloat16* __restrict__ wq,
                                                      __hip_bfloat16* __restrict__ ows) {
  extern __shared__ __attribute__((aligned(16))) char smem[];
  lds_char* L = (lds_char*)smem;
  const unsigned L0 = (unsigned)(uintptr_t)smem;
  const int tid = threadIdx.x, lane = tid & 63;
  const int w = __builtin_amdgcn_readfirstlane(tid >> 6);
  const int qg = w & 3, kh = w >> 2;
  const int r32 = lane & 31, hi = lane >> 5;
  const int q4 = (lane & 15) >> 2, p4 = lane & 3, g1 = (lane >> 4) & 1;
  const int bid = blockIdx.x;
  const int head = 4 * (bid & 7) + ((bid >> 3) & 3), b = bid >> 5;
  const char* kbase = (const char*)kc + ((size_t)(b * NH + head) * SK) * 256;
  const char* vbase = (const char*)vc + ((size_t)(b * NH + head) * SK) * 256;
  const char* wqbase = (const char*)wq + head * 256;

  const int drow = lane >> 4, dpos = lane & 15;
  unsigned ko[4];
#pragma unroll
  for (int j = 0; j < 4; ++j) ko[j] = (unsigned)(((w & 3) * 16 + drow) * 256 + ((dpos ^ (4 * j + drow)) << 4));
  const unsigned vo = (unsigned)(((w & 3) * 16 + drow) * 256 + ((dpos ^ (drow << 2)) << 4));
  unsigned wo2[2], xo2[2];
#pragma unroll
  for (int j = 0; j < 2; ++j) {
    wo2[j] = (unsigned)(((w & 3) * 8 + 4 * j + drow) * 8192 + ((dpos ^ (drow << 2)) << 4) - j * 1024);
    xo2[j] = (unsigned)(((w & 3) * 32 + 16 * j + (lane >> 2)) * 1024 + (((lane & 3) ^ drow) << 4) - j * 1024);
  }
  const char* xbase = (const char*)x + (size_t)b * SQ * 1024;

#define ISSUE_K(t, la) dma4(ko[0], ko[1], ko[2], ko[3], kbase + (size_t)(t) * 16384, (la) + (w & 3) * 4096)
#define ISSUE_V(t, la) dma4(vo, vo, vo, vo, vbase + (size_t)(t) * 16384, (la) + (w & 3) * 4096)
#define ROT 1
  const int crot = ROT ? 2 * b + (head & 1) + 8 * ((head >> 1) & 1) : 0;
#define ISSUE_P(c) do { const int cc_ = ((c) + crot) & 15; \
    dma2(wo2[0], wo2[1], wqbase + (size_t)cc_ * 32 * 8192, L0 + P_OFF + ((c) % 8) * 16384 + (w & 3) * 2048); \
    dma2(xo2[0], xo2[1], xbase + cc_ * 64, L0 + P_OFF + ((c) % 8) * 16384 + 8192 + (w & 3) * 2048); } while (0)

#define K0C 0
#define K1C 2
  if (w >= 4) { ISSUE_P(0); ISSUE_P(1); ISSUE_P(2); ISSUE_P(3); ISSUE_P(4); ISSUE_P(5); ISSUE_P(6); }
  else { if (K0C < 0) ISSUE_K(0, L0 + kslot(0)); if (K1C < 0) ISSUE_K(1, L0 + kslot(1)); }

  f32x16 qa[2] = {};
  {
    const int ps = ((p4 & 1) << 1) | (p4 >> 1);
    int wa[2], xa[2];
#pragma unroll
    for (int dd = 0; dd < 2; ++dd) {
      const int dblk = 2 * kh + dd;
      wa[dd] = (8 * hi + q4) * 256 + (((4 * dblk + 2 * g1 + (ps >> 1)) ^ (q4 << 2)) << 4) + 8 * (ps & 1);
      xa[dd] = 8192 + (qg * 32 + r32) * 64 + (((2 * dd + hi) ^ ((r32 >> 2) & 3)) << 4);
    }
#pragma unroll
    for (int c = 0; c < 16; ++c) {
      if (w >= 4) { if (c < 11) WAITVM(24); else if (c < 13) WAITVM(20); else if (c < 15) WAITVM(16); else WAITVM(12); }
      BARRIER();
      if (w >= 4) {
        if (c + 7 < 16) ISSUE_P(c + 7);
        if (c == 9 || c == 11 || c == 13) ISSUE_V((c - 9) / 2, L0 + vslot((c - 9) / 2));
      } else {
        if (c == K0C) ISSUE_K(0, L0 + kslot(0));
        if (c == K1C) ISSUE_K(1, L0 + kslot(1));
        if (c == 10 || c == 12 || c == 14) ISSUE_K((c - 6) / 2, L0 + kslot((c - 6) / 2));
      }
      lds_char* wb = L + P_OFF + (c % 8) * 16384;
#pragma unroll
      for (int s = 0; s < 2; ++s) {
        const bf16x8 xf = lds_r128(wb + xa[s]);
#pragma unroll
        for (int dd = 0; dd < 2; ++dd) {
          s16x4 a0 = lds_tr(wb + wa[dd] + s * 4096);
          s16x4 a1 = lds_tr(wb + wa[dd] + s * 4096 + 1024);
          qa[dd] = MFMA32(cat8(a0, a1), xf, qa[dd]);
        }
      }
    }
  }
  bf16x8 qf[8];
  {
    bf16x8 own[4];
#pragma unroll
    for (int dd = 0; dd < 2; ++dd)
#pragma unroll
      for (int sp = 0; sp < 2; ++sp)
      {
        u32x4 t = __builtin_bit_cast(u32x4, pack8(qa[dd][8 * sp + 0], qa[dd][8 * sp + 1], qa[dd][8 * sp + 2], qa[dd][8 * sp + 3],
                                                  qa[dd][8 * sp + 4], qa[dd][8 * sp + 5], qa[dd][8 * sp + 6], qa[dd][8 * sp + 7]));
#pragma unroll
        for (int e = 0; e < 4; ++e) t[e] = cvtpk(__uint_as_float(t[e] << 16) * CSC, __uint_as_float(t[e] & 0xffff0000u) * CSC);
        own[dd * 2 + sp] = __builtin_bit_cast(bf16x8, t);
      }
    BARRIER();
#pragma unroll
    for (int i = 0; i < 4; ++i) *(__attribute__((address_space(3))) bf16x8*)(L + XCH_OFF + w * 4096 + i * 1024 + lane * 16) = own[i];
    if (w < 4) WAITVM(12);
    BARRIER();
#pragma unroll
    for (int i = 0; i < 4; ++i) {
      bf16x8 o = lds_r128(L + XCH_OFF + (w ^ 4) * 4096 + i * 1024 + lane * 16);
      if (kh == 0) { qf[i] = own[i]; qf[4 + i] = o; } else { qf[4 + i] = own[i]; qf[i] = o; }
    }
  }

  f32x16 oacc[4] = {};
  const int krow = kh * 32 + r32;
  const int kaddr0 = krow * 256 + ((hi ^ (krow & 15)) << 4);
  int vaddr0[4];
#pragma unroll
  for (int d = 0; d < 4; ++d) vaddr0[d] = (kh * 32 + 4 * hi + q4) * 256 + ((d ^ q4) << 6) + g1 * 32 + p4 * 8;

  bf16x8 kf[8], vf[4][2], pf[2];
  f32x16 sc = {}, sn, negm;
#pragma unroll
  for (int s = 0; s < 8; ++s) kf[s] = lds_r128(L + kslot(0) + (kaddr0 ^ (s << 5)));
#pragma unroll
  for (int s = 0; s < 8; ++s) sc = MFMA32(kf[s], qf[s], sc);
#pragma unroll
  for (int s = 0; s < 8; ++s) kf[s] = lds_r128(L + kslot(1) + (kaddr0 ^ (s << 5)));
  float m;
  {
    float tmax = sc[0];
#pragma unroll
    for (int r = 1; r < 16; ++r) tmax = fmaxf(tmax, sc[r]);
    m = swapmax(tmax);
#pragma unroll
    for (int r = 0; r < 16; ++r) { sc[r] -= m; negm[r] = -m; }
  }
  float l0 = 0.f, l1 = 0.f;
  const char* dbase = w < 4 ? kbase : vbase;
  const unsigned dof0 = w < 4 ? ko[0] : vo, dof1 = w < 4 ? ko[1] : vo, dof2 = w < 4 ? ko[2] : vo, dof3 = w < 4 ? ko[3] : vo;
  const int dahead = w < 4 ? 6 : 4;
  int ks = kslot(2), vs = vslot(0), ksp = kslot(1), vsp = vslot(4);

#define RDV(d, s) vf[d][s] = cat8(lds_tr(vb + vaddr0[d] + (s) * 4096), lds_tr(vb + vaddr0[d] + (s) * 4096 + 2048))
#define RDK(s) kf[s] = lds_r128(kb + (kaddr0 ^ ((s) << 5)))
#define EXP(SC, r) SC[r] = __builtin_amdgcn_exp2f(SC[r])
#define CVT(SC, s, e) pw[s][e] = cvtpk(SC[8 * (s) + 2 * (e)], SC[8 * (s) + 2 * (e) + 1])
#define ADD(SC, r) do { l0 = fadd_s(l0, SC[r]); l1 = fadd_s(l1, SC[(r) + 1]); } while (0)
#define PVM(d, s) oacc[d] = MFMA32(vf[d][s], __builtin_bit_cast(bf16x8, pw[s]), oacc[d])
#define QKM(SN, s) SN = MFMA32(kf[s], qf[s], SN)
#define DMA(J, o) if (doit) dmap<J>(o, dptr, dla)
#define RESCALE(SN, tmax) do { \
    if (__builtin_amdgcn_ballot_w64(!(tmax <= 8.f)) != 0) { \
      const float mn = m + fmaxf(tmax, 0.f); \
      const float dl = mn - m; \
      const float alpha = __builtin_amdgcn_exp2f(-dl); \
      _Pragma("unroll") for (int d = 0; d < 4; ++d) oacc[d] *= alpha; \
      _Pragma("unroll") for (int r = 0; r < 16; ++r) { SN[r] -= dl; negm[r] = -mn; } \
      l0 *= alpha; l1 *= alpha; m = mn; \
    } } while (0)
#define BODY(SC, SN, HASK) do { \
    lds_char* vb = L + vs; \
    lds_char* kb = L + ks; \
    u32x4 pw[2]; \
    SN = MFMA32(kf[0], qf[0], negm); RDV(0, 0); RDV(1, 0); EXP(SC, 0); EXP(SC, 1); GAP(); \
    QKM(SN, 1); RDV(2, 0); RDV(3, 0); EXP(SC, 2); EXP(SC, 3); CVT(SC, 0, 0); GAP(); \
    QKM(SN, 2); RDV(0, 1); RDV(1, 1); EXP(SC, 4); EXP(SC, 5); CVT(SC, 0, 1); GAP(); \
    QKM(SN, 3); RDV(2, 1); RDV(3, 1); EXP(SC, 6); EXP(SC, 7); CVT(SC, 0, 2); GAP(); \
    QKM(SN, 4); DMA(0, dof0); EXP(SC, 8); EXP(SC, 9); CVT(SC, 0, 3); GAP(); \
    QKM(SN, 5); DMA(1, dof1); EXP(SC, 10); EXP(SC, 11); CVT(SC, 1, 0); GAP(); \
    QKM(SN, 6); DMA(2, dof2); EXP(SC, 12); EXP(SC, 13); CVT(SC, 1, 1); GAP(); \
    QKM(SN, 7); DMA(3, dof3); EXP(SC, 14); EXP(SC, 15); CVT(SC, 1, 2); GAP(); \
    PVM(0, 0); if (HASK) { RDK(0); RDK(1); } CVT(SC, 1, 3); ADD(SC, 0); ADD(SC, 2); GAP(); \
    PVM(1, 0); if (HASK) { RDK(2); RDK(3); } ADD(SC, 4); ADD(SC, 6); GAP(); \
    PVM(2, 0); if (HASK) { RDK(4); RDK(5); } ADD(SC, 8); ADD(SC, 10); GAP(); \
    PVM(3, 0); if (HASK) { RDK(6); RDK(7); } ADD(SC, 12); ADD(SC, 14); GAP(); \
    float t0 = fmaxf(fmaxf(SN[0], SN[1]), SN[2]), t1 = fmaxf(fmaxf(SN[3], SN[4]), SN[5]); \
    PVM(0, 1); t0 = fmaxf(fmaxf(t0, SN[6]), SN[7]); t1 = fmaxf(fmaxf(t1, SN[8]), SN[9]); GAP(); \
    PVM(1, 1); t0 = fmaxf(fmaxf(t0, SN[10]), SN[11]); t1 = fmaxf(fmaxf(t1, SN[12]), SN[13]); GAP(); \
    PVM(2, 1); t0 = fmaxf(fmaxf(t0, SN[14]), SN[15]); t0 = swapmax(fmaxf(t0, t1)); GAP(); \
    PVM(3, 1); GAP(); \
    RESCALE(SN, t0); \
    ksp = ks; vsp = vs; \
    ks = ks == 0 ? 16384 : (ks == 114688 ? 0 : ks + 32768); \
    vs = vs == 131072 ? 147456 : (vs == 147456 ? 32768 : vs + 32768); } while (0)

  {
    WAITVM(8);
    BARRIER();
    if (w < 4) { ISSUE_K(5, L0 + kslot(0)); ISSUE_K(6, L0 + kslot(1)); }
    else { ISSUE_V(3, L0 + vslot(3)); ISSUE_V(4, L0 + vslot(4)); }
    const bool doit = false; const char* dptr = dbase; const unsigned dla = 0;
    BODY(sc, sn, true);
  }
#define DSETUP(it) \
    const char* dptr = dbase + (size_t)((it) + dahead) * 16384; \
    const unsigned dla = L0 + (w < 4 ? ksp : vsp) + (w & 3) * 4096;
#pragma unroll 1
  for (int it = 1; it < 25; it += 2) {
    { WAITVM(12); BARRIER(); const bool doit = true; DSETUP(it); BODY(sn, sc, true); }
    { WAITVM(12); BARRIER(); const bool doit = true; DSETUP(it + 1); BODY(sc, sn, true); }
  }
#define TAILWAIT(it) do { \
    const int nout = w < 4 ? 29 - (it) : 31 - (it); \
    if (nout >= 3) WAITVM(12); else if (nout == 2) WAITVM(8); else if (nout == 1) WAITVM(4); else WAITVM(0); } while (0)
#pragma unroll 1
  for (int it = 25; it < 29; it += 2) {
    { TAILWAIT(it); BARRIER(); const bool doit = it + dahead < NT; DSETUP(it); BODY(sn, sc, true); }
    { TAILWAIT(it + 1); BARRIER(); const bool doit = it + 1 + dahead < NT; DSETUP(it + 1); BODY(sc, sn, true); }
  }
  {
    const bool doit = false; const char* dptr = dbase; const unsigned dla = 0;
    TAILWAIT(29); BARRIER(); BODY(sn, sc, true);
    TAILWAIT(30); BARRIER(); BODY(sc, sn, false);
  }
  {
    WAITVM(0);
    BARRIER();
    lds_char* vb = L + vs;
    u32x4 pw[2];
#pragma unroll
    for (int s = 0; s < 2; ++s)
#pragma unroll
      for (int d = 0; d < 4; ++d) RDV(d, s);
#pragma unroll
    for (int r = 0; r < 16; ++r) EXP(sn, r);
#pragma unroll
    for (int r = 0; r < 16; r += 2) ADD(sn, r);
#pragma unroll
    for (int s = 0; s < 2; ++s)
#pragma unroll
      for (int e = 0; e < 4; ++e) CVT(sn, s, e);
#pragma unroll
    for (int s = 0; s < 2; ++s)
#pragma unroll
      for (int d = 0; d < 4; ++d) PVM(d, s);
  }

  const float l = swapadd(l0 + l1);
  {
    int lane;
    asm volatile("v_mbcnt_lo_u32_b32 %0, -1, 0\n\tv_mbcnt_hi_u32_b32 %0, -1, %0" : "=v"(lane));
    const int r32 = lane & 31, hi = lane >> 5;
    float* ml = (float*)(smem + 147456);
    lds_char* xo = L + 81920;
    if (hi == 0) { ml[w * 64 + r32] = m; ml[w * 64 + 32 + r32] = l; }
#pragma unroll
    for (int dd = 0; dd < 2; ++dd)
#pragma unroll
      for (int g = 0; g < 4; ++g) {
        f32x4 v;
        if (kh) { v[0] = oacc[dd][4 * g]; v[1] = oacc[dd][4 * g + 1]; v[2] = oacc[dd][4 * g + 2]; v[3] = oacc[dd][4 * g + 3]; }
        else { v[0] = oacc[2 + dd][4 * g]; v[1] = oacc[2 + dd][4 * g + 1]; v[2] = oacc[2 + dd][4 * g + 2]; v[3] = oacc[2 + dd][4 * g + 3]; }
        *(__attribute__((address_space(3))) f32x4*)(xo + w * 8192 + (dd * 4 + g) * 1024 + lane * 16) = v;
      }
    BARRIER();
    const float mp = ml[(w ^ 4) * 64 + r32], lp = ml[(w ^ 4) * 64 + 32 + r32];
    const float mf = fmaxf(m, mp);
    const float wa = __builtin_amdgcn_exp2f(m - mf), wb = __builtin_amdgcn_exp2f(mp - mf);
    const float inv = 1.f / (wa * l + wb * lp);
    const float fa = wa * inv, fb = wb * inv;
    const int mblk = b * 4 + qg;
#pragma unroll
    for (int dd = 0; dd < 2; ++dd) {
      float o[16];
#pragma unroll
      for (int g = 0; g < 4; ++g) {
        f32x4 v = *(__attribute__((address_space(3))) f32x4*)(xo + (w ^ 4) * 8192 + (dd * 4 + g) * 1024 + lane * 16);
#pragma unroll
        for (int e = 0; e < 4; ++e) {
          const float own = kh ? oacc[2 + dd][4 * g + e] : oacc[dd][4 * g + e];
          o[4 * g + e] = own * fa + v[e] * fb;
        }
      }
      const int dblk = 2 * kh + dd;
#pragma unroll
      for (int sp = 0; sp < 2; ++sp) {
        bf16x8 f = pack8(o[8 * sp + 0], o[8 * sp + 1], o[8 * sp + 2], o[8 * sp + 3], o[8 * sp + 4], o[8 * sp + 5], o[8 * sp + 6], o[8 * sp + 7]);
        const size_t ks = (size_t)mblk * 256 + head * 8 + dblk * 2 + sp;
        *(bf16x8*)((char*)ows + (ks * 64 + lane) * 16) = f;
      }
    }
  }
}

__global__ __launch_bounds__(512, 2) void oproj_kernel(const __hip_bfloat16* __restrict__ ows, const __hip_bfloat16* __restrict__ wo,
                                                       __hip_bfloat16* __restrict__ out) {
  extern __shared__ __attribute__((aligned(16))) char smem[];
  lds_char* L = (lds_char*)smem;
  const unsigned L0 = (unsigned)(uintptr_t)smem;
  const int tid = threadIdx.x, lane = tid & 63;
  const int w = __builtin_amdgcn_readfirstlane(tid >> 6);
  const int r32 = lane & 31, hi = lane >> 5;
  const int q4 = (lane & 15) >> 2, p4 = lane & 3, g1 = (lane >> 4) & 1;
  const int bid = blockIdx.x;
  const int mblk = (bid & 7) * 4 + ((bid >> 3) & 3), nblk = bid >> 5;

  bf16x8 of[16][2];
  const char* ob = (const char*)ows + ((size_t)mblk * 256 + w * 32) * 1024;
  const unsigned oo = lane * 16;
  const char* wb = (const char*)wo + (size_t)(w * 512) * 1024 + nblk * 128;
  const int drow = lane >> 3, dpos = lane & 7;
  const unsigned wof = (unsigned)(drow * 1024 + ((dpos ^ (((drow >> 1) & 1) << 2)) << 4));
  const unsigned lw = L0 + w * 20480;
#define ISSUE_STEP(c) do { \
    asm volatile("global_load_dwordx4 %0, %1, %2 offset:0" : "=v"(of[c][0]) : "v"(oo), "s"(ob + (c) * 2048) : "memory"); \
    asm volatile("global_load_dwordx4 %0, %1, %2 offset:1024" : "=v"(of[c][1]) : "v"(oo), "s"(ob + (c) * 2048) : "memory"); \
    _Pragma("unroll") for (int j_ = 0; j_ < 4; ++j_) \
      dma1(wof, wb + (size_t)((c) * 32 + j_ * 8) * 1024, lw + ((c) % 5) * 4096 + j_ * 1024); } while (0)
  ISSUE_STEP(0); ISSUE_STEP(1); ISSUE_STEP(2); ISSUE_STEP(3);
  f32x16 acc[2] = {};
  int ta[2];
#pragma unroll
  for (int nb = 0; nb < 2; ++nb) ta[nb] = w * 20480 + (4 * hi + q4) * 128 + ((nb ^ (q4 >> 1)) << 6) + g1 * 32 + p4 * 8;
#pragma unroll
  for (int c = 0; c < 16; ++c) {
    if (c < 13) WAITVML(18); else if (c == 13) WAITVML(12); else if (c == 14) WAITVML(6); else WAITVML(0);
    PIN(of[c][0]); PIN(of[c][1]);
    bf16x8 af[2][2];
#pragma unroll
    for (int t2 = 0; t2 < 2; ++t2)
#pragma unroll
      for (int nb = 0; nb < 2; ++nb)
        af[t2][nb] = cat8(lds_tr(L + ta[nb] + (c % 5) * 4096 + t2 * 2048), lds_tr(L + ta[nb] + (c % 5) * 4096 + t2 * 2048 + 1024));
    if (c + 4 < 16) ISSUE_STEP(c + 4);
#pragma unroll
    for (int t2 = 0; t2 < 2; ++t2)
#pragma unroll
      for (int nb = 0; nb < 2; ++nb) acc[nb] = MFMA32(af[t2][nb], of[c][t2], acc[nb]);
  }
  BARRIER();
#pragma unroll
  for (int nb = 0; nb < 2; ++nb)
#pragma unroll
    for (int g = 0; g < 4; ++g) {
      f32x4 v = {acc[nb][4 * g], acc[nb][4 * g + 1], acc[nb][4 * g + 2], acc[nb][4 * g + 3]};
      *(__attribute__((address_space(3))) f32x4*)(L + w * 8192 + (nb * 4 + g) * 1024 + lane * 16) = v;
    }
  BARRIER();
  f32x4 s = {0.f, 0.f, 0.f, 0.f};
#pragma unroll
  for (int ww = 0; ww < 8; ++ww) s += *(__attribute__((address_space(3))) f32x4*)(L + ww * 8192 + w * 1024 + lane * 16);
  const int nb = w >> 2, g = w & 3;
  u32x2 pk = {cvtpk(s[0], s[1]), cvtpk(s[2], s[3])};
  *(u32x2*)((char*)out + ((size_t)(mblk * 32 + r32) * 512 + nblk * 64 + nb * 32 + 8 * g + 4 * hi) * 2) = pk;
}

extern "C" void kernel_launch(void* const* d_in, const int* in_sizes, int n_in, void* d_out, int out_size, void* d_ws, size_t ws_size,
                              hipStream_t stream) {
  const __hip_bfloat16* x = (const __hip_bfloat16*)d_in[0];
  const __hip_bfloat16* kc = (const __hip_bfloat16*)d_in[1];
  const __hip_bfloat16* vc = (const __hip_bfloat16*)d_in[2];
  const __hip_bfloat16* wq = (const __hip_bfloat16*)d_in[3];
  const __hip_bfloat16* wo = (const __hip_bfloat16*)d_in[4];
  __hip_bfloat16* out = (__hip_bfloat16*)d_out;
  __hip_bfloat16* ows = (__hip_bfloat16*)d_ws;
  (void)hipFuncSetAttribute((const void*)attn_kernel, hipFuncAttributeMaxDynamicSharedMemorySize, LDS1);
  (void)hipFuncSetAttribute((const void*)oproj_kernel, hipFuncAttributeMaxDynamicSharedMemorySize, LDS2);
  attn_kernel<<<256, 512, LDS1, stream>>>(x, kc, vc, wq, ows);
  oproj_kernel<<<256, 512, LDS2, stream>>>(ows, wo, out);
}
```

```cpp
#include <hip/hip_runtime.h>
#include <hip/hip_bf16.h>
#include <stdint.h>

typedef __attribute__((ext_vector_type(8))) short bf16x8;
typedef __attribute__((ext_vector_type(4))) short s16x4;
typedef __attribute__((ext_vector_type(16))) float f32x16;
typedef __attribute__((ext_vector_type(4))) float f32x4;
typedef __attribute__((ext_vector_type(4))) unsigned u32x4;
typedef __attribute__((ext_vector_type(2))) unsigned u32x2;
typedef __attribute__((address_space(3))) char lds_char;
typedef short v4i16_t __attribute__((ext_vector_type(4)));

constexpr int NB = 8, SQ = 128, DM = 512, NH = 32, SK = 2048, DH = 128;
constexpr int NT = SK / 64;
constexpr int LDS1 = 163840;
constexpr int P_OFF = 32768;
constexpr int XCH_OFF = 131072;
constexpr int LDS2 = 163840;
constexpr float CSC = 0.08838834764831845f * 1.4426950408889634f;

#define MFMA32(a, b, c) __builtin_amdgcn_mfma_f32_32x32x16_bf16(a, b, c, 0, 0, 0)

typedef __attribute__((ext_vector_type(2))) float f32x2;
typedef __attribute__((ext_vector_type(2))) __bf16 bf16x2_t;
__device__ __forceinline__ unsigned cvtpk(float lo, float hi) {
  f32x2 v = {lo, hi};
  return __builtin_bit_cast(unsigned, __builtin_convertvector(v, bf16x2_t));
}
__device__ __forceinline__ bf16x8 pack8(float a0, float a1, float a2, float a3, float a4, float a5, float a6, float a7) {
  u32x4 w = {cvtpk(a0, a1), cvtpk(a2, a3), cvtpk(a4, a5), cvtpk(a6, a7)};
  return __builtin_bit_cast(bf16x8, w);
}
__device__ __forceinline__ bf16x8 lds_r128(lds_char* p) { return *(__attribute__((address_space(3))) bf16x8*)p; }
__device__ __forceinline__ s16x4 lds_tr(lds_char* p) {
  return __builtin_bit_cast(s16x4, __builtin_amdgcn_ds_read_tr16_b64_v4i16((__attribute__((address_space(3))) v4i16_t*)p));
}
__device__ __forceinline__ bf16x8 cat8(s16x4 a, s16x4 b) { return __builtin_shufflevector(a, b, 0, 1, 2, 3, 4, 5, 6, 7); }

__device__ __forceinline__ void dma4(unsigned o0, unsigned o1, unsigned o2, unsigned o3, const void* sbase, unsigned ldsaddr) {
  unsigned keep;
  asm volatile(
      "s_mov_b32 %0, m0\n\t"
      "s_mov_b32 m0, %6\n\t"
      "s_nop 0\n\t"
      "global_load_lds_dwordx4 %1, %5 nt\n\t"
      "global_load_lds_dwordx4 %2, %5 offset:1024 nt\n\t"
      "global_load_lds_dwordx4 %3, %5 offset:2048 nt\n\t"
      "global_load_lds_dwordx4 %4, %5 offset:3072 nt\n\t"
      "s_mov_b32 m0, %0"
      : "=&s"(keep) : "v"(o0), "v"(o1), "v"(o2), "v"(o3), "s"(sbase), "s"(ldsaddr) : "memory");
}
__device__ __forceinline__ void dma2(unsigned o0, unsigned o1, const void* sbase, unsigned ldsaddr) {
  unsigned keep;
  asm volatile(
      "s_mov_b32 %0, m0\n\t"
      "s_mov_b32 m0, %4\n\t"
      "s_nop 0\n\t"
      "global_load_lds_dwordx4 %1, %3\n\t"
      "global_load_lds_dwordx4 %2, %3 offset:1024\n\t"
      "s_mov_b32 m0, %0"
      : "=&s"(keep) : "v"(o0), "v"(o1), "s"(sbase), "s"(ldsaddr) : "memory");
}
__device__ __forceinline__ void dma1(unsigned o0, const void* sbase, unsigned ldsaddr) {
  unsigned keep;
  asm volatile(
      "s_mov_b32 %0, m0\n\t"
      "s_mov_b32 m0, %3\n\t"
      "s_nop 0\n\t"
      "global_load_lds_dwordx4 %1, %2\n\t"
      "s_mov_b32 m0, %0"
      : "=&s"(keep) : "v"(o0), "s"(sbase), "s"(ldsaddr) : "memory");
}
template <int J> __device__ __forceinline__ void dmap(unsigned o, const void* sbase, unsigned ldsaddr) {
  unsigned keep;
  asm volatile("s_mov_b32 %0, m0\n\ts_mov_b32 m0, %3\n\ts_nop 0\n\tglobal_load_lds_dwordx4 %1, %2 offset:%4 nt\n\ts_mov_b32 m0, %0"
               : "=&s"(keep) : "v"(o), "s"(sbase), "s"(ldsaddr), "i"(J * 1024) : "memory");
}
template <int J> __device__ __forceinline__ void dmapw(unsigned o, const void* sbase, unsigned ldsaddr) {
  unsigned keep;
  asm volatile("s_mov_b32 %0, m0\n\ts_mov_b32 m0, %3\n\ts_nop 0\n\tglobal_load_lds_dwordx4 %1, %2 offset:%4\n\ts_mov_b32 m0, %0"
               : "=&s"(keep) : "v"(o), "s"(sbase), "s"(ldsaddr), "i"(J * 1024) : "memory");
}
__device__ __forceinline__ int lane_id() {
  int lane;
  asm volatile("v_mbcnt_lo_u32_b32 %0, -1, 0\n\tv_mbcnt_hi_u32_b32 %0, -1, %0" : "=v"(lane));
  return lane;
}
__device__ __forceinline__ float fadd_s(float a, float b) { float r; asm("v_add_f32_e32 %0, %1, %2" : "=v"(r) : "v"(a), "v"(b)); return r; }
#define GAP() __builtin_amdgcn_sched_barrier(0)
__device__ __forceinline__ int kslot(int i) { return i == 0 ? 0 : i * 32768 - 16384; }
__device__ __forceinline__ int vslot(int i) { return i == 4 ? 147456 : i * 32768 + 32768; }
#define WAITVM(n) asm volatile("s_waitcnt vmcnt(" #n ")" ::: "memory")
#define WAITVML(n) asm volatile("s_waitcnt vmcnt(" #n ") lgkmcnt(0)" ::: "memory")
#define BARRIER() asm volatile("s_waitcnt lgkmcnt(0)\n\ts_barrier" ::: "memory")
#define PIN(x) asm volatile("" : "+v"(x))

__device__ __forceinline__ float swapmax(float v) {
  auto rr = __builtin_amdgcn_permlane32_swap(__float_as_uint(v), __float_as_uint(v), false, false);
  return fmaxf(__uint_as_float(rr[0]), __uint_as_float(rr[1]));
}
__device__ __forceinline__ float swapadd(float v) {
  auto rr = __builtin_amdgcn_permlane32_swap(__float_as_uint(v), __float_as_uint(v), false, false);
  return __uint_as_float(rr[0]) + __uint_as_float(rr[1]);
}

__global__ __launch_bounds__(512, 2) void fused_kernel(const __hip_bfloat16* __restrict__ x, const __hip_bfloat16* __restrict__ kc,
                                                       const __hip_bfloat16* __restrict__ vc, const __hip_bfloat16* __restrict__ wq,
                                                       const __hip_bfloat16* __restrict__ wo, char* ows, unsigned* flg,
                                                       __hip_bfloat16* __restrict__ out) {
  extern __shared__ __attribute__((aligned(16))) char smem[];
  lds_char* L = (lds_char*)smem;
  const unsigned L0 = (unsigned)(uintptr_t)smem;
  const int tid = threadIdx.x, lane = tid & 63;
  const int w = __builtin_amdgcn_readfirstlane(tid >> 6);
  const int qg = w & 3, kh = w >> 2;
  const int r32 = lane & 31, hi = lane >> 5;
  const int q4 = (lane & 15) >> 2, p4 = lane & 3, g1 = (lane >> 4) & 1;
  const int bid = blockIdx.x;
  const int head = 4 * (bid & 7) + ((bid >> 3) & 3), b = bid >> 5;
  const char* kbase = (const char*)kc + ((size_t)(b * NH + head) * SK) * 256;
  const char* vbase = (const char*)vc + ((size_t)(b * NH + head) * SK) * 256;
  const char* wqbase = (const char*)wq + head * 256;
#define FSTR 256
#define XA 2
  const int xcd = bid & 7, jx = bid >> 3;
  const int nblk = ((xcd & ((8 >> XA) - 1)) << XA) | (jx & ((1 << XA) - 1)), mblk = (xcd >> (3 - XA)) * (32 >> XA) + (jx >> XA);
  int fia = (mblk >> 2) * 32 + w;
  int fib = ((((4 * b) % (32 >> XA)) << XA) | (w & ((1 << XA) - 1))) * 8 + ((((4 * b) / (32 >> XA)) << (3 - XA)) | (w >> XA)) - (32 << XA);
  int fic = bid;
  asm volatile("" : "+s"(fia), "+s"(fib), "+s"(fic));
#define FOFF(lane) (((lane) < 8 ? ((lane) < 4 ? fia + 8 * (lane) : fib + ((lane) << (3 + XA))) : fic) * FSTR)
  unsigned fv;
  asm volatile("global_load_dword %0, %1, %2 sc1" : "=v"(fv) : "v"((unsigned)FOFF(lane)), "s"(flg) : "memory");

  const int drow = lane >> 4, dpos = lane & 15;
  unsigned ko[4];
#pragma unroll
  for (int j = 0; j < 4; ++j) ko[j] = (unsigned)(((w & 3) * 16 + drow) * 256 + ((dpos ^ (4 * j + drow)) << 4));
  const unsigned vo = (unsigned)(((w & 3) * 16 + drow) * 256 + ((dpos ^ (drow << 2)) << 4));
  unsigned wo2[2], xo2[2];
#pragma unroll
  for (int j = 0; j < 2; ++j) {
    wo2[j] = (unsigned)(((w & 3) * 8 + 4 * j + drow) * 8192 + ((dpos ^ (drow << 2)) << 4) - j * 1024);
    xo2[j] = (unsigned)(((w & 3) * 32 + 16 * j + (lane >> 2)) * 1024 + (((lane & 3) ^ drow) << 4) - j * 1024);
  }
  const char* xbase = (const char*)x + (size_t)b * SQ * 1024;

#define ISSUE_K(t, la) dma4(ko[0], ko[1], ko[2], ko[3], kbase + (size_t)(t) * 16384, (la) + (w & 3) * 4096)
#define ISSUE_V(t, la) dma4(vo, vo, vo, vo, vbase + (size_t)(t) * 16384, (la) + (w & 3) * 4096)
#define ROT 1
  const int crot = ROT ? 2 * b + (head & 1) + 8 * ((head >> 1) & 1) : 0;
#define ISSUE_P(c) do { const int cc_ = ((c) + crot) & 15; \
    dma2(wo2[0], wo2[1], wqbase + (size_t)cc_ * 32 * 8192, L0 + P_OFF + ((c) % 8) * 16384 + (w & 3) * 2048); \
    dma2(xo2[0], xo2[1], xbase + cc_ * 64, L0 + P_OFF + ((c) % 8) * 16384 + 8192 + (w & 3) * 2048); } while (0)

#define K0C 0
#define K1C 2
  if (w >= 4) { ISSUE_P(0); ISSUE_P(1); ISSUE_P(2); ISSUE_P(3); ISSUE_P(4); ISSUE_P(5); ISSUE_P(6); }
  else { if (K0C < 0) ISSUE_K(0, L0 + kslot(0)); if (K1C < 0) ISSUE_K(1, L0 + kslot(1)); }

  f32x16 qa[2] = {};
  {
    const int ps = ((p4 & 1) << 1) | (p4 >> 1);
    int wa[2], xa[2];
#pragma unroll
    for (int dd = 0; dd < 2; ++dd) {
      const int dblk = 2 * kh + dd;
      wa[dd] = (8 * hi + q4) * 256 + (((4 * dblk + 2 * g1 + (ps >> 1)) ^ (q4 << 2)) << 4) + 8 * (ps & 1);
      xa[dd] = 8192 + (qg * 32 + r32) * 64 + (((2 * dd + hi) ^ ((r32 >> 2) & 3)) << 4);
    }
#pragma unroll
    for (int c = 0; c < 16; ++c) {
      if (w >= 4) { if (c < 11) WAITVM(24); else if (c < 13) WAITVM(20); else if (c < 15) WAITVM(16); else WAITVM(12); }
      BARRIER();
      if (w >= 4) {
        if (c + 7 < 16) ISSUE_P(c + 7);
        if (c == 9 || c == 11 || c == 13) ISSUE_V((c - 9) / 2, L0 + vslot((c - 9) / 2));
      } else {
        if (c == K0C) ISSUE_K(0, L0 + kslot(0));
        if (c == K1C) ISSUE_K(1, L0 + kslot(1));
        if (c == 10 || c == 12 || c == 14) ISSUE_K((c - 6) / 2, L0 + kslot((c - 6) / 2));
      }
      lds_char* wb = L + P_OFF + (c % 8) * 16384;
#pragma unroll
      for (int s = 0; s < 2; ++s) {
        const bf16x8 xf = lds_r128(wb + xa[s]);
#pragma unroll
        for (int dd = 0; dd < 2; ++dd) {
          s16x4 a0 = lds_tr(wb + wa[dd] + s * 4096);
          s16x4 a1 = lds_tr(wb + wa[dd] + s * 4096 + 1024);
          qa[dd] = MFMA32(cat8(a0, a1), xf, qa[dd]);
        }
      }
    }
  }
  bf16x8 qf[8];
  unsigned f0[9];
  {
    bf16x8 own[4];
#pragma unroll
    for (int dd = 0; dd < 2; ++dd)
#pragma unroll
      for (int sp = 0; sp < 2; ++sp)
      {
        u32x4 t = __builtin_bit_cast(u32x4, pack8(qa[dd][8 * sp + 0], qa[dd][8 * sp + 1], qa[dd][8 * sp + 2], qa[dd][8 * sp + 3],
                                                  qa[dd][8 * sp + 4], qa[dd][8 * sp + 5], qa[dd][8 * sp + 6], qa[dd][8 * sp + 7]));
#pragma unroll
        for (int e = 0; e < 4; ++e) t[e] = cvtpk(__uint_as_float(t[e] << 16) * CSC, __uint_as_float(t[e] & 0xffff0000u) * CSC);
        own[dd * 2 + sp] = __builtin_bit_cast(bf16x8, t);
      }
    BARRIER();
#pragma unroll
    for (int i = 0; i < 4; ++i) *(__attribute__((address_space(3))) bf16x8*)(L + XCH_OFF + w * 4096 + i * 1024 + lane * 16) = own[i];
    if (w < 4) WAITVM(12);
    BARRIER();
    PIN(fv);
#pragma unroll
    for (int i = 0; i < 9; ++i) f0[i] = __builtin_amdgcn_readlane(fv, i);
#pragma unroll
    for (int i = 0; i < 4; ++i) {
      bf16x8 o = lds_r128(L + XCH_OFF + (w ^ 4) * 4096 + i * 1024 + lane * 16);
      if (kh == 0) { qf[i] = own[i]; qf[4 + i] = o; } else { qf[4 + i] = own[i]; qf[i] = o; }
    }
  }

  f32x16 oacc[4] = {};
  const int krow = kh * 32 + r32;
  const int kaddr0 = krow * 256 + ((hi ^ (krow & 15)) << 4);
  int vaddr0[4];
#pragma unroll
  for (int d = 0; d < 4; ++d) vaddr0[d] = (kh * 32 + 4 * hi + q4) * 256 + ((d ^ q4) << 6) + g1 * 32 + p4 * 8;

  bf16x8 kf[8], vf[4][2], pf[2];
  f32x16 sc = {}, sn, negm;
#pragma unroll
  for (int s = 0; s < 8; ++s) kf[s] = lds_r128(L + kslot(0) + (kaddr0 ^ (s << 5)));
#pragma unroll
  for (int s = 0; s < 8; ++s) sc = MFMA32(kf[s], qf[s], sc);
#pragma unroll
  for (int s = 0; s < 8; ++s) kf[s] = lds_r128(L + kslot(1) + (kaddr0 ^ (s << 5)));
  float m;
  {
    float tmax = sc[0];
#pragma unroll
    for (int r = 1; r < 16; ++r) tmax = fmaxf(tmax, sc[r]);
    m = swapmax(tmax);
#pragma unroll
    for (int r = 0; r < 16; ++r) { sc[r] -= m; negm[r] = -m; }
  }
  float l0 = 0.f, l1 = 0.f;
  const char* dbase = w < 4 ? kbase : vbase;
  unsigned dof0 = w < 4 ? ko[0] : vo, dof1 = w < 4 ? ko[1] : vo, dof2 = w < 4 ? ko[2] : vo, dof3 = w < 4 ? ko[3] : vo;
  const int dahead = w < 4 ? 6 : 4;
  int ks = kslot(2), vs = vslot(0), ksp = kslot(1), vsp = vslot(4);

#define RDV(d, s) vf[d][s] = cat8(lds_tr(vb + vaddr0[d] + (s) * 4096), lds_tr(vb + vaddr0[d] + (s) * 4096 + 2048))
#define RDK(s) kf[s] = lds_r128(kb + (kaddr0 ^ ((s) << 5)))
#define EXP(SC, r) SC[r] = __builtin_amdgcn_exp2f(SC[r])
#define CVT(SC, s, e) pw[s][e] = cvtpk(SC[8 * (s) + 2 * (e)], SC[8 * (s) + 2 * (e) + 1])
#define ADD(SC, r) do { l0 = fadd_s(l0, SC[r]); l1 = fadd_s(l1, SC[(r) + 1]); } while (0)
#define PVM(d, s) oacc[d] = MFMA32(vf[d][s], __builtin_bit_cast(bf16x8, pw[s]), oacc[d])
#define QKM(SN, s) SN = MFMA32(kf[s], qf[s], SN)
#define DMA(J, o) do { if (dmode == 1) dmap<J>(o, dptr, dla); else if (dmode == 2) dmapw<J>(o, dptr, dla); } while (0)
#define RESCALE(SN, tmax) do { \
    if (__builtin_amdgcn_ballot_w64(!(tmax <= 8.f)) != 0) { \
      const float mn = m + fmaxf(tmax, 0.f); \
      const float dl = mn - m; \
      const float alpha = __builtin_amdgcn_exp2f(-dl); \
      _Pragma("unroll") for (int d = 0; d < 4; ++d) oacc[d] *= alpha; \
      _Pragma("unroll") for (int r = 0; r < 16; ++r) { SN[r] -= dl; negm[r] = -mn; } \
      l0 *= alpha; l1 *= alpha; m = mn; \
    } } while (0)
#define BODY(SC, SN, HASK) do { \
    lds_char* vb = L + vs; \
    lds_char* kb = L + ks; \
    u32x4 pw[2]; \
    SN = MFMA32(kf[0], qf[0], negm); RDV(0, 0); RDV(1, 0); EXP(SC, 0); EXP(SC, 1); GAP(); \
    QKM(SN, 1); RDV(2, 0); RDV(3, 0); EXP(SC, 2); EXP(SC, 3); CVT(SC, 0, 0); GAP(); \
    QKM(SN, 2); RDV(0, 1); RDV(1, 1); EXP(SC, 4); EXP(SC, 5); CVT(SC, 0, 1); GAP(); \
    QKM(SN, 3); RDV(2, 1); RDV(3, 1); EXP(SC, 6); EXP(SC, 7); CVT(SC, 0, 2); GAP(); \
    QKM(SN, 4); DMA(0, dof0); EXP(SC, 8); EXP(SC, 9); CVT(SC, 0, 3); GAP(); \
    QKM(SN, 5); DMA(1, dof1); EXP(SC, 10); EXP(SC, 11); CVT(SC, 1, 0); GAP(); \
    QKM(SN, 6); DMA(2, dof2); EXP(SC, 12); EXP(SC, 13); CVT(SC, 1, 1); GAP(); \
    QKM(SN, 7); DMA(3, dof3); EXP(SC, 14); EXP(SC, 15); CVT(SC, 1, 2); GAP(); \
    PVM(0, 0); if (HASK) { RDK(0); RDK(1); } CVT(SC, 1, 3); ADD(SC, 0); ADD(SC, 2); GAP(); \
    PVM(1, 0); if (HASK) { RDK(2); RDK(3); } ADD(SC, 4); ADD(SC, 6); GAP(); \
    PVM(2, 0); if (HASK) { RDK(4); RDK(5); } ADD(SC, 8); ADD(SC, 10); GAP(); \
    PVM(3, 0); if (HASK) { RDK(6); RDK(7); } ADD(SC, 12); ADD(SC, 14); GAP(); \
    float t0 = fmaxf(fmaxf(SN[0], SN[1]), SN[2]), t1 = fmaxf(fmaxf(SN[3], SN[4]), SN[5]); \
    PVM(0, 1); t0 = fmaxf(fmaxf(t0, SN[6]), SN[7]); t1 = fmaxf(fmaxf(t1, SN[8]), SN[9]); GAP(); \
    PVM(1, 1); t0 = fmaxf(fmaxf(t0, SN[10]), SN[11]); t1 = fmaxf(fmaxf(t1, SN[12]), SN[13]); GAP(); \
    PVM(2, 1); t0 = fmaxf(fmaxf(t0, SN[14]), SN[15]); t0 = swapmax(fmaxf(t0, t1)); GAP(); \
    PVM(3, 1); GAP(); \
    RESCALE(SN, t0); \
    ksp = ks; vsp = vs; \
    ks = ks == 0 ? 16384 : (ks == 114688 ? 0 : ks + 32768); \
    vs = vs == 131072 ? 147456 : (vs == 147456 ? 32768 : vs + 32768); } while (0)

  {
    WAITVM(8);
    BARRIER();
    if (w < 4) { ISSUE_K(5, L0 + kslot(0)); ISSUE_K(6, L0 + kslot(1)); }
    else { ISSUE_V(3, L0 + vslot(3)); ISSUE_V(4, L0 + vslot(4)); }
    const int dmode = 0; const char* dptr = dbase; const unsigned dla = 0;
    BODY(sc, sn, true);
  }
#define DSETUP(it) \
    const char* dptr = dbase + (size_t)((it) + dahead) * 16384; \
    const unsigned dla = L0 + (w < 4 ? ksp : vsp) + (w & 3) * 4096;
#pragma unroll 1
  for (int it = 1; it < 25; it += 2) {
    { WAITVM(12); BARRIER(); const int dmode = 1; DSETUP(it); BODY(sn, sc, true); }
    { WAITVM(12); BARRIER(); const int dmode = 1; DSETUP(it + 1); BODY(sc, sn, true); }
  }
  const char* wot = (const char*)wo + nblk * 128 + (size_t)(w & 3) * 1048576;
#define WOFFS() do { const int ln_ = lane_id(), dr_ = ln_ >> 3; \
    dof0 = (unsigned)(dr_ * 1024 + (((ln_ & 7) ^ (((dr_ >> 1) & 1) << 2)) << 4)); \
    dof1 = dof0 + 7168; dof2 = dof0 + 524288 - 2048; dof3 = dof0 + 524288 + 8192 - 3072; } while (0)
#define TSETUP(it, ku, vu) \
    asm volatile("" : "+s"(ks), "+s"(vs), "+s"(ksp), "+s"(vsp)); \
    const int dmode = w < 4 ? ((it) + 6 < NT ? 1 : (ku) >= 0 ? 2 : 0) : ((it) + 4 < NT ? 1 : (vu) >= 0 ? 2 : 0); \
    const char* dptr = dmode == 1 ? dbase + (size_t)((it) + dahead) * 16384 : wot + (w < 4 ? (ku) : (vu)) * 16384; \
    const unsigned dla = L0 + (w < 4 ? ksp : vsp) + (w & 3) * 4096;
  { WAITVM(12); BARRIER(); TSETUP(25, -1, -1); BODY(sn, sc, true); }
  if (w < 4) WOFFS();
  { WAITVM(12); BARRIER(); TSETUP(26, 24, -1); BODY(sc, sn, true); }
  { WAITVM(12); BARRIER(); TSETUP(27, 25, -1); BODY(sn, sc, true); }
  if (w >= 4) WOFFS();
  { WAITVM(12); BARRIER(); TSETUP(28, 26, 27); BODY(sc, sn, true); }
  { WAITVM(12); BARRIER(); TSETUP(29, 28, 29); BODY(sn, sc, true); }
  { WAITVM(12); BARRIER(); TSETUP(30, 30, 31); BODY(sc, sn, false); }
  asm volatile("" : "+s"(vs));

  const int ln = lane_id();
  const int r32e = ln & 31, hie = ln >> 5;
  const unsigned wof0 = (unsigned)((ln >> 3) * 1024 + (((ln & 7) ^ ((((ln >> 3) >> 1) & 1) << 2)) << 4)), wof1 = wof0 + 7168;
  const int tw0 = w * 2048 + (4 * hie + ((ln & 15) >> 2)) * 128 + ((((ln & 15) >> 3)) << 6) + ((ln >> 4) & 1) * 32 + (ln & 3) * 8;
  const int tw1 = tw0 ^ 64;
  const char* wob = (const char*)wo + nblk * 128 + (size_t)w * 524288;
#define CELL(i) ((i) == 0 ? 49152 : (i) == 1 ? 81920 : (i) == 2 ? 114688 : (i) == 3 ? 98304 : (i) == 4 ? 0 : \
                 (i) == 5 ? 131072 : (i) == 6 ? 16384 : (i) == 7 ? 147456 : (i) == 8 ? 32768 : 65536)
#define RDW(dst, i) do { dst[0] = cat8(lds_tr(L + CELL(i) + tw0), lds_tr(L + CELL(i) + tw0 + 1024)); \
                         dst[1] = cat8(lds_tr(L + CELL(i) + tw1), lds_tr(L + CELL(i) + tw1 + 1024)); } while (0)
#define DMAW(u, i) dma2(wof0, wof1, wob + (u) * 16384, L0 + CELL(i) + w * 2048)
#define WAITL() asm volatile("s_waitcnt lgkmcnt(0)" ::: "memory")
  bf16x8 A[8][2];
  const unsigned foff = (unsigned)FOFF(ln);
  unsigned pv;
#define LDFLAGS() asm volatile("global_load_dword %0, %1, %2 sc1" : "=v"(pv) : "v"(foff), "s"(flg) : "memory")
  {
    WAITVM(8);
    BARRIER();
    LDFLAGS();
    lds_char* vb = L + vs;
    u32x4 pw[2];
#pragma unroll
    for (int s = 0; s < 2; ++s)
#pragma unroll
      for (int d = 0; d < 4; ++d) RDV(d, s);
    RDW(A[0], 0); RDW(A[1], 1); RDW(A[2], 2); RDW(A[3], 3);
#pragma unroll
    for (int r = 0; r < 16; ++r) EXP(sn, r);
#pragma unroll
    for (int r = 0; r < 16; r += 2) ADD(sn, r);
#pragma unroll
    for (int s = 0; s < 2; ++s)
#pragma unroll
      for (int e = 0; e < 4; ++e) CVT(sn, s, e);
#pragma unroll
    for (int s = 0; s < 2; ++s)
#pragma unroll
      for (int d = 0; d < 4; ++d) PVM(d, s);
  }

  const char* ob = ows + ((size_t)mblk * 256 + w * 32) * 1024;
  unsigned f0v = f0[0];
#pragma unroll
  for (int i = 1; i < 8; ++i) asm volatile("v_writelane_b32 %0, %1, %2" : "+v"(f0v) : "s"(f0[i]), "i"(i));
  unsigned rdy, pend = 7;
  int pos2 = 0;
  const char *wcur, *ocur;
  bf16x8 of[8];
  const unsigned oo = ln * 16;
#define GETRDY() do { PIN(pv); rdy = (unsigned)__builtin_amdgcn_ballot_w64(pv != f0v) & 255u; } while (0)
#define POLL(cond) do { if (!(cond)) { \
      int spins_ = 0; \
      do { __builtin_amdgcn_s_sleep(2); unsigned pq_; \
        asm volatile("global_load_dword %0, %1, %2 sc1\n\ts_waitcnt vmcnt(0)" : "=v"(pq_) : "v"(foff), "s"(flg) : "memory"); \
        rdy = (unsigned)__builtin_amdgcn_ballot_w64(pq_ != f0v) & 255u; \
      } while (!(cond) && ++spins_ < (1 << 18)); } } while (0)
#define CHOOSE(p) do { \
    const int h_ = __builtin_ctz((rdy & pend) | 8u) & 3; pend &= ~(1u << h_); if (h_ == 2) pos2 = (p); \
    wcur = wob + h_ * 131072; ocur = ob + h_ * 8192; asm volatile("" : "+s"(wcur), "+s"(ocur)); LDFLAGS(); } while (0)
#define PUBLISH(v) do { BARRIER(); if (w == 0 && ln == 0) \
    __hip_atomic_store((__attribute__((address_space(1))) unsigned*)(flg + fic * (FSTR / 4)), f0[8] + (v), __ATOMIC_RELAXED, __HIP_MEMORY_SCOPE_AGENT); } while (0)
#define RING(j) ((j) % 10 == 0 ? 9 : (j) % 10 < 6 ? (j) % 10 + 3 : (j) % 10 - 6)
#define LDO(j) asm volatile("global_load_dwordx4 %0, %1, %2 offset:%3 sc1" : "=v"(of[(j) & 7]) : "v"(oo), "s"(ocur + (((j) >> 2) & 1) * 4096), "i"(((j) & 3) * 1024) : "memory")
#define ISS(j) do { if ((j) < 24) dma2(wof0, wof1, wcur + ((j) & 7) * 16384, L0 + CELL(RING(j)) + w * 2048); LDO(j); } while (0)

  const float l = swapadd(l0 + l1);
  {
    float* ml = (float*)(smem + CELL(8));
    if (hie == 0) { ml[w * 512 + r32e] = m; ml[w * 512 + 32 + r32e] = l; }
#pragma unroll
    for (int dd = 0; dd < 2; ++dd)
#pragma unroll
      for (int g = 0; g < 4; ++g) {
        f32x4 v;
        if (kh) { v[0] = oacc[dd][4 * g]; v[1] = oacc[dd][4 * g + 1]; v[2] = oacc[dd][4 * g + 2]; v[3] = oacc[dd][4 * g + 3]; }
        else { v[0] = oacc[2 + dd][4 * g]; v[1] = oacc[2 + dd][4 * g + 1]; v[2] = oacc[2 + dd][4 * g + 2]; v[3] = oacc[2 + dd][4 * g + 3]; }
        *(__attribute__((address_space(3))) f32x4*)(L + CELL(dd * 2 + (g >> 1)) + w * 2048 + (g & 1) * 1024 + ln * 16) = v;
      }
    WAITVM(0);
    BARRIER();
    GETRDY();
    RDW(A[4], 4); RDW(A[5], 5); RDW(A[6], 6); RDW(A[7], 7);
    const float mp = ml[(w ^ 4) * 512 + r32e], lp = ml[(w ^ 4) * 512 + 32 + r32e];
    const float mf = fmaxf(m, mp);
    const float wa = __builtin_amdgcn_exp2f(m - mf), wb = __builtin_amdgcn_exp2f(mp - mf);
    const float inv = 1.f / (wa * l + wb * lp);
    const float fa = wa * inv, fb = wb * inv;
    const char* ost = ows + ((size_t)(b * 4 + qg) * 256 + head * 8 + 4 * kh) * 1024;
#pragma unroll
    for (int dd = 0; dd < 2; ++dd) {
      float o[16];
#pragma unroll
      for (int g = 0; g < 4; ++g) {
        f32x4 v = *(__attribute__((address_space(3))) f32x4*)(L + CELL(dd * 2 + (g >> 1)) + (w ^ 4) * 2048 + (g & 1) * 1024 + ln * 16);
#pragma unroll
        for (int e = 0; e < 4; ++e) {
          const float own = kh ? oacc[2 + dd][4 * g + e] : oacc[dd][4 * g + e];
          o[4 * g + e] = own * fa + v[e] * fb;
        }
      }
#pragma unroll
      for (int sp = 0; sp < 2; ++sp) {
        bf16x8 f = pack8(o[8 * sp + 0], o[8 * sp + 1], o[8 * sp + 2], o[8 * sp + 3], o[8 * sp + 4], o[8 * sp + 5], o[8 * sp + 6], o[8 * sp + 7]);
        asm volatile("global_store_dwordx4 %0, %1, %2 offset:%3 sc1\n\ts_nop 1" :: "v"((unsigned)(ln * 16)), "v"(f), "s"(ost), "i"((dd * 2 + sp) * 1024) : "memory");
      }
    }
    WAITL();
    const bool early = (rdy & 7u) != 0;
#define ST3()
    if (!early) { WAITVM(0); PUBLISH(1u); ST3(); POLL((rdy & 7u) != 0); }
    CHOOSE(0); ISS(0); ISS(1); ISS(2); ISS(3); ISS(4);
    if (early) { WAITVM(16); PUBLISH(1u); ST3(); }
  }
  ISS(5); ISS(6); ISS(7);
  f32x16 pa[3][2] = {}, acc[2];
#define STQ(j)
#define NOPS(u) (((u) < 24 ? 3 : (u) < 32 ? 1 : 0) + ((u) < 24 && (u) % 8 == 0 ? 1 : 0))
#define NAFT(j) (NOPS((j) - 7) + NOPS((j) - 6) + NOPS((j) - 5) + NOPS((j) - 4) + NOPS((j) - 3) + NOPS((j) - 2) + NOPS((j) - 1))
#define STEP(j) do { asm volatile("s_waitcnt vmcnt(%0)" :: "i"(NAFT(j)) : "memory"); PIN(of[(j) & 7]); \
    if ((j) < 32) { bf16x8 t_[2]; RDW(t_, RING((j) - 8)); \
      pa[(j) / 8 - 1][0] = MFMA32(t_[0], of[(j) & 7], pa[(j) / 8 - 1][0]); pa[(j) / 8 - 1][1] = MFMA32(t_[1], of[(j) & 7], pa[(j) / 8 - 1][1]); } \
    else { acc[0] = MFMA32(A[(j) & 7][0], of[(j) & 7], acc[0]); acc[1] = MFMA32(A[(j) & 7][1], of[(j) & 7], acc[1]); } \
    if ((j) == 8 || (j) == 16) { GETRDY(); POLL((rdy & pend) != 0); CHOOSE((j) / 8); } \
    if ((j) == 24) { GETRDY(); POLL((rdy & 8u) != 0); ocur = ob + 3 * 8192; asm volatile("" : "+s"(ocur)); } \
    STQ(j); if ((j) < 32) { WAITL(); ISS(j); } } while (0)
  STEP(8); STEP(9); STEP(10); STEP(11); STEP(12); STEP(13); STEP(14); STEP(15);
  STEP(16); STEP(17); STEP(18); STEP(19); STEP(20); STEP(21); STEP(22); STEP(23);
  STEP(24); STEP(25); STEP(26); STEP(27); STEP(28); STEP(29); STEP(30); STEP(31);
#pragma unroll
  for (int nb = 0; nb < 2; ++nb) {
    if (pos2 == 0) acc[nb] = (pa[1][nb] + pa[2][nb]) + pa[0][nb];
    else if (pos2 == 1) acc[nb] = (pa[0][nb] + pa[2][nb]) + pa[1][nb];
    else acc[nb] = (pa[0][nb] + pa[1][nb]) + pa[2][nb];
  }
  STEP(32); STEP(33); STEP(34); STEP(35); STEP(36); STEP(37); STEP(38); STEP(39);
#pragma unroll
  for (int nb = 0; nb < 2; ++nb)
#pragma unroll
    for (int g = 0; g < 4; ++g) {
      f32x4 v = {acc[nb][4 * g], acc[nb][4 * g + 1], acc[nb][4 * g + 2], acc[nb][4 * g + 3]};
      *(__attribute__((address_space(3))) f32x4*)(L + CELL(nb * 2 + (g >> 1)) + w * 2048 + (g & 1) * 1024 + ln * 16) = v;
    }
  POLL((rdy & 0xF0u) == 0xF0u);
  PUBLISH(2u);
  {
    const int cb = (w >> 1) == 0 ? CELL(0) : (w >> 1) == 1 ? CELL(1) : (w >> 1) == 2 ? CELL(2) : CELL(3);
    f32x4 s = {0.f, 0.f, 0.f, 0.f};
#pragma unroll
    for (int ww = 0; ww < 8; ++ww) s += *(__attribute__((address_space(3))) f32x4*)(L + cb + ww * 2048 + (w & 1) * 1024 + ln * 16);
    const int nb = w >> 2, g = w & 3;
    u32x2 pk = {cvtpk(s[0], s[1]), cvtpk(s[2], s[3])};
    *(u32x2*)((char*)out + ((size_t)(mblk * 32 + r32e) * 512 + nblk * 64 + nb * 32 + 8 * g + 4 * hie) * 2) = pk;
  }
}

extern "C" void kernel_launch(void* const* d_in, const int* in_sizes, int n_in, void* d_out, int out_size, void* d_ws, size_t ws_size,
                              hipStream_t stream) {
  const __hip_bfloat16* x = (const __hip_bfloat16*)d_in[0];
  const __hip_bfloat16* kc = (const __hip_bfloat16*)d_in[1];
  const __hip_bfloat16* vc = (const __hip_bfloat16*)d_in[2];
  const __hip_bfloat16* wq = (const __hip_bfloat16*)d_in[3];
  const __hip_bfloat16* wo = (const __hip_bfloat16*)d_in[4];
  __hip_bfloat16* out = (__hip_bfloat16*)d_out;
  (void)hipFuncSetAttribute((const void*)fused_kernel, hipFuncAttributeMaxDynamicSharedMemorySize, LDS1);
  fused_kernel<<<256, 512, LDS1, stream>>>(x, kc, vc, wq, wo, (char*)d_ws, (unsigned*)((char*)d_ws + (16u << 20)), out);
}
```

```cpp
#include <hip/hip_runtime.h>
#include <hip/hip_bf16.h>
#include <stdint.h>

typedef __attribute__((ext_vector_type(8))) short bf16x8;
typedef __attribute__((ext_vector_type(4))) short s16x4;
typedef __attribute__((ext_vector_type(16))) float f32x16;
typedef __attribute__((ext_vector_type(4))) float f32x4;
typedef __attribute__((ext_vector_type(4))) unsigned u32x4;
typedef __attribute__((ext_vector_type(2))) unsigned u32x2;
typedef __attribute__((address_space(3))) char lds_char;
typedef short v4i16_t __attribute__((ext_vector_type(4)));

constexpr int NB = 8, SQ = 128, DM = 512, NH = 32, SK = 2048, DH = 128;
constexpr int NT = SK / 64;
constexpr int LDS1 = 163840;
constexpr int P_OFF = 32768;
constexpr int XCH_OFF = 131072;
constexpr int LDS2 = 163840;
constexpr float CSC = 0.08838834764831845f * 1.4426950408889634f;

#define MFMA32(a, b, c) __builtin_amdgcn_mfma_f32_32x32x16_bf16(a, b, c, 0, 0, 0)

typedef __attribute__((ext_vector_type(2))) float f32x2;
typedef __attribute__((ext_vector_type(2))) __bf16 bf16x2_t;
__device__ __forceinline__ unsigned cvtpk(float lo, float hi) {
  f32x2 v = {lo, hi};
  return __builtin_bit_cast(unsigned, __builtin_convertvector(v, bf16x2_t));
}
__device__ __forceinline__ bf16x8 pack8(float a0, float a1, float a2, float a3, float a4, float a5, float a6, float a7) {
  u32x4 w = {cvtpk(a0, a1), cvtpk(a2, a3), cvtpk(a4, a5), cvtpk(a6, a7)};
  return __builtin_bit_cast(bf16x8, w);
}
__device__ __forceinline__ bf16x8 lds_r128(lds_char* p) { return *(__attribute__((address_space(3))) bf16x8*)p; }
__device__ __forceinline__ s16x4 lds_tr(lds_char* p) {
  return __builtin_bit_cast(s16x4, __builtin_amdgcn_ds_read_tr16_b64_v4i16((__attribute__((address_space(3))) v4i16_t*)p));
}
__device__ __forceinline__ bf16x8 cat8(s16x4 a, s16x4 b) { return __builtin_shufflevector(a, b, 0, 1, 2, 3, 4, 5, 6, 7); }

__device__ __forceinline__ void dma4(unsigned o0, unsigned o1, unsigned o2, unsigned o3, const void* sbase, unsigned ldsaddr) {
  unsigned keep;
  asm volatile(
      "s_mov_b32 %0, m0\n\t"
      "s_mov_b32 m0, %6\n\t"
      "s_nop 0\n\t"
      "global_load_lds_dwordx4 %1, %5 nt\n\t"
      "global_load_lds_dwordx4 %2, %5 offset:1024 nt\n\t"
      "global_load_lds_dwordx4 %3, %5 offset:2048 nt\n\t"
      "global_load_lds_dwordx4 %4, %5 offset:3072 nt\n\t"
      "s_mov_b32 m0, %0"
      : "=&s"(keep) : "v"(o0), "v"(o1), "v"(o2), "v"(o3), "s"(sbase), "s"(ldsaddr) : "memory");
}
__device__ __forceinline__ void dma2(unsigned o0, unsigned o1, const void* sbase, unsigned ldsaddr) {
  unsigned keep;
  asm volatile(
      "s_mov_b32 %0, m0\n\t"
      "s_mov_b32 m0, %4\n\t"
      "s_nop 0\n\t"
      "global_load_lds_dwordx4 %1, %3\n\t"
      "global_load_lds_dwordx4 %2, %3 offset:1024\n\t"
      "s_mov_b32 m0, %0"
      : "=&s"(keep) : "v"(o0), "v"(o1), "s"(sbase), "s"(ldsaddr) : "memory");
}
__device__ __forceinline__ void dma1s(unsigned o0, const void* sbase, unsigned ldsaddr) {
  unsigned keep;
  asm volatile(
      "s_mov_b32 %0, m0\n\t"
      "s_mov_b32 m0, %3\n\t"
      "s_nop 0\n\t"
      "global_load_lds_dwordx4 %1, %2 sc1\n\t"
      "s_mov_b32 m0, %0"
      : "=&s"(keep) : "v"(o0), "s"(sbase), "s"(ldsaddr) : "memory");
}
template <int J> __device__ __forceinline__ void dmap(unsigned o, const void* sbase, unsigned ldsaddr) {
  unsigned keep;
  asm volatile("s_mov_b32 %0, m0\n\ts_mov_b32 m0, %3\n\ts_nop 0\n\tglobal_load_lds_dwordx4 %1, %2 offset:%4 nt\n\ts_mov_b32 m0, %0"
               : "=&s"(keep) : "v"(o), "s"(sbase), "s"(ldsaddr), "i"(J * 1024) : "memory");
}
template <int J> __device__ __forceinline__ void dmapw(unsigned o, const void* sbase, unsigned ldsaddr) {
  unsigned keep;
  asm volatile("s_mov_b32 %0, m0\n\ts_mov_b32 m0, %3\n\ts_nop 0\n\tglobal_load_lds_dwordx4 %1, %2 offset:%4\n\ts_mov_b32 m0, %0"
               : "=&s"(keep) : "v"(o), "s"(sbase), "s"(ldsaddr), "i"(J * 1024) : "memory");
}
__device__ __forceinline__ int lane_id() {
  int lane;
  asm volatile("v_mbcnt_lo_u32_b32 %0, -1, 0\n\tv_mbcnt_hi_u32_b32 %0, -1, %0" : "=v"(lane));
  return lane;
}
__device__ __forceinline__ float fadd_s(float a, float b) { float r; asm("v_add_f32_e32 %0, %1, %2" : "=v"(r) : "v"(a), "v"(b)); return r; }
#define GAP() __builtin_amdgcn_sched_barrier(0)
__device__ __forceinline__ int kslot(int i) { return i == 0 ? 0 : i * 32768 - 16384; }
__device__ __forceinline__ int vslot(int i) { return i == 4 ? 147456 : i * 32768 + 32768; }
#define WAITVM(n) asm volatile("s_waitcnt vmcnt(" #n ")" ::: "memory")
#define WAITVML(n) asm volatile("s_waitcnt vmcnt(" #n ") lgkmcnt(0)" ::: "memory")
#define BARRIER() asm volatile("s_waitcnt lgkmcnt(0)\n\ts_barrier" ::: "memory")
#define PIN(x) asm volatile("" : "+v"(x))

__device__ __forceinline__ float swapmax(float v) {
  auto rr = __builtin_amdgcn_permlane32_swap(__float_as_uint(v), __float_as_uint(v), false, false);
  return fmaxf(__uint_as_float(rr[0]), __uint_as_float(rr[1]));
}
__device__ __forceinline__ float swapadd(float v) {
  auto rr = __builtin_amdgcn_permlane32_swap(__float_as_uint(v), __float_as_uint(v), false, false);
  return __uint_as_float(rr[0]) + __uint_as_float(rr[1]);
}

__global__ __launch_bounds__(512, 2) void fused_kernel(const __hip_bfloat16* __restrict__ x, const __hip_bfloat16* __restrict__ kc,
                                                       const __hip_bfloat16* __restrict__ vc, const __hip_bfloat16* __restrict__ wq,
                                                       const __hip_bfloat16* __restrict__ wo, char* ows, unsigned* flg,
                                                       __hip_bfloat16* __restrict__ out) {
  extern __shared__ __attribute__((aligned(16))) char smem[];
  lds_char* L = (lds_char*)smem;
  const unsigned L0 = (unsigned)(uintptr_t)smem;
  const int tid = threadIdx.x, lane = tid & 63;
  const int w = __builtin_amdgcn_readfirstlane(tid >> 6);
  const int qg = w & 3, kh = w >> 2;
  const int r32 = lane & 31, hi = lane >> 5;
  const int q4 = (lane & 15) >> 2, p4 = lane & 3, g1 = (lane >> 4) & 1;
  const int bid = blockIdx.x;
  const int head = 4 * (bid & 7) + ((bid >> 3) & 3), b = bid >> 5;
  const char* kbase = (const char*)kc + ((size_t)(b * NH + head) * SK) * 256;
  const char* vbase = (const char*)vc + ((size_t)(b * NH + head) * SK) * 256;
  const char* wqbase = (const char*)wq + head * 256;
#define FSTR 128
  const int xq = bid & 7, jq = (bid >> 3) & 3;
  int fla = ((b * 8 + xq) * 4 + qg) * FSTR, flb = (256 + (b * 4 + jq) * 8 + (xq >> 1) + 4 * kh) * FSTR - 32, flo = (256 + (b * 4 + jq) * 8 + w) * FSTR + xq * 4;
  asm volatile("" : "+s"(fla), "+s"(flb), "+s"(flo));
  unsigned fv;
  asm volatile("global_load_dword %0, %1, %2 sc1" : "=v"(fv) : "v"((unsigned)(lane < 16 ? (lane < 8 ? fla : flb) + lane * 4 : flo)), "s"(flg) : "memory");

  const int drow = lane >> 4, dpos = lane & 15;
  unsigned ko[4];
#pragma unroll
  for (int j = 0; j < 4; ++j) ko[j] = (unsigned)(((w & 3) * 16 + drow) * 256 + ((dpos ^ (4 * j + drow)) << 4));
  const unsigned vo = (unsigned)(((w & 3) * 16 + drow) * 256 + ((dpos ^ (drow << 2)) << 4));
  unsigned wo2[2], xo2[2];
#pragma unroll
  for (int j = 0; j < 2; ++j) {
    wo2[j] = (unsigned)(((w & 3) * 8 + 4 * j + drow) * 8192 + ((dpos ^ (drow << 2)) << 4) - j * 1024);
    xo2[j] = (unsigned)(((w & 3) * 32 + 16 * j + (lane >> 2)) * 1024 + (((lane & 3) ^ drow) << 4) - j * 1024);
  }
  const char* xbase = (const char*)x + (size_t)b * SQ * 1024;

#define ISSUE_K(t, la) dma4(ko[0], ko[1], ko[2], ko[3], kbase + (size_t)(t) * 16384, (la) + (w & 3) * 4096)
#define ISSUE_V(t, la) dma4(vo, vo, vo, vo, vbase + (size_t)(t) * 16384, (la) + (w & 3) * 4096)
#define ROT 1
  const int crot = ROT ? 2 * b + (head & 1) + 8 * ((head >> 1) & 1) : 0;
#define ISSUE_P(c) do { const int cc_ = ((c) + crot) & 15; \
    dma2(wo2[0], wo2[1], wqbase + (size_t)cc_ * 32 * 8192, L0 + P_OFF + ((c) % 8) * 16384 + (w & 3) * 2048); \
    dma2(xo2[0], xo2[1], xbase + cc_ * 64, L0 + P_OFF + ((c) % 8) * 16384 + 8192 + (w & 3) * 2048); } while (0)

#define K0C 0
#define K1C 2
  if (w >= 4) { ISSUE_P(0); ISSUE_P(1); ISSUE_P(2); ISSUE_P(3); ISSUE_P(4); ISSUE_P(5); ISSUE_P(6); }
  else { if (K0C < 0) ISSUE_K(0, L0 + kslot(0)); if (K1C < 0) ISSUE_K(1, L0 + kslot(1)); }

  f32x16 qa[2] = {};
  {
    const int ps = ((p4 & 1) << 1) | (p4 >> 1);
    int wa[2], xa[2];
#pragma unroll
    for (int dd = 0; dd < 2; ++dd) {
      const int dblk = 2 * kh + dd;
      wa[dd] = (8 * hi + q4) * 256 + (((4 * dblk + 2 * g1 + (ps >> 1)) ^ (q4 << 2)) << 4) + 8 * (ps & 1);
      xa[dd] = 8192 + (qg * 32 + r32) * 64 + (((2 * dd + hi) ^ ((r32 >> 2) & 3)) << 4);
    }
#pragma unroll
    for (int c = 0; c < 16; ++c) {
      if (w >= 4) { if (c < 11) WAITVM(24); else if (c < 13) WAITVM(20); else if (c < 15) WAITVM(16); else WAITVM(12); }
      BARRIER();
      if (w >= 4) {
        if (c + 7 < 16) ISSUE_P(c + 7);
        if (c == 9 || c == 11 || c == 13) ISSUE_V((c - 9) / 2, L0 + vslot((c - 9) / 2));
      } else {
        if (c == K0C) ISSUE_K(0, L0 + kslot(0));
        if (c == K1C) ISSUE_K(1, L0 + kslot(1));
        if (c == 10 || c == 12 || c == 14) ISSUE_K((c - 6) / 2, L0 + kslot((c - 6) / 2));
      }
      lds_char* wb = L + P_OFF + (c % 8) * 16384;
#pragma unroll
      for (int s = 0; s < 2; ++s) {
        const bf16x8 xf = lds_r128(wb + xa[s]);
#pragma unroll
        for (int dd = 0; dd < 2; ++dd) {
          s16x4 a0 = lds_tr(wb + wa[dd] + s * 4096);
          s16x4 a1 = lds_tr(wb + wa[dd] + s * 4096 + 1024);
          qa[dd] = MFMA32(cat8(a0, a1), xf, qa[dd]);
        }
      }
    }
  }
  bf16x8 qf[8];
  unsigned f0[17], f0a;
  {
    bf16x8 own[4];
#pragma unroll
    for (int dd = 0; dd < 2; ++dd)
#pragma unroll
      for (int sp = 0; sp < 2; ++sp)
      {
        u32x4 t = __builtin_bit_cast(u32x4, pack8(qa[dd][8 * sp + 0], qa[dd][8 * sp + 1], qa[dd][8 * sp + 2], qa[dd][8 * sp + 3],
                                                  qa[dd][8 * sp + 4], qa[dd][8 * sp + 5], qa[dd][8 * sp + 6], qa[dd][8 * sp + 7]));
#pragma unroll
        for (int e = 0; e < 4; ++e) t[e] = cvtpk(__uint_as_float(t[e] << 16) * CSC, __uint_as_float(t[e] & 0xffff0000u) * CSC);
        own[dd * 2 + sp] = __builtin_bit_cast(bf16x8, t);
      }
    BARRIER();
#pragma unroll
    for (int i = 0; i < 4; ++i) *(__attribute__((address_space(3))) bf16x8*)(L + XCH_OFF + w * 4096 + i * 1024 + lane * 16) = own[i];
    if (w < 4) WAITVM(12);
    BARRIER();
    PIN(fv);
#pragma unroll
    for (int i = 0; i < 17; ++i) f0[i] = __builtin_amdgcn_readlane(fv, i);
    f0a = __builtin_amdgcn_readlane(fv, 2 * jq + kh);
#pragma unroll
    for (int i = 0; i < 4; ++i) {
      bf16x8 o = lds_r128(L + XCH_OFF + (w ^ 4) * 4096 + i * 1024 + lane * 16);
      if (kh == 0) { qf[i] = own[i]; qf[4 + i] = o; } else { qf[4 + i] = own[i]; qf[i] = o; }
    }
  }

  f32x16 oacc[4] = {};
  const int krow = kh * 32 + r32;
  const int kaddr0 = krow * 256 + ((hi ^ (krow & 15)) << 4);
  int vaddr0[4];
#pragma unroll
  for (int d = 0; d < 4; ++d) vaddr0[d] = (kh * 32 + 4 * hi + q4) * 256 + ((d ^ q4) << 6) + g1 * 32 + p4 * 8;

#define CL(c) ((c) == 0 ? 49152 : (c) == 1 ? 81920 : (c) == 2 ? 114688 : (c) == 3 ? 98304 : (c) == 4 ? 0 : (c) == 5 ? 131072 : (c) == 6 ? 16384 : 65536)
  bf16x8 kf[8], vf[4][2], W0[8][2];
  int wa[2];
  const int cw0[2] = {kh ? CL(1) : CL(0), kh ? CL(0) : CL(1)};
  f32x16 sc = {}, sn, negm;
#pragma unroll
  for (int s = 0; s < 8; ++s) kf[s] = lds_r128(L + kslot(0) + (kaddr0 ^ (s << 5)));
#pragma unroll
  for (int s = 0; s < 8; ++s) sc = MFMA32(kf[s], qf[s], sc);
#pragma unroll
  for (int s = 0; s < 8; ++s) kf[s] = lds_r128(L + kslot(1) + (kaddr0 ^ (s << 5)));
  float m;
  {
    float tmax = sc[0];
#pragma unroll
    for (int r = 1; r < 16; ++r) tmax = fmaxf(tmax, sc[r]);
    m = swapmax(tmax);
#pragma unroll
    for (int r = 0; r < 16; ++r) { sc[r] -= m; negm[r] = -m; }
  }
  float l0 = 0.f, l1 = 0.f;
  const char* dbase = w < 4 ? kbase : vbase;
  unsigned dof0 = w < 4 ? ko[0] : vo, dof1 = w < 4 ? ko[1] : vo, dof2 = w < 4 ? ko[2] : vo, dof3 = w < 4 ? ko[3] : vo;
  const int dahead = w < 4 ? 6 : 4;
  int ks = kslot(2), vs = vslot(0), ksp = kslot(1), vsp = vslot(4);

#define RDV(d, s) vf[d][s] = cat8(lds_tr(vb + vaddr0[d] + (s) * 4096), lds_tr(vb + vaddr0[d] + (s) * 4096 + 2048))
#define RDK(s) kf[s] = lds_r128(kb + (kaddr0 ^ ((s) << 5)))
#define RDW(c, nb, s) cat8(lds_tr(L + (c) + wa[nb] + (s) * 4096), lds_tr(L + (c) + wa[nb] + (s) * 4096 + 2048))
#define KRD(HASK, i) do { if (HASK) { RDK(2 * (i)); RDK(2 * (i) + 1); } else { \
    W0[2 * (i)][0] = RDW(cw0[(i) >> 1], 0, (2 * (i)) & 3); W0[2 * (i)][1] = RDW(cw0[(i) >> 1], 1, (2 * (i)) & 3); \
    W0[2 * (i) + 1][0] = RDW(cw0[(i) >> 1], 0, (2 * (i) + 1) & 3); W0[2 * (i) + 1][1] = RDW(cw0[(i) >> 1], 1, (2 * (i) + 1) & 3); } } while (0)
#define EXP(SC, r) SC[r] = __builtin_amdgcn_exp2f(SC[r])
#define CVT(SC, s, e) pw[s][e] = cvtpk(SC[8 * (s) + 2 * (e)], SC[8 * (s) + 2 * (e) + 1])
#define ADD(SC, r) do { l0 = fadd_s(l0, SC[r]); l1 = fadd_s(l1, SC[(r) + 1]); } while (0)
#define PVM(d, s) oacc[d] = MFMA32(vf[d][s], __builtin_bit_cast(bf16x8, pw[s]), oacc[d])
#define QKM(SN, s) SN = MFMA32(kf[s], qf[s], SN)
#define DMA(J, o) do { if (dmode == 1) dmap<J>(o, dptr, dla); else if (dmode == 2) dmapw<J>(o, dptr, dla); } while (0)
#define RESCALE(SN, tmax) do { \
    if (__builtin_amdgcn_ballot_w64(!(tmax <= 8.f)) != 0) { \
      const float mn = m + fmaxf(tmax, 0.f); \
      const float dl = mn - m; \
      const float alpha = __builtin_amdgcn_exp2f(-dl); \
      _Pragma("unroll") for (int d = 0; d < 4; ++d) oacc[d] *= alpha; \
      _Pragma("unroll") for (int r = 0; r < 16; ++r) { SN[r] -= dl; negm[r] = -mn; } \
      l0 *= alpha; l1 *= alpha; m = mn; \
    } } while (0)
#define BODY(SC, SN, HASK) do { \
    lds_char* vb = L + vs; \
    lds_char* kb = L + ks; \
    u32x4 pw[2]; \
    SN = MFMA32(kf[0], qf[0], negm); RDV(0, 0); RDV(1, 0); EXP(SC, 0); EXP(SC, 1); GAP(); \
    QKM(SN, 1); RDV(2, 0); RDV(3, 0); EXP(SC, 2); EXP(SC, 3); CVT(SC, 0, 0); GAP(); \
    QKM(SN, 2); RDV(0, 1); RDV(1, 1); EXP(SC, 4); EXP(SC, 5); CVT(SC, 0, 1); GAP(); \
    QKM(SN, 3); RDV(2, 1); RDV(3, 1); EXP(SC, 6); EXP(SC, 7); CVT(SC, 0, 2); GAP(); \
    QKM(SN, 4); DMA(0, dof0); EXP(SC, 8); EXP(SC, 9); CVT(SC, 0, 3); GAP(); \
    QKM(SN, 5); DMA(1, dof1); EXP(SC, 10); EXP(SC, 11); CVT(SC, 1, 0); GAP(); \
    QKM(SN, 6); DMA(2, dof2); EXP(SC, 12); EXP(SC, 13); CVT(SC, 1, 1); GAP(); \
    QKM(SN, 7); DMA(3, dof3); EXP(SC, 14); EXP(SC, 15); CVT(SC, 1, 2); GAP(); \
    PVM(0, 0); KRD(HASK, 0); CVT(SC, 1, 3); ADD(SC, 0); ADD(SC, 2); GAP(); \
    PVM(1, 0); KRD(HASK, 1); ADD(SC, 4); ADD(SC, 6); GAP(); \
    PVM(2, 0); KRD(HASK, 2); ADD(SC, 8); ADD(SC, 10); GAP(); \
    PVM(3, 0); KRD(HASK, 3); ADD(SC, 12); ADD(SC, 14); GAP(); \
    float t0 = fmaxf(fmaxf(SN[0], SN[1]), SN[2]), t1 = fmaxf(fmaxf(SN[3], SN[4]), SN[5]); \
    PVM(0, 1); t0 = fmaxf(fmaxf(t0, SN[6]), SN[7]); t1 = fmaxf(fmaxf(t1, SN[8]), SN[9]); GAP(); \
    PVM(1, 1); t0 = fmaxf(fmaxf(t0, SN[10]), SN[11]); t1 = fmaxf(fmaxf(t1, SN[12]), SN[13]); GAP(); \
    PVM(2, 1); t0 = fmaxf(fmaxf(t0, SN[14]), SN[15]); t0 = swapmax(fmaxf(t0, t1)); GAP(); \
    PVM(3, 1); GAP(); \
    RESCALE(SN, t0); \
    ksp = ks; vsp = vs; \
    ks = ks == 0 ? 16384 : (ks == 114688 ? 0 : ks + 32768); \
    vs = vs == 131072 ? 147456 : (vs == 147456 ? 32768 : vs + 32768); } while (0)

  {
    WAITVM(8);
    BARRIER();
    if (w < 4) { ISSUE_K(5, L0 + kslot(0)); ISSUE_K(6, L0 + kslot(1)); }
    else { ISSUE_V(3, L0 + vslot(3)); ISSUE_V(4, L0 + vslot(4)); }
    const int dmode = 0; const char* dptr = dbase; const unsigned dla = 0;
    BODY(sc, sn, true);
  }
#define DSETUP(it) \
    const char* dptr = dbase + (size_t)((it) + dahead) * 16384; \
    const unsigned dla = L0 + (w < 4 ? ksp : vsp) + (w & 3) * 4096;
#pragma unroll 1
  for (int it = 1; it < 25; it += 2) {
    { WAITVM(12); BARRIER(); const int dmode = 1; DSETUP(it); BODY(sn, sc, true); }
    { WAITVM(12); BARRIER(); const int dmode = 1; DSETUP(it + 1); BODY(sc, sn, true); }
  }
  const char* wot = (const char*)wo + (size_t)xq * 524288 + jq * 256;
#define WOC(c) (wot + (((jq + ((c) >> 1)) & 3) * 128 + ((c) & 1) * 64) * 1024)
#define WOFFS() do { const int ln_ = lane_id(), dr_ = ln_ >> 4; \
    dof0 = (unsigned)(((w & 3) * 16 + dr_) * 1024 + (((ln_ & 15) ^ (dr_ << 2)) << 4)); \
    dof1 = dof0 + 3072; dof2 = dof0 + 6144; dof3 = dof0 + 9216; } while (0)
#define TSETUP(it, ku, vu) \
    asm volatile("" : "+s"(ks), "+s"(vs), "+s"(ksp), "+s"(vsp)); \
    const int dmode = w < 4 ? ((it) + 6 < NT ? 1 : (ku) >= 0 ? 2 : 0) : ((it) + 4 < NT ? 1 : (vu) >= 0 ? 2 : 0); \
    const char* dptr = dmode == 1 ? dbase + (size_t)((it) + dahead) * 16384 : (w < 4 ? WOC((ku) & 7) : WOC((vu) & 7)); \
    const unsigned dla = L0 + (w < 4 ? ksp : vsp) + (w & 3) * 4096;
  { WAITVM(12); BARRIER(); TSETUP(25, -1, -1); BODY(sn, sc, true); }
  if (w < 4) WOFFS();
  { WAITVM(12); BARRIER(); TSETUP(26, 0, -1); BODY(sc, sn, true); }
  { WAITVM(12); BARRIER(); TSETUP(27, 1, -1); BODY(sn, sc, true); }
  if (w >= 4) WOFFS();
  { WAITVM(12); BARRIER(); TSETUP(28, 2, 3); BODY(sc, sn, true); }
  { WAITVM(12); BARRIER(); TSETUP(29, 4, 5); BODY(sn, sc, true); }
  wa[0] = kh ? vaddr0[2] - 8192 : vaddr0[0]; wa[1] = kh ? vaddr0[3] - 8192 : vaddr0[1];
  { if (w < 4) WAITVM(8); else WAITVM(12); BARRIER(); TSETUP(30, -1, -1); BODY(sc, sn, false); }
  asm volatile("" : "+s"(vs));

  const int ln = lane_id();
  const int r32e = ln & 31, hie = ln >> 5;
#define WAITL() asm volatile("s_waitcnt lgkmcnt(0)" ::: "memory")
  unsigned foff = (unsigned)(fla + (ln & 7) * 4);
  unsigned pv;
#define LDFLAGS() asm volatile("global_load_dword %0, %1, %2 sc1" : "=v"(pv) : "v"(foff), "s"(flg) : "memory")
  {
    WAITVM(8);
    BARRIER();
    lds_char* vb = L + vs;
    u32x4 pw[2];
#pragma unroll
    for (int s = 0; s < 2; ++s)
#pragma unroll
      for (int d = 0; d < 4; ++d) RDV(d, s);
#pragma unroll
    for (int r = 0; r < 16; ++r) EXP(sn, r);
#pragma unroll
    for (int r = 0; r < 16; r += 2) ADD(sn, r);
#pragma unroll
    for (int s = 0; s < 2; ++s)
#pragma unroll
      for (int e = 0; e < 4; ++e) CVT(sn, s, e);
#pragma unroll
    for (int s = 0; s < 2; ++s)
#pragma unroll
      for (int d = 0; d < 4; ++d) PVM(d, s);
  }

  unsigned f0v = f0[0];
#pragma unroll
  for (int i = 1; i < 16; ++i) asm volatile("v_writelane_b32 %0, %1, %2" : "+v"(f0v) : "s"(f0[i]), "i"(i));
  unsigned rdy;
  const unsigned oo = ln * 16;
#define RDYOF(v) do { const unsigned c_ = (unsigned)__builtin_amdgcn_ballot_w64((v) != f0v); rdy = (c_ & (c_ >> 1) & 0x55u) | (c_ & 0xFF00u); } while (0)
#define POLL(cond) do { \
      int spins_ = 0; \
      do { __builtin_amdgcn_s_sleep(1); unsigned pq_; \
        asm volatile("global_load_dword %0, %1, %2 sc1\n\ts_waitcnt vmcnt(0)" : "=v"(pq_) : "v"(foff), "s"(flg) : "memory"); \
        RDYOF(pq_); \
      } while (!(cond) && ++spins_ < (1 << 18)); } while (0)
#define SETFLAG(o, v) __hip_atomic_store((__attribute__((address_space(1))) unsigned*)((char*)flg + (o)), (v), __ATOMIC_RELAXED, __HIP_MEMORY_SCOPE_AGENT)
  const int flao = fla + (2 * jq + kh) * 4;
#define XBOX(ww) ((((ww) >> 1) == 0 ? CL(0) : ((ww) >> 1) == 1 ? CL(1) : ((ww) >> 1) == 2 ? 32768 : 147456) + ((ww) & 1) * 8192)

  const float l = swapadd(l0 + l1);
  f32x16 acc[2] = {};
  const int xo = XBOX(w ^ 4) + ln * 16, xi = XBOX(w) + ln * 16;
  {
    float* ml = (float*)(smem + CL(6));
    if (hie == 0) { ml[(w ^ 4) * 512 + r32e] = m; ml[(w ^ 4) * 512 + 32 + r32e] = l; }
#pragma unroll
    for (int dd = 0; dd < 2; ++dd)
#pragma unroll
      for (int g = 0; g < 4; ++g) {
        f32x4 v;
        if (kh) { v[0] = oacc[dd][4 * g]; v[1] = oacc[dd][4 * g + 1]; v[2] = oacc[dd][4 * g + 2]; v[3] = oacc[dd][4 * g + 3]; }
        else { v[0] = oacc[2 + dd][4 * g]; v[1] = oacc[2 + dd][4 * g + 1]; v[2] = oacc[2 + dd][4 * g + 2]; v[3] = oacc[2 + dd][4 * g + 3]; }
        *(__attribute__((address_space(3))) f32x4*)(L + xo + (dd * 4 + g) * 1024) = v;
      }
    BARRIER();
    LDFLAGS();
    const float mp = ml[w * 512 + r32e], lp = ml[w * 512 + 32 + r32e];
    const float mf = fmaxf(m, mp);
    const float wa_ = __builtin_amdgcn_exp2f(m - mf), wb_ = __builtin_amdgcn_exp2f(mp - mf);
    const float inv = 1.f / (wa_ * l + wb_ * lp);
    const float fa = wa_ * inv, fb = wb_ * inv;
    const char* ost = ows + ((size_t)(b * 4 + qg) * 256 + head * 8 + 4 * kh) * 1024;
    bf16x8 own[4];
#pragma unroll
    for (int dd = 0; dd < 2; ++dd) {
      float o[16];
#pragma unroll
      for (int g = 0; g < 4; ++g) {
        f32x4 v = *(__attribute__((address_space(3))) f32x4*)(L + xi + (dd * 4 + g) * 1024);
#pragma unroll
        for (int e = 0; e < 4; ++e) {
          const float ow = kh ? oacc[2 + dd][4 * g + e] : oacc[dd][4 * g + e];
          o[4 * g + e] = ow * fa + v[e] * fb;
        }
      }
#pragma unroll
      for (int sp = 0; sp < 2; ++sp) {
        own[dd * 2 + sp] = pack8(o[8 * sp + 0], o[8 * sp + 1], o[8 * sp + 2], o[8 * sp + 3], o[8 * sp + 4], o[8 * sp + 5], o[8 * sp + 6], o[8 * sp + 7]);
        asm volatile("global_store_dwordx4 %0, %1, %2 offset:%3 sc1\n\ts_nop 1" :: "v"(oo), "v"(own[dd * 2 + sp]), "s"(ost), "i"((dd * 2 + sp) * 1024) : "memory");
      }
    }
    WAITL();
    {
      const unsigned d0 = (unsigned)((w * 8 + (ln >> 4)) * 1024 + (((ln & 15) ^ ((ln >> 4) << 2)) << 4));
      dma2(d0, d0 + 3072, WOC(6), L0 + CL(6) + w * 2048);
      dma2(d0, d0 + 3072, WOC(7), L0 + CL(7) + w * 2048);
    }
#pragma unroll
    for (int i = 0; i < 4; ++i) *(__attribute__((address_space(3))) bf16x8*)(L + xi + i * 1024) = own[i];
#pragma unroll
    for (int i = 0; i < 4; ++i)
#pragma unroll
      for (int nb = 0; nb < 2; ++nb) acc[nb] = MFMA32(W0[i][nb], own[i], acc[nb]);
    WAITVM(8);
    PIN(pv); RDYOF(pv);
  }
  {
    const char* ob = ows + ((size_t)(b * 4 + qg) * 256 + xq * 32) * 1024;
    unsigned pend = 0x55u & ~(1u << (2 * jq));
    int pos3 = 0, cb0[3], cb1[3], done = 0;
    bf16x8 of[3][8];
    f32x16 pa[3][2] = {};
#define ST3()
#define PUB(n) do { if (done == 0) { done = 1; asm volatile("s_waitcnt vmcnt(%0)" :: "i"((n) + 4) : "memory"); if (ln == 0) SETFLAG(flao, f0a + 1); ST3(); \
      asm volatile("s_waitcnt vmcnt(%0)" :: "i"((n) + 1) : "memory"); BARRIER(); \
      bf16x8 pf_[4]; \
      _Pragma("unroll") for (int i = 0; i < 4; ++i) pf_[i] = lds_r128(L + xo + i * 1024); \
      _Pragma("unroll") for (int i = 0; i < 4; ++i) { acc[0] = MFMA32(W0[4 + i][0], pf_[i], acc[0]); acc[1] = MFMA32(W0[4 + i][1], pf_[i], acc[1]); } } } while (0)
#define MF(p, n) do { if (done == (p) + 1) { done = (p) + 2; \
      _Pragma("unroll") for (int k = 0; k < 8; ++k) { \
        const int c = k < 4 ? cb0[p] : cb1[p]; \
        const bf16x8 t0 = RDW(c, 0, k & 3), t1 = RDW(c, 1, k & 3); \
        asm volatile("s_waitcnt vmcnt(%1)" : "+v"(of[p][k]) : "i"((n) + 7 - k)); \
        pa[p][0] = MFMA32(t0, of[p][k], pa[p][0]); pa[p][1] = MFMA32(t1, of[p][k], pa[p][1]); } } } while (0)
#define LDO(p, k) asm volatile("global_load_dwordx4 %0, %1, %2 offset:%3 sc1" : "=v"(of[p][k]) : "v"(oo), "s"(oc_ + ((k) >> 2) * 4096), "i"(((k) & 3) * 1024) : "memory")
#define TAKE(p) do { \
    const int jj_ = __builtin_ctz((rdy & pend) | 256u), h_ = ((jj_ >> 1) - jq) & 3; pend &= ~(1u << jj_); if (h_ == 3) pos3 = (p); \
    cb0[p] = h_ == 1 ? CL(2) : h_ == 2 ? CL(4) : CL(6); cb1[p] = h_ == 1 ? CL(3) : h_ == 2 ? CL(5) : CL(7); \
    const char* oc_ = ob + (jj_ & 6) * 4096; asm volatile("" : "+s"(oc_)); \
    LDO(p, 0); LDO(p, 1); LDO(p, 2); LDO(p, 3); LDO(p, 4); LDO(p, 5); LDO(p, 6); LDO(p, 7); } while (0)
    if (!(rdy & pend)) { PUB(0); POLL((rdy & pend) != 0); }
    TAKE(0);
    if (!(rdy & pend)) { PUB(8); MF(0, 0); POLL((rdy & pend) != 0); }
    TAKE(1);
    if (!(rdy & pend)) { PUB(16); MF(0, 8); MF(1, 0); POLL((rdy & pend) != 0); }
    TAKE(2);
    PUB(24); MF(0, 16); MF(1, 8); MF(2, 0);
#pragma unroll
    for (int nb = 0; nb < 2; ++nb) {
      if (pos3 == 0) acc[nb] += (pa[1][nb] + pa[2][nb]) + pa[0][nb];
      else if (pos3 == 1) acc[nb] += (pa[0][nb] + pa[2][nb]) + pa[1][nb];
      else acc[nb] += (pa[0][nb] + pa[1][nb]) + pa[2][nb];
    }
  }
  {
    char* pws = ows + (32u << 20);
    const char* pst = pws + (size_t)(b * 32 + jq * 8 + 2 * qg) * 65536 + xq * 8192 + kh * 4096;
    const unsigned po = (unsigned)((r32e >> 4) * 65536 + hie * 256 + (r32e & 15) * 16);
#pragma unroll
    for (int nb = 0; nb < 2; ++nb)
#pragma unroll
      for (int g = 0; g < 4; ++g) {
        f32x4 v = {acc[nb][4 * g], acc[nb][4 * g + 1], acc[nb][4 * g + 2], acc[nb][4 * g + 3]};
        asm volatile("global_store_dwordx4 %0, %1, %2 offset:%3 sc1\n\ts_nop 1" :: "v"(po), "v"(v), "s"(pst), "i"((nb * 4 + g) * 512) : "memory");
      }
    foff = (unsigned)(flb + 32 + (ln & 7) * 4);
    LDFLAGS();
    WAITVM(0);
    if (ln == 0) { SETFLAG(flo, f0[16] + 1); SETFLAG(flao, f0a + 2); }
    PIN(pv); RDYOF(pv);
    const char* pin = pws + (size_t)bid * 65536 + w * 1024;
    const int z0 = XBOX(w) + 4096, z1 = XBOX(w ^ 4) - 4096;
    unsigned pend = 0xFF00u;
#pragma unroll 1
    while (pend) {
      if (!(rdy & pend)) POLL((rdy & pend) != 0);
      const int i_ = __builtin_ctz((rdy & pend) | 65536u);
      if (i_ == 16) break;
      pend &= ~(1u << i_);
      const int src = i_ & 7;
      dma1s(oo, pin + src * 8192, L0 + (src < 4 ? z0 : z1) + src * 1024);
    }
    if (ln == 0) SETFLAG(flo, f0[16] + 2);
    WAITVM(1);
    f32x4 s = *(__attribute__((address_space(3))) f32x4*)(L + z0 + ln * 16);
#pragma unroll
    for (int i = 1; i < 8; ++i) s += *(__attribute__((address_space(3))) f32x4*)(L + (i < 4 ? z0 : z1) + i * 1024 + ln * 16);
    u32x2 pk = {cvtpk(s[0], s[1]), cvtpk(s[2], s[3])};
    *(u32x2*)((char*)out + ((size_t)(b * 128 + 16 * xq + (ln & 15)) * 512 + 128 * jq + 16 * w + 8 * hie + 4 * ((ln >> 4) & 1)) * 2) = pk;
  }
}

extern "C" void kernel_launch(void* const* d_in, const int* in_sizes, int n_in, void* d_out, int out_size, void* d_ws, size_t ws_size,
                              hipStream_t stream) {
  const __hip_bfloat16* x = (const __hip_bfloat16*)d_in[0];
  const __hip_bfloat16* kc = (const __hip_bfloat16*)d_in[1];
  const __hip_bfloat16* vc = (const __hip_bfloat16*)d_in[2];
  const __hip_bfloat16* wq = (const __hip_bfloat16*)d_in[3];
  const __hip_bfloat16* wo = (const __hip_bfloat16*)d_in[4];
  __hip_bfloat16* out = (__hip_bfloat16*)d_out;
  (void)hipFuncSetAttribute((const void*)fused_kernel, hipFuncAttributeMaxDynamicSharedMemorySize, LDS1);
  fused_kernel<<<256, 512, LDS1, stream>>>(x, kc, vc, wq, wo, (char*)d_ws, (unsigned*)((char*)d_ws + (16u << 20)), out);
}
```

```cpp
#include <hip/hip_runtime.h>
#include <hip/hip_bf16.h>
#include <stdint.h>

typedef __attribute__((ext_vector_type(8))) short bf16x8;
typedef __attribute__((ext_vector_type(4))) short s16x4;
typedef __attribute__((ext_vector_type(16))) float f32x16;
typedef __attribute__((ext_vector_type(4))) float f32x4;
typedef __attribute__((ext_vector_type(4))) unsigned u32x4;
typedef __attribute__((ext_vector_type(2))) unsigned u32x2;
typedef __attribute__((address_space(3))) char lds_char;
typedef short v4i16_t __attribute__((ext_vector_type(4)));

constexpr int NB = 8, SQ = 128, DM = 512, NH = 32, SK = 2048, DH = 128;
constexpr int NT = SK / 64;
constexpr int LDS1 = 163840;
#define NPB 6
#define PBUF(c) (LDS1 - NPB * 16384 + (((c) + 4 * NPB - 16) % NPB) * 16384)
#define TCL 0, 2, 4, 6, 8, 9
__device__ constexpr int tile_step(int n) { constexpr int e[6] = {TCL}; return n < 10 - NPB ? e[n] : n + 6; }
__device__ constexpr int p_after(int c) {
  int mx = (c > 0 ? c : 0) - 1 + NPB; if (mx > 15) mx = 15;
  int n = 4 * (mx - (c + 1));
  for (int v = 2; v <= 6; v += 2) if (tile_step(v) < c && tile_step(v) >= c + 1 - NPB) n += 4;
  return n;
}
constexpr int XCH_OFF = 131072;
constexpr int LDS2 = 163840;
constexpr float CSC = 0.08838834764831845f * 1.4426950408889634f;

#define POL "nt"
#define MFMA32(a, b, c) __builtin_amdgcn_mfma_f32_32x32x16_bf16(a, b, c, 0, 0, 0)

typedef __attribute__((ext_vector_type(2))) float f32x2;
typedef __attribute__((ext_vector_type(2))) __bf16 bf16x2_t;
__device__ __forceinline__ unsigned cvtpk(float lo, float hi) {
  f32x2 v = {lo, hi};
  return __builtin_bit_cast(unsigned, __builtin_convertvector(v, bf16x2_t));
}
__device__ __forceinline__ bf16x8 pack8(float a0, float a1, float a2, float a3, float a4, float a5, float a6, float a7) {
  u32x4 w = {cvtpk(a0, a1), cvtpk(a2, a3), cvtpk(a4, a5), cvtpk(a6, a7)};
  return __builtin_bit_cast(bf16x8, w);
}
__device__ __forceinline__ bf16x8 lds_r128(lds_char* p) { return *(__attribute__((address_space(3))) bf16x8*)p; }
__device__ __forceinline__ s16x4 lds_tr(lds_char* p) {
  return __builtin_bit_cast(s16x4, __builtin_amdgcn_ds_read_tr16_b64_v4i16((__attribute__((address_space(3))) v4i16_t*)p));
}
__device__ __forceinline__ bf16x8 cat8(s16x4 a, s16x4 b) { return __builtin_shufflevector(a, b, 0, 1, 2, 3, 4, 5, 6, 7); }

__device__ __forceinline__ void dma4(unsigned o0, unsigned o1, unsigned o2, unsigned o3, const void* sbase, unsigned ldsaddr) {
  unsigned keep;
  asm volatile(
      "s_mov_b32 %0, m0\n\t"
      "s_mov_b32 m0, %6\n\t"
      "s_nop 0\n\t"
      "global_load_lds_dwordx4 %1, %5 " POL "\n\t"
      "global_load_lds_dwordx4 %2, %5 offset:1024 " POL "\n\t"
      "global_load_lds_dwordx4 %3, %5 offset:2048 " POL "\n\t"
      "global_load_lds_dwordx4 %4, %5 offset:3072 " POL "\n\t"
      "s_mov_b32 m0, %0"
      : "=&s"(keep) : "v"(o0), "v"(o1), "v"(o2), "v"(o3), "s"(sbase), "s"(ldsaddr) : "memory");
}
__device__ __forceinline__ void dma2(unsigned o0, unsigned o1, const void* sbase, unsigned ldsaddr) {
  unsigned keep;
  asm volatile(
      "s_mov_b32 %0, m0\n\t"
      "s_mov_b32 m0, %4\n\t"
      "s_nop 0\n\t"
      "global_load_lds_dwordx4 %1, %3\n\t"
      "global_load_lds_dwordx4 %2, %3 offset:1024\n\t"
      "s_mov_b32 m0, %0"
      : "=&s"(keep) : "v"(o0), "v"(o1), "s"(sbase), "s"(ldsaddr) : "memory");
}
__device__ __forceinline__ void dma1(unsigned o0, const void* sbase, unsigned ldsaddr) {
  unsigned keep;
  asm volatile(
      "s_mov_b32 %0, m0\n\t"
      "s_mov_b32 m0, %3\n\t"
      "s_nop 0\n\t"
      "global_load_lds_dwordx4 %1, %2\n\t"
      "s_mov_b32 m0, %0"
      : "=&s"(keep) : "v"(o0), "s"(sbase), "s"(ldsaddr) : "memory");
}
template <int J> __device__ __forceinline__ void dmap(unsigned o, const void* sbase, unsigned ldsaddr) {
  unsigned keep;
  asm volatile("s_mov_b32 %0, m0\n\ts_mov_b32 m0, %3\n\ts_nop 0\n\tglobal_load_lds_dwordx4 %1, %2 offset:%4 " POL "\n\ts_mov_b32 m0, %0"
               : "=&s"(keep) : "v"(o), "s"(sbase), "s"(ldsaddr), "i"(J * 1024) : "memory");
}
template <int J> __device__ __forceinline__ void dmapw(unsigned o, const void* sbase, unsigned ldsaddr) {
  unsigned keep;
  asm volatile("s_mov_b32 %0, m0\n\ts_mov_b32 m0, %3\n\ts_nop 0\n\tglobal_load_lds_dwordx4 %1, %2 offset:%4\n\ts_mov_b32 m0, %0"
               : "=&s"(keep) : "v"(o), "s"(sbase), "s"(ldsaddr), "i"(J * 1024) : "memory");
}
__device__ __forceinline__ int lane_id() {
  int lane;
  asm volatile("v_mbcnt_lo_u32_b32 %0, -1, 0\n\tv_mbcnt_hi_u32_b32 %0, -1, %0" : "=v"(lane));
  return lane;
}
__device__ __forceinline__ float fadd_s(float a, float b) { float r; asm("v_add_f32_e32 %0, %1, %2" : "=v"(r) : "v"(a), "v"(b)); return r; }
#define GAP() __builtin_amdgcn_sched_barrier(0)
__device__ __forceinline__ int kslot(int i) { return i == 0 ? 0 : i * 32768 - 16384; }
__device__ __forceinline__ int vslot(int i) { return i == 4 ? 147456 : i * 32768 + 32768; }
#define WAITVM(n) asm volatile("s_waitcnt vmcnt(" #n ")" ::: "memory")
#define WAITVML(n) asm volatile("s_waitcnt vmcnt(" #n ") lgkmcnt(0)" ::: "memory")
#define BARRIER() asm volatile("s_waitcnt lgkmcnt(0)\n\ts_barrier" ::: "memory")
#define PIN(x) asm volatile("" : "+v"(x))

__device__ __forceinline__ float swapmax(float v) {
  auto rr = __builtin_amdgcn_permlane32_swap(__float_as_uint(v), __float_as_uint(v), false, false);
  return fmaxf(__uint_as_float(rr[0]), __uint_as_float(rr[1]));
}
__device__ __forceinline__ float swapadd(float v) {
  auto rr = __builtin_amdgcn_permlane32_swap(__float_as_uint(v), __float_as_uint(v), false, false);
  return __uint_as_float(rr[0]) + __uint_as_float(rr[1]);
}

__global__ __launch_bounds__(512, 2) void fused_kernel(const __hip_bfloat16* __restrict__ x, const __hip_bfloat16* __restrict__ kc,
                                                       const __hip_bfloat16* __restrict__ vc, const __hip_bfloat16* __restrict__ wq,
                                                       const __hip_bfloat16* __restrict__ wo, char* ows, unsigned* flg,
                                                       __hip_bfloat16* __restrict__ out) {
  extern __shared__ __attribute__((aligned(16))) char smem[];
  lds_char* L = (lds_char*)smem;
  const unsigned L0 = (unsigned)(uintptr_t)smem;
  const int tid = threadIdx.x, lane = tid & 63;
  const int w = __builtin_amdgcn_readfirstlane(tid >> 6);
  const int qg = w & 3, kh = w >> 2;
  const int r32 = lane & 31, hi = lane >> 5;
  const int q4 = (lane & 15) >> 2, p4 = lane & 3, g1 = (lane >> 4) & 1;
  const int bid = blockIdx.x;
#define HMAP 0
  const int head = 4 * (bid & 7) + ((bid >> 3) & 3), b = bid >> 5;
#define HW_ 4
#define HI_ 1
  const char* kbase = (const char*)kc + ((size_t)(b * NH + head) * SK) * 256;
  const char* vbase = (const char*)vc + ((size_t)(b * NH + head) * SK) * 256;
  const char* wqbase = (const char*)wq + head * 256;
#define FSTR 256
#define XA 2
  const int xcd = bid & 7, jx = bid >> 3;
  const int nblk = ((xcd & ((8 >> XA) - 1)) << XA) | (jx & ((1 << XA) - 1)), mblk = (xcd >> (3 - XA)) * (32 >> XA) + (jx >> XA);
  int fia = (mblk >> 2) * 32 + w;
  int fib = ((((4 * b) % (32 >> XA)) << XA) | (w & ((1 << XA) - 1))) * 8 + ((((4 * b) / (32 >> XA)) << (3 - XA)) | (w >> XA)) - (32 << XA);
  int fic = bid;
  asm volatile("" : "+s"(fia), "+s"(fib), "+s"(fic));
#define FOFF(lane) (((lane) < 8 ? ((lane) < 4 ? fia + 8 * (lane) : fib + ((lane) << (3 + XA))) : fic) * FSTR)
  unsigned fv;
  asm volatile("global_load_dword %0, %1, %2 sc1" : "=v"(fv) : "v"((unsigned)FOFF(lane)), "s"(flg) : "memory");

  const int drow = lane >> 4, dpos = lane & 15;
  unsigned ko[4];
#pragma unroll
  for (int j = 0; j < 4; ++j) ko[j] = (unsigned)(((w & 3) * 16 + drow) * 256 + ((dpos ^ (4 * j + drow)) << 4));
  const unsigned vo = (unsigned)(((w & 3) * 16 + drow) * 256 + ((dpos ^ (drow << 2)) << 4));
  unsigned wo2[2], xo2[2];
#pragma unroll
  for (int j = 0; j < 2; ++j) {
    wo2[j] = (unsigned)(((w & 3) * 8 + 4 * j + drow) * 8192 + ((dpos ^ (drow << 2)) << 4) - j * 1024);
    xo2[j] = (unsigned)(((w & 3) * 32 + 16 * j + (lane >> 2)) * 1024 + (((lane & 3) ^ drow) << 4) - j * 1024);
  }
  const char* xbase = (const char*)x + (size_t)b * SQ * 1024;

#define ISSUE_K(t, la) dma4(ko[0], ko[1], ko[2], ko[3], kbase + (size_t)(t) * 16384, (la) + (w & 3) * 4096)
#define ISSUE_V(t, la) dma4(vo, vo, vo, vo, vbase + (size_t)(t) * 16384, (la) + (w & 3) * 4096)
#define ROT 1
  const int crot = ROT ? 2 * b + ((bid >> 3) & 1) + 8 * ((bid >> 4) & 1) : 0;
#define ISSUE_P(c) do { const int cc_ = ((c) + crot) & 15; \
    dma2(PV_W0, PV_W1, PV_WB, L0 + PBUF(c) + (w & 3) * 2048); \
    dma2(PV_X0, PV_X1, PV_XB, L0 + PBUF(c) + 8192 + (w & 3) * 2048); } while (0)
#define PV 0
#define PV_X0 xo2[0]
#define PV_X1 xo2[1]
#define PV_XB (xbase + cc_ * 64)
#define PV_W0 wo2[0]
#define PV_W1 wo2[1]
#define PV_WB (wqbase + (size_t)cc_ * 32 * 8192)

#define ISSUE_T(c) do { \
    if (w >= 4) { \
      if (tile_step(2) == (c)) ISSUE_V(0, L0 + vslot(0)); \
      if (tile_step(4) == (c)) ISSUE_V(1, L0 + vslot(1)); \
      if (tile_step(6) == (c)) ISSUE_V(2, L0 + vslot(2)); \
    } else { \
      if (tile_step(0) == (c)) ISSUE_K(0, L0 + kslot(0)); \
      if (tile_step(1) == (c)) ISSUE_K(1, L0 + kslot(1)); \
      if (tile_step(3) == (c)) ISSUE_K(2, L0 + kslot(2)); \
      if (tile_step(5) == (c)) ISSUE_K(3, L0 + kslot(3)); \
      if (tile_step(7) == (c)) ISSUE_K(4, L0 + kslot(4)); \
    } } while (0)
  if (w >= 4) {
#pragma unroll
    for (int c = 0; c < NPB; ++c) ISSUE_P(c);
  }
  ISSUE_T(-2);

  f32x16 qa[2] = {};
  {
    const int ps = ((p4 & 1) << 1) | (p4 >> 1);
    int wa[2], xa[2];
#pragma unroll
    for (int dd = 0; dd < 2; ++dd) {
      const int dblk = 2 * kh + dd;
      wa[dd] = (8 * hi + q4) * 256 + (((4 * dblk + 2 * g1 + (ps >> 1)) ^ (q4 << 2)) << 4) + 8 * (ps & 1);
      xa[dd] = 8192 + (qg * 32 + r32) * 64 + (((2 * dd + hi) ^ ((r32 >> 2) & 3)) << 4);
    }
    bf16x8 xf[2][2], af[2][2][2];
#pragma unroll
    for (int c = -1; c < 16; ++c) {
      if (w >= 4 && c < 15) asm volatile("s_waitcnt vmcnt(%0)" :: "i"(p_after(c)) : "memory");
      BARRIER();
      if (w >= 4 && c >= 0 && c + NPB < 16) ISSUE_P(c + NPB);
      ISSUE_T(c);
      if (c < 15) {
        lds_char* wb = L + PBUF(c + 1);
#pragma unroll
        for (int s = 0; s < 2; ++s) {
          xf[(c + 1) & 1][s] = lds_r128(wb + xa[s]);
#pragma unroll
          for (int dd = 0; dd < 2; ++dd)
            af[(c + 1) & 1][s][dd] = cat8(lds_tr(wb + wa[dd] + s * 4096), lds_tr(wb + wa[dd] + s * 4096 + 1024));
        }
      }
      GAP();
      if (c >= 0) {
#pragma unroll
        for (int s = 0; s < 2; ++s)
#pragma unroll
          for (int dd = 0; dd < 2; ++dd) qa[dd] = MFMA32(af[c & 1][s][dd], xf[c & 1][s], qa[dd]);
      }
      GAP();
    }
  }
  bf16x8 qf[8];
  unsigned f0[9];
  {
    bf16x8 own[4];
#pragma unroll
    for (int dd = 0; dd < 2; ++dd)
#pragma unroll
      for (int sp = 0; sp < 2; ++sp)
      {
        u32x4 t = __builtin_bit_cast(u32x4, pack8(qa[dd][8 * sp + 0], qa[dd][8 * sp + 1], qa[dd][8 * sp + 2], qa[dd][8 * sp + 3],
                                                  qa[dd][8 * sp + 4], qa[dd][8 * sp + 5], qa[dd][8 * sp + 6], qa[dd][8 * sp + 7]));
#pragma unroll
        for (int e = 0; e < 4; ++e) t[e] = cvtpk(__uint_as_float(t[e] << 16) * CSC, __uint_as_float(t[e] & 0xffff0000u) * CSC);
        own[dd * 2 + sp] = __builtin_bit_cast(bf16x8, t);
      }
#pragma unroll
    for (int i = 0; i < 4; ++i) *(__attribute__((address_space(3))) bf16x8*)(L + XCH_OFF + w * 4096 + i * 1024 + lane * 16) = own[i];
    if (w < 4) WAITVM(12);
    BARRIER();
    PIN(fv);
#pragma unroll
    for (int i = 0; i < 9; ++i) f0[i] = __builtin_amdgcn_readlane(fv, i);
#pragma unroll
    for (int i = 0; i < 4; ++i) {
      bf16x8 o = lds_r128(L + XCH_OFF + (w ^ 4) * 4096 + i * 1024 + lane * 16);
      if (kh == 0) { qf[i] = own[i]; qf[4 + i] = o; } else { qf[4 + i] = own[i]; qf[i] = o; }
    }
  }

  f32x16 oacc[4] = {};
  const int krow = kh * 32 + r32;
  const int kaddr0 = krow * 256 + ((hi ^ (krow & 15)) << 4);
  int vaddr0[4];
#pragma unroll
  for (int d = 0; d < 4; ++d) vaddr0[d] = (kh * 32 + 4 * hi + q4) * 256 + ((d ^ q4) << 6) + g1 * 32 + p4 * 8;

  bf16x8 kf[8], vf[4][2], pf[2];
  f32x16 sc = {}, sn, negm;
#pragma unroll
  for (int s = 0; s < 8; ++s) kf[s] = lds_r128(L + kslot(0) + (kaddr0 ^ (s << 5)));
#pragma unroll
  for (int s = 0; s < 8; ++s) sc = MFMA32(kf[s], qf[s], sc);
#pragma unroll
  for (int s = 0; s < 8; ++s) kf[s] = lds_r128(L + kslot(1) + (kaddr0 ^ (s << 5)));
  float m;
  {
    float tmax = sc[0];
#pragma unroll
    for (int r = 1; r < 16; ++r) tmax = fmaxf(tmax, sc[r]);
    m = swapmax(tmax);
#pragma unroll
    for (int r = 0; r < 16; ++r) { sc[r] -= m; negm[r] = -m; }
  }
  float l0 = 0.f, l1 = 0.f;
  const char* dbase = w < 4 ? kbase : vbase;
  unsigned dof0 = w < 4 ? ko[0] : vo, dof1 = w < 4 ? ko[1] : vo, dof2 = w < 4 ? ko[2] : vo, dof3 = w < 4 ? ko[3] : vo;
  const int dahead = w < 4 ? 6 : 4;
  int ks = kslot(2), vs = vslot(0), ksp = kslot(1), vsp = vslot(4);

#define RDV(d, s) vf[d][s] = cat8(lds_tr(vb + vaddr0[d] + (s) * 4096), lds_tr(vb + vaddr0[d] + (s) * 4096 + 2048))
#define RDK(s) kf[s] = lds_r128(kb + (kaddr0 ^ ((s) << 5)))
#define EXP(SC, r) SC[r] = __builtin_amdgcn_exp2f(SC[r])
#define CVT(SC, s, e) pw[s][e] = cvtpk(SC[8 * (s) + 2 * (e)], SC[8 * (s) + 2 * (e) + 1])
#define ADD(SC, r) do { l0 = fadd_s(l0, SC[r]); l1 = fadd_s(l1, SC[(r) + 1]); } while (0)
#define PVM(d, s) oacc[d] = MFMA32(vf[d][s], __builtin_bit_cast(bf16x8, pw[s]), oacc[d])
#define QKM(SN, s) SN = MFMA32(kf[s], qf[s], SN)
#define DMA(J, o) do { if (dmode == 1) dmap<J>(o, dptr, dla); else if (dmode == 2) dmapw<J>(o, dptr, dla); } while (0)
#define RESCALE(SN, tmax) do { \
    if (__builtin_amdgcn_ballot_w64(!(tmax <= 8.f)) != 0) { \
      const float mn = m + fmaxf(tmax, 0.f); \
      const float dl = mn - m; \
      const float alpha = __builtin_amdgcn_exp2f(-dl); \
      _Pragma("unroll") for (int d = 0; d < 4; ++d) oacc[d] *= alpha; \
      _Pragma("unroll") for (int r = 0; r < 16; ++r) { SN[r] -= dl; negm[r] = -mn; } \
      l0 *= alpha; l1 *= alpha; m = mn; \
    } } while (0)
#define BODY(SC, SN, HASK) do { \
    lds_char* vb = L + vs; \
    lds_char* kb = L + ks; \
    u32x4 pw[2]; \
    SN = MFMA32(kf[0], qf[0], negm); RDV(0, 0); RDV(1, 0); EXP(SC, 0); EXP(SC, 1); GAP(); \
    QKM(SN, 1); RDV(2, 0); RDV(3, 0); EXP(SC, 2); EXP(SC, 3); CVT(SC, 0, 0); GAP(); \
    QKM(SN, 2); RDV(0, 1); RDV(1, 1); EXP(SC, 4); EXP(SC, 5); CVT(SC, 0, 1); GAP(); \
    QKM(SN, 3); RDV(2, 1); RDV(3, 1); EXP(SC, 6); EXP(SC, 7); CVT(SC, 0, 2); GAP(); \
    QKM(SN, 4); DMA(0, dof0); EXP(SC, 8); EXP(SC, 9); CVT(SC, 0, 3); GAP(); \
    QKM(SN, 5); DMA(1, dof1); EXP(SC, 10); EXP(SC, 11); CVT(SC, 1, 0); GAP(); \
    QKM(SN, 6); DMA(2, dof2); EXP(SC, 12); EXP(SC, 13); CVT(SC, 1, 1); GAP(); \
    QKM(SN, 7); DMA(3, dof3); EXP(SC, 14); EXP(SC, 15); CVT(SC, 1, 2); GAP(); \
    PVM(0, 0); if (HASK) { RDK(0); RDK(1); } CVT(SC, 1, 3); ADD(SC, 0); ADD(SC, 2); GAP(); \
    PVM(1, 0); if (HASK) { RDK(2); RDK(3); } ADD(SC, 4); ADD(SC, 6); GAP(); \
    PVM(2, 0); if (HASK) { RDK(4); RDK(5); } ADD(SC, 8); ADD(SC, 10); GAP(); \
    PVM(3, 0); if (HASK) { RDK(6); RDK(7); } ADD(SC, 12); ADD(SC, 14); GAP(); \
    float t0 = fmaxf(fmaxf(SN[0], SN[1]), SN[2]), t1 = fmaxf(fmaxf(SN[3], SN[4]), SN[5]); \
    PVM(0, 1); t0 = fmaxf(fmaxf(t0, SN[6]), SN[7]); t1 = fmaxf(fmaxf(t1, SN[8]), SN[9]); GAP(); \
    PVM(1, 1); t0 = fmaxf(fmaxf(t0, SN[10]), SN[11]); t1 = fmaxf(fmaxf(t1, SN[12]), SN[13]); GAP(); \
    PVM(2, 1); t0 = fmaxf(fmaxf(t0, SN[14]), SN[15]); t0 = swapmax(fmaxf(t0, t1)); GAP(); \
    PVM(3, 1); GAP(); \
    RESCALE(SN, t0); \
    ksp = ks; vsp = vs; \
    ks = ks == 0 ? 16384 : (ks == 114688 ? 0 : ks + 32768); \
    vs = vs == 131072 ? 147456 : (vs == 147456 ? 32768 : vs + 32768); } while (0)

  {
    WAITVM(8);
    BARRIER();
    if (w < 4) { ISSUE_K(5, L0 + kslot(0)); ISSUE_K(6, L0 + kslot(1)); }
    else { ISSUE_V(3, L0 + vslot(3)); ISSUE_V(4, L0 + vslot(4)); }
    const int dmode = 0; const char* dptr = dbase; const unsigned dla = 0;
    BODY(sc, sn, true);
  }
#define DSETUP(it) \
    const char* dptr = dbase + (size_t)((it) + dahead) * 16384; \
    const unsigned dla = L0 + (w < 4 ? ksp : vsp) + (w & 3) * 4096;
#pragma unroll 1
  for (int it = 1; it < 25; it += 2) {
    { WAITVM(12); BARRIER(); const int dmode = 1; DSETUP(it); BODY(sn, sc, true); }
    { WAITVM(12); BARRIER(); const int dmode = 1; DSETUP(it + 1); BODY(sc, sn, true); }
  }
  const char* wot = (const char*)wo + nblk * 128 + (size_t)(w & 3) * (2 * HW_ * 131072) + (3 * HI_ * 131072 - 24 * 16384);
#define WOFFS() do { const int ln_ = lane_id(), dr_ = ln_ >> 3; \
    dof0 = (unsigned)(dr_ * 1024 + (((ln_ & 7) ^ (((dr_ >> 1) & 1) << 2)) << 4)); \
    dof1 = dof0 + 7168; dof2 = dof0 + HW_ * 131072 - 2048; dof3 = dof0 + HW_ * 131072 + 8192 - 3072; } while (0)
#define TSETUP(it, ku, vu) \
    asm volatile("" : "+s"(ks), "+s"(vs), "+s"(ksp), "+s"(vsp)); \
    const int dmode = w < 4 ? ((it) + 6 < NT ? 1 : (ku) >= 0 ? 2 : 0) : ((it) + 4 < NT ? 1 : (vu) >= 0 ? 2 : 0); \
    const char* dptr = dmode == 1 ? dbase + (size_t)((it) + dahead) * 16384 : wot + (w < 4 ? (ku) : (vu)) * 16384; \
    const unsigned dla = L0 + (w < 4 ? ksp : vsp) + (w & 3) * 4096;
  { WAITVM(12); BARRIER(); TSETUP(25, -1, -1); BODY(sn, sc, true); }
  if (w < 4) WOFFS();
  { WAITVM(12); BARRIER(); TSETUP(26, 24, -1); BODY(sc, sn, true); }
  { WAITVM(12); BARRIER(); TSETUP(27, 25, -1); BODY(sn, sc, true); }
  if (w >= 4) WOFFS();
  { WAITVM(12); BARRIER(); TSETUP(28, 26, 27); BODY(sc, sn, true); }
  { WAITVM(12); BARRIER(); TSETUP(29, 28, 29); BODY(sn, sc, true); }
  { WAITVM(12); BARRIER(); TSETUP(30, 30, 31); BODY(sc, sn, false); }
  asm volatile("" : "+s"(vs));

  const int ln = lane_id();
  const int r32e = ln & 31, hie = ln >> 5;
  const unsigned wof0 = (unsigned)((ln >> 3) * 1024 + (((ln & 7) ^ ((((ln >> 3) >> 1) & 1) << 2)) << 4)), wof1 = wof0 + 7168;
  const int tw0 = w * 2048 + (4 * hie + ((ln & 15) >> 2)) * 128 + ((((ln & 15) >> 3)) << 6) + ((ln >> 4) & 1) * 32 + (ln & 3) * 8;
  const int tw1 = tw0 ^ 64;
  const char* wob = (const char*)wo + nblk * 128 + (size_t)w * (HW_ * 131072);
#define CELL(i) ((i) == 0 ? 49152 : (i) == 1 ? 81920 : (i) == 2 ? 114688 : (i) == 3 ? 98304 : (i) == 4 ? 0 : \
                 (i) == 5 ? 131072 : (i) == 6 ? 16384 : (i) == 7 ? 147456 : (i) == 8 ? 32768 : 65536)
#define RDW(dst, i) do { dst[0] = cat8(lds_tr(L + CELL(i) + tw0), lds_tr(L + CELL(i) + tw0 + 1024)); \
                         dst[1] = cat8(lds_tr(L + CELL(i) + tw1), lds_tr(L + CELL(i) + tw1 + 1024)); } while (0)
#define DMAW(u, i) dma2(wof0, wof1, wob + (u) * 16384, L0 + CELL(i) + w * 2048)
#define WAITL() asm volatile("s_waitcnt lgkmcnt(0)" ::: "memory")
  bf16x8 A[8][2];
  const unsigned foff = (unsigned)FOFF(ln);
  unsigned pv;
#define LDFLAGS() asm volatile("global_load_dword %0, %1, %2 sc1" : "=v"(pv) : "v"(foff), "s"(flg) : "memory")
  {
    WAITVM(8);
    BARRIER();
    LDFLAGS();
    lds_char* vb = L + vs;
    u32x4 pw[2];
#pragma unroll
    for (int s = 0; s < 2; ++s)
#pragma unroll
      for (int d = 0; d < 4; ++d) RDV(d, s);
    RDW(A[0], 0); RDW(A[1], 1); RDW(A[2], 2); RDW(A[3], 3);
#pragma unroll
    for (int r = 0; r < 16; ++r) EXP(sn, r);
#pragma unroll
    for (int r = 0; r < 16; r += 2) ADD(sn, r);
#pragma unroll
    for (int s = 0; s < 2; ++s)
#pragma unroll
      for (int e = 0; e < 4; ++e) CVT(sn, s, e);
#pragma unroll
    for (int s = 0; s < 2; ++s)
#pragma unroll
      for (int d = 0; d < 4; ++d) PVM(d, s);
  }

  const char* ob = ows + ((size_t)mblk * 256 + w * HW_ * 8) * 1024;
  unsigned f0v = f0[0];
#pragma unroll
  for (int i = 1; i < 8; ++i) asm volatile("v_writelane_b32 %0, %1, %2" : "+v"(f0v) : "s"(f0[i]), "i"(i));
  unsigned rdy, pend = 7;
  int pos2 = 0;
  const char *wcur, *ocur;
  bf16x8 of[8];
  const unsigned oo = ln * 16;
#define GETRDY() do { PIN(pv); rdy = (unsigned)__builtin_amdgcn_ballot_w64(pv != f0v) & 255u; } while (0)
#define POLL(cond) do { if (!(cond)) { \
      int spins_ = 0; \
      do { __builtin_amdgcn_s_sleep(2); unsigned pq_; \
        asm volatile("global_load_dword %0, %1, %2 sc1\n\ts_waitcnt vmcnt(0)" : "=v"(pq_) : "v"(foff), "s"(flg) : "memory"); \
        rdy = (unsigned)__builtin_amdgcn_ballot_w64(pq_ != f0v) & 255u; \
      } while (!(cond) && ++spins_ < (1 << 18)); } } while (0)
#define CHOOSE(p) do { \
    const int h_ = __builtin_ctz((rdy & pend) | 8u) & 3; pend &= ~(1u << h_); if (h_ == 2) pos2 = (p); \
    wcur = wob + h_ * (HI_ * 131072); ocur = ob + h_ * (HI_ * 8192); asm volatile("" : "+s"(wcur), "+s"(ocur)); LDFLAGS(); } while (0)
#define PUBLISH(v) do { BARRIER(); if (w == 0 && ln == 0) \
    __hip_atomic_store((__attribute__((address_space(1))) unsigned*)(flg + fic * (FSTR / 4)), f0[8] + (v), __ATOMIC_RELAXED, __HIP_MEMORY_SCOPE_AGENT); } while (0)
#define RING(j) ((j) % 10 == 0 ? 9 : (j) % 10 < 6 ? (j) % 10 + 3 : (j) % 10 - 6)
#define LDO(j) asm volatile("global_load_dwordx4 %0, %1, %2 offset:%3 sc1" : "=v"(of[(j) & 7]) : "v"(oo), "s"(ocur + (((j) >> 2) & 1) * 4096), "i"(((j) & 3) * 1024) : "memory")
#define ISS(j) do { if ((j) < 24) dma2(wof0, wof1, wcur + ((j) & 7) * 16384, L0 + CELL(RING(j)) + w * 2048); LDO(j); } while (0)

  const float l = swapadd(l0 + l1);
  {
    float* ml = (float*)(smem + CELL(8));
    if (hie == 0) { ml[w * 512 + r32e] = m; ml[w * 512 + 32 + r32e] = l; }
#pragma unroll
    for (int dd = 0; dd < 2; ++dd)
#pragma unroll
      for (int g = 0; g < 4; ++g) {
        f32x4 v;
        if (kh) { v[0] = oacc[dd][4 * g]; v[1] = oacc[dd][4 * g + 1]; v[2] = oacc[dd][4 * g + 2]; v[3] = oacc[dd][4 * g + 3]; }
        else { v[0] = oacc[2 + dd][4 * g]; v[1] = oacc[2 + dd][4 * g + 1]; v[2] = oacc[2 + dd][4 * g + 2]; v[3] = oacc[2 + dd][4 * g + 3]; }
        *(__attribute__((address_space(3))) f32x4*)(L + CELL(dd * 2 + (g >> 1)) + w * 2048 + (g & 1) * 1024 + ln * 16) = v;
      }
    WAITVM(0);
    BARRIER();
    GETRDY();
    RDW(A[4], 4); RDW(A[5], 5); RDW(A[6], 6); RDW(A[7], 7);
    const float mp = ml[(w ^ 4) * 512 + r32e], lp = ml[(w ^ 4) * 512 + 32 + r32e];
    const float mf = fmaxf(m, mp);
    const float wa = __builtin_amdgcn_exp2f(m - mf), wb = __builtin_amdgcn_exp2f(mp - mf);
    const float inv = 1.f / (wa * l + wb * lp);
    const float fa = wa * inv, fb = wb * inv;
    const char* ost = ows + ((size_t)(b * 4 + qg) * 256 + head * 8 + 4 * kh) * 1024;
#pragma unroll
    for (int dd = 0; dd < 2; ++dd) {
      float o[16];
#pragma unroll
      for (int g = 0; g < 4; ++g) {
        f32x4 v = *(__attribute__((address_space(3))) f32x4*)(L + CELL(dd * 2 + (g >> 1)) + (w ^ 4) * 2048 + (g & 1) * 1024 + ln * 16);
#pragma unroll
        for (int e = 0; e < 4; ++e) {
          const float own = kh ? oacc[2 + dd][4 * g + e] : oacc[dd][4 * g + e];
          o[4 * g + e] = own * fa + v[e] * fb;
        }
      }
#pragma unroll
      for (int sp = 0; sp < 2; ++sp) {
        bf16x8 f = pack8(o[8 * sp + 0], o[8 * sp + 1], o[8 * sp + 2], o[8 * sp + 3], o[8 * sp + 4], o[8 * sp + 5], o[8 * sp + 6], o[8 * sp + 7]);
        asm volatile("global_store_dwordx4 %0, %1, %2 offset:%3 sc1\n\ts_nop 1" :: "v"((unsigned)(ln * 16)), "v"(f), "s"(ost), "i"((dd * 2 + sp) * 1024) : "memory");
      }
    }
    WAITL();
    const bool early = (rdy & 7u) != 0;
#define ST3()
    if (!early) { WAITVM(0); PUBLISH(1u); ST3(); POLL((rdy & 7u) != 0); }
    CHOOSE(0); ISS(0); ISS(1); ISS(2); ISS(3); ISS(4);
    if (early) { WAITVM(16); PUBLISH(1u); ST3(); }
  }
  ISS(5); ISS(6); ISS(7);
  f32x16 pa[3][2] = {}, acc[2];
#define STQ(j)
#define NOPS(u) (((u) < 24 ? 3 : (u) < 32 ? 1 : 0) + ((u) < 24 && (u) % 8 == 0 ? 1 : 0))
#define NAFT(j) (NOPS((j) - 7) + NOPS((j) - 6) + NOPS((j) - 5) + NOPS((j) - 4) + NOPS((j) - 3) + NOPS((j) - 2) + NOPS((j) - 1))
#define STEP(j) do { asm volatile("s_waitcnt vmcnt(%0)" :: "i"(NAFT(j)) : "memory"); PIN(of[(j) & 7]); \
    if ((j) < 32) { bf16x8 t_[2]; RDW(t_, RING((j) - 8)); \
      pa[(j) / 8 - 1][0] = MFMA32(t_[0], of[(j) & 7], pa[(j) / 8 - 1][0]); pa[(j) / 8 - 1][1] = MFMA32(t_[1], of[(j) & 7], pa[(j) / 8 - 1][1]); } \
    else { acc[0] = MFMA32(A[(j) & 7][0], of[(j) & 7], acc[0]); acc[1] = MFMA32(A[(j) & 7][1], of[(j) & 7], acc[1]); } \
    if ((j) == 8 || (j) == 16) { GETRDY(); POLL((rdy & pend) != 0); CHOOSE((j) / 8); } \
    if ((j) == 24) { GETRDY(); POLL((rdy & 8u) != 0); ocur = ob + 3 * HI_ * 8192; asm volatile("" : "+s"(ocur)); } \
    STQ(j); if ((j) < 32) { WAITL(); ISS(j); } } while (0)
  STEP(8); STEP(9); STEP(10); STEP(11); STEP(12); STEP(13); STEP(14); STEP(15);
  STEP(16); STEP(17); STEP(18); STEP(19); STEP(20); STEP(21); STEP(22); STEP(23);
  STEP(24); STEP(25); STEP(26); STEP(27); STEP(28); STEP(29); STEP(30); STEP(31);
#pragma unroll
  for (int nb = 0; nb < 2; ++nb) {
    if (pos2 == 0) acc[nb] = (pa[1][nb] + pa[2][nb]) + pa[0][nb];
    else if (pos2 == 1) acc[nb] = (pa[0][nb] + pa[2][nb]) + pa[1][nb];
    else acc[nb] = (pa[0][nb] + pa[1][nb]) + pa[2][nb];
  }
  STEP(32); STEP(33); STEP(34); STEP(35); STEP(36); STEP(37); STEP(38); STEP(39);
#pragma unroll
  for (int nb = 0; nb < 2; ++nb)
#pragma unroll
    for (int g = 0; g < 4; ++g) {
      f32x4 v = {acc[nb][4 * g], acc[nb][4 * g + 1], acc[nb][4 * g + 2], acc[nb][4 * g + 3]};
      *(__attribute__((address_space(3))) f32x4*)(L + CELL(nb * 2 + (g >> 1)) + w * 2048 + (g & 1) * 1024 + ln * 16) = v;
    }
  POLL((rdy & 0xF0u) == 0xF0u);
  PUBLISH(2u);
  {
    const int cb = (w >> 1) == 0 ? CELL(0) : (w >> 1) == 1 ? CELL(1) : (w >> 1) == 2 ? CELL(2) : CELL(3);
    f32x4 s = {0.f, 0.f, 0.f, 0.f};
#pragma unroll
    for (int ww = 0; ww < 8; ++ww) s += *(__attribute__((address_space(3))) f32x4*)(L + cb + ww * 2048 + (w & 1) * 1024 + ln * 16);
    const int nb = w >> 2, g = w & 3;
    u32x2 pk = {cvtpk(s[0], s[1]), cvtpk(s[2], s[3])};
    *(u32x2*)((char*)out + ((size_t)(mblk * 32 + r32e) * 512 + nblk * 64 + nb * 32 + 8 * g + 4 * hie) * 2) = pk;
  }
}

extern "C" void kernel_launch(void* const* d_in, const int* in_sizes, int n_in, void* d_out, int out_size, void* d_ws, size_t ws_size,
                              hipStream_t stream) {
  const __hip_bfloat16* x = (const __hip_bfloat16*)d_in[0];
  const __hip_bfloat16* kc = (const __hip_bfloat16*)d_in[1];
  const __hip_bfloat16* vc = (const __hip_bfloat16*)d_in[2];
  const __hip_bfloat16* wq = (const __hip_bfloat16*)d_in[3];
  const __hip_bfloat16* wo = (const __hip_bfloat16*)d_in[4];
  __hip_bfloat16* out = (__hip_bfloat16*)d_out;
  (void)hipFuncSetAttribute((const void*)fused_kernel, hipFuncAttributeMaxDynamicSharedMemorySize, LDS1);
  fused_kernel<<<256, 512, LDS1, stream>>>(x, kc, vc, wq, wo, (char*)d_ws, (unsigned*)((char*)d_ws + (16u << 20)), out);
}
```

```cpp
#include <hip/hip_runtime.h>
#include <hip/hip_bf16.h>
#include <stdint.h>


typedef __attribute__((ext_vector_type(8))) short bf16x8;
typedef __attribute__((ext_vector_type(4))) short s16x4;
typedef __attribute__((ext_vector_type(16))) float f32x16;
typedef __attribute__((ext_vector_type(4))) float f32x4;
typedef __attribute__((ext_vector_type(4))) unsigned u32x4;
typedef __attribute__((ext_vector_type(2))) unsigned u32x2;
typedef __attribute__((address_space(3))) char lds_char;
typedef short v4i16_t __attribute__((ext_vector_type(4)));

constexpr int NB = 8, SQ = 128, DM = 512, NH = 32, SK = 2048, DH = 128;
constexpr int NT = SK / 64;
constexpr int LDS1 = 163840;
#define NPB 6
#define PBUF(c) (LDS1 - NPB * 16384 + (((c) + 4 * NPB - 16) % NPB) * 16384)
__device__ constexpr int tile_step(int n) { return n < 10 - NPB ? 2 * n : n + 6; }
__device__ constexpr int p_after(int c) {
  int mx = (c > 0 ? c : 0) - 1 + NPB; if (mx > 15) mx = 15;
  int n = 4 * (mx - (c + 1));
  for (int v = 2; v <= 6; v += 2) if (tile_step(v) < c && tile_step(v) >= c + 1 - NPB) n += 4;
  return n;
}
constexpr int XCH_OFF = 131072;
constexpr float CSC = 0.08838834764831845f * 1.4426950408889634f;

#define POL "nt"
#define MFMA32(a, b, c) __builtin_amdgcn_mfma_f32_32x32x16_bf16(a, b, c, 0, 0, 0)

typedef __attribute__((ext_vector_type(2))) float f32x2;
typedef __attribute__((ext_vector_type(2))) __bf16 bf16x2_t;
__device__ __forceinline__ unsigned cvtpk(float lo, float hi) {
  f32x2 v = {lo, hi};
  return __builtin_bit_cast(unsigned, __builtin_convertvector(v, bf16x2_t));
}
__device__ __forceinline__ bf16x8 pack8(float a0, float a1, float a2, float a3, float a4, float a5, float a6, float a7) {
  u32x4 w = {cvtpk(a0, a1), cvtpk(a2, a3), cvtpk(a4, a5), cvtpk(a6, a7)};
  return __builtin_bit_cast(bf16x8, w);
}
__device__ __forceinline__ bf16x8 lds_r128(lds_char* p) { return *(__attribute__((address_space(3))) bf16x8*)p; }
__device__ __forceinline__ s16x4 lds_tr(lds_char* p) {
  return __builtin_bit_cast(s16x4, __builtin_amdgcn_ds_read_tr16_b64_v4i16((__attribute__((address_space(3))) v4i16_t*)p));
}
__device__ __forceinline__ bf16x8 cat8(s16x4 a, s16x4 b) { return __builtin_shufflevector(a, b, 0, 1, 2, 3, 4, 5, 6, 7); }

__device__ __forceinline__ void dma4(unsigned o0, unsigned o1, unsigned o2, unsigned o3, const void* sbase, unsigned ldsaddr) {
  unsigned keep;
  asm volatile(
      "s_mov_b32 %0, m0\n\t"
      "s_mov_b32 m0, %6\n\t"
      "s_nop 0\n\t"
      "global_load_lds_dwordx4 %1, %5 " POL "\n\t"
      "global_load_lds_dwordx4 %2, %5 offset:1024 " POL "\n\t"
      "global_load_lds_dwordx4 %3, %5 offset:2048 " POL "\n\t"
      "global_load_lds_dwordx4 %4, %5 offset:3072 " POL "\n\t"
      "s_mov_b32 m0, %0"
      : "=&s"(keep) : "v"(o0), "v"(o1), "v"(o2), "v"(o3), "s"(sbase), "s"(ldsaddr) : "memory");
}
__device__ __forceinline__ void dma2(unsigned o0, unsigned o1, const void* sbase, unsigned ldsaddr) {
  unsigned keep;
  asm volatile(
      "s_mov_b32 %0, m0\n\t"
      "s_mov_b32 m0, %4\n\t"
      "s_nop 0\n\t"
      "global_load_lds_dwordx4 %1, %3\n\t"
      "global_load_lds_dwordx4 %2, %3 offset:1024\n\t"
      "s_mov_b32 m0, %0"
      : "=&s"(keep) : "v"(o0), "v"(o1), "s"(sbase), "s"(ldsaddr) : "memory");
}
template <int J> __device__ __forceinline__ void dmap(unsigned o, const void* sbase, unsigned ldsaddr) {
  unsigned keep;
  asm volatile("s_mov_b32 %0, m0\n\ts_mov_b32 m0, %3\n\ts_nop 0\n\tglobal_load_lds_dwordx4 %1, %2 offset:%4 " POL "\n\ts_mov_b32 m0, %0"
               : "=&s"(keep) : "v"(o), "s"(sbase), "s"(ldsaddr), "i"(J * 1024) : "memory");
}
template <int J> __device__ __forceinline__ void dmapw(unsigned o, const void* sbase, unsigned ldsaddr) {
  unsigned keep;
  asm volatile("s_mov_b32 %0, m0\n\ts_mov_b32 m0, %3\n\ts_nop 0\n\tglobal_load_lds_dwordx4 %1, %2 offset:%4\n\ts_mov_b32 m0, %0"
               : "=&s"(keep) : "v"(o), "s"(sbase), "s"(ldsaddr), "i"(J * 1024) : "memory");
}
__device__ __forceinline__ int lane_id() {
  int lane;
  asm volatile("v_mbcnt_lo_u32_b32 %0, -1, 0\n\tv_mbcnt_hi_u32_b32 %0, -1, %0" : "=v"(lane));
  return lane;
}
__device__ __forceinline__ float fadd_s(float a, float b) { float r; asm("v_add_f32_e32 %0, %1, %2" : "=v"(r) : "v"(a), "v"(b)); return r; }
#define GAP() __builtin_amdgcn_sched_barrier(0)
__device__ __forceinline__ int kslot(int i) { return i == 0 ? 0 : i * 32768 - 16384; }
__device__ __forceinline__ int vslot(int i) { return i == 4 ? 147456 : i * 32768 + 32768; }
#define WAITVM(n) asm volatile("s_waitcnt vmcnt(" #n ")" ::: "memory")
#define BARRIER() asm volatile("s_waitcnt lgkmcnt(0)\n\ts_barrier" ::: "memory")
#define PIN(x) asm volatile("" : "+v"(x))

__device__ __forceinline__ float swapmax(float v) {
  auto rr = __builtin_amdgcn_permlane32_swap(__float_as_uint(v), __float_as_uint(v), false, false);
  return fmaxf(__uint_as_float(rr[0]), __uint_as_float(rr[1]));
}
__device__ __forceinline__ float swapadd(float v) {
  auto rr = __builtin_amdgcn_permlane32_swap(__float_as_uint(v), __float_as_uint(v), false, false);
  return __uint_as_float(rr[0]) + __uint_as_float(rr[1]);
}

__global__ __launch_bounds__(512, 2) void fused_kernel(const __hip_bfloat16* __restrict__ x, const __hip_bfloat16* __restrict__ kc,
                                                       const __hip_bfloat16* __restrict__ vc, const __hip_bfloat16* __restrict__ wq,
                                                       const __hip_bfloat16* __restrict__ wo, char* ows, unsigned* flg,
                                                       __hip_bfloat16* __restrict__ out) {
  extern __shared__ __attribute__((aligned(16))) char smem[];
  lds_char* L = (lds_char*)smem;
  const unsigned L0 = (unsigned)(uintptr_t)smem;
  const int tid = threadIdx.x, lane = tid & 63;
  const int w = __builtin_amdgcn_readfirstlane(tid >> 6);
  const int qg = w & 3, kh = w >> 2;
  const int r32 = lane & 31, hi = lane >> 5;
  const int q4 = (lane & 15) >> 2, p4 = lane & 3, g1 = (lane >> 4) & 1;
  const int bid = blockIdx.x;
  const int head = 4 * (bid & 7) + ((bid >> 3) & 3), b = bid >> 5;
  const char* kbase = (const char*)kc + ((size_t)(b * NH + head) * SK) * 256;
  const char* vbase = (const char*)vc + ((size_t)(b * NH + head) * SK) * 256;
  const char* wqbase = (const char*)wq + head * 256;
#define FSTR 256
#define XA 2
  const int xcd = bid & 7, jx = bid >> 3;
  const int nblk = ((xcd & ((8 >> XA) - 1)) << XA) | (jx & ((1 << XA) - 1)), mblk = (xcd >> (3 - XA)) * (32 >> XA) + (jx >> XA);
  int fia = (mblk >> 2) * 32 + w;
  int fib = ((((4 * b) % (32 >> XA)) << XA) | (w & ((1 << XA) - 1))) * 8 + ((((4 * b) / (32 >> XA)) << (3 - XA)) | (w >> XA)) - (32 << XA);
  int fic = bid;
  asm volatile("" : "+s"(fia), "+s"(fib), "+s"(fic));
#define FOFF(lane) (((lane) < 8 ? ((lane) < 4 ? fia + 8 * (lane) : fib + ((lane) << (3 + XA))) : fic) * FSTR)
  unsigned fv;
  asm volatile("global_load_dword %0, %1, %2 sc1" : "=v"(fv) : "v"((unsigned)FOFF(lane)), "s"(flg) : "memory");

  const int drow = lane >> 4, dpos = lane & 15;
  unsigned ko[4];
#pragma unroll
  for (int j = 0; j < 4; ++j) ko[j] = (unsigned)(((w & 3) * 16 + drow) * 256 + ((dpos ^ (4 * j + drow)) << 4));
  const unsigned vo = (unsigned)(((w & 3) * 16 + drow) * 256 + ((dpos ^ (drow << 2)) << 4));
  unsigned wo2[2], xo2[2];
#pragma unroll
  for (int j = 0; j < 2; ++j) {
    wo2[j] = (unsigned)(((w & 3) * 8 + 4 * j + drow) * 8192 + ((dpos ^ (drow << 2)) << 4) - j * 1024);
    xo2[j] = (unsigned)(((w & 3) * 32 + 16 * j + (lane >> 2)) * 1024 + (((lane & 3) ^ drow) << 4) - j * 1024);
  }
  const char* xbase = (const char*)x + (size_t)b * SQ * 1024;

#define TOFF(t) ((size_t)(t) * 16384)
#define ISSUE_K(t, la) dma4(ko[0], ko[1], ko[2], ko[3], kbase + TOFF(t), (la) + (w & 3) * 4096)
#define ISSUE_V(t, la) dma4(vo, vo, vo, vo, vbase + TOFF(t), (la) + (w & 3) * 4096)
  const int crot = 2 * b + ((bid >> 3) & 1) + 8 * ((bid >> 4) & 1);
#define ISSUE_P(c) do { const int cc_ = ((c) + crot) & 15; \
    dma2(wo2[0], wo2[1], wqbase + (size_t)cc_ * 32 * 8192, L0 + PBUF(c) + (w & 3) * 2048); \
    dma2(xo2[0], xo2[1], xbase + cc_ * 64, L0 + PBUF(c) + 8192 + (w & 3) * 2048); } while (0)
#define ISSUE_T(c) do { \
    if (w >= 4) { \
      if (tile_step(2) == (c)) ISSUE_V(0, L0 + vslot(0)); \
      if (tile_step(4) == (c)) ISSUE_V(1, L0 + vslot(1)); \
      if (tile_step(6) == (c)) ISSUE_V(2, L0 + vslot(2)); \
    } else { \
      if (tile_step(0) == (c)) ISSUE_K(0, L0 + kslot(0)); \
      if (tile_step(1) == (c)) ISSUE_K(1, L0 + kslot(1)); \
      if (tile_step(3) == (c)) ISSUE_K(2, L0 + kslot(2)); \
      if (tile_step(5) == (c)) ISSUE_K(3, L0 + kslot(3)); \
      if (tile_step(7) == (c)) ISSUE_K(4, L0 + kslot(4)); \
    } } while (0)
  if (w >= 4) {
#pragma unroll
    for (int c = 0; c < NPB; ++c) ISSUE_P(c);
  }

  f32x16 qa[2] = {};
  {
    const int ps = ((p4 & 1) << 1) | (p4 >> 1);
    int wa[2], xa[2];
#pragma unroll
    for (int dd = 0; dd < 2; ++dd) {
      const int dblk = 2 * kh + dd;
      wa[dd] = (8 * hi + q4) * 256 + (((4 * dblk + 2 * g1 + (ps >> 1)) ^ (q4 << 2)) << 4) + 8 * (ps & 1);
      xa[dd] = 8192 + (qg * 32 + r32) * 64 + (((2 * dd + hi) ^ ((r32 >> 2) & 3)) << 4);
    }
    bf16x8 xf[2][2], af[2][2][2];
#pragma unroll
    for (int c = -1; c < 16; ++c) {
      if (w >= 4 && c < 15) asm volatile("s_waitcnt vmcnt(%0)" :: "i"(p_after(c)) : "memory");
      BARRIER();
      if (w >= 4 && c >= 0 && c + NPB < 16) ISSUE_P(c + NPB);
      ISSUE_T(c);
      if (c < 15) {
        lds_char* wb = L + PBUF(c + 1);
#pragma unroll
        for (int s = 0; s < 2; ++s) {
          xf[(c + 1) & 1][s] = lds_r128(wb + xa[s]);
#pragma unroll
          for (int dd = 0; dd < 2; ++dd)
            af[(c + 1) & 1][s][dd] = cat8(lds_tr(wb + wa[dd] + s * 4096), lds_tr(wb + wa[dd] + s * 4096 + 1024));
        }
      }
      GAP();
      if (c >= 0) {
#pragma unroll
        for (int s = 0; s < 2; ++s)
#pragma unroll
          for (int dd = 0; dd < 2; ++dd) qa[dd] = MFMA32(af[c & 1][s][dd], xf[c & 1][s], qa[dd]);
      }
      GAP();
    }
  }
  bf16x8 qf[8];
  unsigned f0[9];
  {
    bf16x8 own[4];
#pragma unroll
    for (int dd = 0; dd < 2; ++dd)
#pragma unroll
      for (int sp = 0; sp < 2; ++sp)
      {
        u32x4 t = __builtin_bit_cast(u32x4, pack8(qa[dd][8 * sp + 0], qa[dd][8 * sp + 1], qa[dd][8 * sp + 2], qa[dd][8 * sp + 3],
                                                  qa[dd][8 * sp + 4], qa[dd][8 * sp + 5], qa[dd][8 * sp + 6], qa[dd][8 * sp + 7]));
#pragma unroll
        for (int e = 0; e < 4; ++e) t[e] = cvtpk(__uint_as_float(t[e] << 16) * CSC, __uint_as_float(t[e] & 0xffff0000u) * CSC);
        own[dd * 2 + sp] = __builtin_bit_cast(bf16x8, t);
      }
#pragma unroll
    for (int i = 0; i < 4; ++i) *(__attribute__((address_space(3))) bf16x8*)(L + XCH_OFF + w * 4096 + i * 1024 + lane * 16) = own[i];
    if (w < 4) WAITVM(12);
    BARRIER();
    PIN(fv);
#pragma unroll
    for (int i = 0; i < 9; ++i) f0[i] = __builtin_amdgcn_readlane(fv, i);
#pragma unroll
    for (int i = 0; i < 4; ++i) {
      bf16x8 o = lds_r128(L + XCH_OFF + (w ^ 4) * 4096 + i * 1024 + lane * 16);
      if (kh == 0) { qf[i] = own[i]; qf[4 + i] = o; } else { qf[4 + i] = own[i]; qf[i] = o; }
    }
  }

  f32x16 oacc[4] = {};
  const int krow = kh * 32 + r32;
  const int kaddr0 = krow * 256 + ((hi ^ (krow & 15)) << 4);
  int vaddr0[4];
#pragma unroll
  for (int d = 0; d < 4; ++d) vaddr0[d] = (kh * 32 + 4 * hi + q4) * 256 + ((d ^ q4) << 6) + g1 * 32 + p4 * 8;

  bf16x8 kf[8], vf[4][2], pf[2];
  f32x16 sc = {}, sn, negm;
#pragma unroll
  for (int s = 0; s < 8; ++s) kf[s] = lds_r128(L + kslot(0) + (kaddr0 ^ (s << 5)));
#pragma unroll
  for (int s = 0; s < 8; ++s) sc = MFMA32(kf[s], qf[s], sc);
#pragma unroll
  for (int s = 0; s < 8; ++s) kf[s] = lds_r128(L + kslot(1) + (kaddr0 ^ (s << 5)));
  float m;
  {
    float tmax = sc[0];
#pragma unroll
    for (int r = 1; r < 16; ++r) tmax = fmaxf(tmax, sc[r]);
    m = swapmax(tmax);
#pragma unroll
    for (int r = 0; r < 16; ++r) { sc[r] -= m; negm[r] = -m; }
  }
  float l0 = 0.f, l1 = 0.f;
  const char* dbase = w < 4 ? kbase : vbase;
  unsigned dof0 = w < 4 ? ko[0] : vo, dof1 = w < 4 ? ko[1] : vo, dof2 = w < 4 ? ko[2] : vo, dof3 = w < 4 ? ko[3] : vo;
  const int dahead = w < 4 ? 6 : 4;
  int ks = kslot(2), vs = vslot(0), ksp = kslot(1), vsp = vslot(4);

#define RDV(d, s) vf[d][s] = cat8(lds_tr(vb + vaddr0[d] + (s) * 4096), lds_tr(vb + vaddr0[d] + (s) * 4096 + 2048))
#define RDK(s) kf[s] = lds_r128(kb + (kaddr0 ^ ((s) << 5)))
#define EXP(SC, r) SC[r] = __builtin_amdgcn_exp2f(SC[r])
#define CVT(SC, s, e) pw[s][e] = cvtpk(SC[8 * (s) + 2 * (e)], SC[8 * (s) + 2 * (e) + 1])
#define ADD(SC, r) do { l0 = fadd_s(l0, SC[r]); l1 = fadd_s(l1, SC[(r) + 1]); } while (0)
#define PVM(d, s) oacc[d] = MFMA32(vf[d][s], __builtin_bit_cast(bf16x8, pw[s]), oacc[d])
#define QKM(SN, s) SN = MFMA32(kf[s], qf[s], SN)
#define DMA(J, o) do { if (dmode == 1) dmap<J>(o, dptr, dla); else if (dmode == 2) dmapw<J>(o, dptr, dla); } while (0)
#define RESCALE(SN, tmax) do { \
    if (__builtin_amdgcn_ballot_w64(!(tmax <= 8.f)) != 0) { \
      const float mn = m + fmaxf(tmax, 0.f); \
      const float dl = mn - m; \
      const float alpha = __builtin_amdgcn_exp2f(-dl); \
      _Pragma("unroll") for (int d = 0; d < 4; ++d) oacc[d] *= alpha; \
      _Pragma("unroll") for (int r = 0; r < 16; ++r) { SN[r] -= dl; negm[r] = -mn; } \
      l0 *= alpha; l1 *= alpha; m = mn; \
    } } while (0)
#define BODY(SC, SN, HASK) do { \
    lds_char* vb = L + vs; \
    lds_char* kb = L + ks; \
    u32x4 pw[2]; \
    SN = MFMA32(kf[0], qf[0], negm); RDV(0, 0); RDV(1, 0); EXP(SC, 0); EXP(SC, 1); GAP(); \
    QKM(SN, 1); RDV(2, 0); RDV(3, 0); EXP(SC, 2); EXP(SC, 3); CVT(SC, 0, 0); GAP(); \
    QKM(SN, 2); RDV(0, 1); RDV(1, 1); EXP(SC, 4); EXP(SC, 5); CVT(SC, 0, 1); GAP(); \
    QKM(SN, 3); RDV(2, 1); RDV(3, 1); EXP(SC, 6); EXP(SC, 7); CVT(SC, 0, 2); GAP(); \
    QKM(SN, 4); DMA(0, dof0); EXP(SC, 8); EXP(SC, 9); CVT(SC, 0, 3); GAP(); \
    QKM(SN, 5); DMA(1, dof1); EXP(SC, 10); EXP(SC, 11); CVT(SC, 1, 0); GAP(); \
    QKM(SN, 6); DMA(2, dof2); EXP(SC, 12); EXP(SC, 13); CVT(SC, 1, 1); GAP(); \
    QKM(SN, 7); DMA(3, dof3); EXP(SC, 14); EXP(SC, 15); CVT(SC, 1, 2); GAP(); \
    PVM(0, 0); if (HASK) { RDK(0); RDK(1); } CVT(SC, 1, 3); ADD(SC, 0); ADD(SC, 2); GAP(); \
    PVM(1, 0); if (HASK) { RDK(2); RDK(3); } ADD(SC, 4); ADD(SC, 6); GAP(); \
    PVM(2, 0); if (HASK) { RDK(4); RDK(5); } ADD(SC, 8); ADD(SC, 10); GAP(); \
    PVM(3, 0); if (HASK) { RDK(6); RDK(7); } ADD(SC, 12); ADD(SC, 14); GAP(); \
    float t0 = fmaxf(fmaxf(SN[0], SN[1]), SN[2]), t1 = fmaxf(fmaxf(SN[3], SN[4]), SN[5]); \
    PVM(0, 1); t0 = fmaxf(fmaxf(t0, SN[6]), SN[7]); t1 = fmaxf(fmaxf(t1, SN[8]), SN[9]); GAP(); \
    PVM(1, 1); t0 = fmaxf(fmaxf(t0, SN[10]), SN[11]); t1 = fmaxf(fmaxf(t1, SN[12]), SN[13]); GAP(); \
    PVM(2, 1); t0 = fmaxf(fmaxf(t0, SN[14]), SN[15]); t0 = swapmax(fmaxf(t0, t1)); GAP(); \
    PVM(3, 1); GAP(); \
    RESCALE(SN, t0); \
    ksp = ks; vsp = vs; \
    ks = ks == 0 ? 16384 : (ks == 114688 ? 0 : ks + 32768); \
    vs = vs == 131072 ? 147456 : (vs == 147456 ? 32768 : vs + 32768); } while (0)

#define PFAT 27
  unsigned pfv;
#define WOPF(it) do { \
    if ((it) == PFAT) { pfv = (unsigned)(w * 512 + (jx >> XA) * 64 + lane_id()) * 1024u; \
      asm volatile("global_load_dword %0, %0, %1" : "+v"(pfv) : "s"((const char*)wo + nblk * 128) : "memory"); } \
    if ((it) == PFAT + 4) PIN(pfv); } while (0)
  {
    WAITVM(8);
    BARRIER();
    if (w < 4) { ISSUE_K(5, L0 + kslot(0)); ISSUE_K(6, L0 + kslot(1)); }
    else { ISSUE_V(3, L0 + vslot(3)); ISSUE_V(4, L0 + vslot(4)); }
    const int dmode = 0; const char* dptr = dbase; const unsigned dla = 0;
    BODY(sc, sn, true);
  }
#define DSETUP(it) \
    const char* dptr = dbase + TOFF((it) + dahead); \
    const unsigned dla = L0 + (w < 4 ? ksp : vsp) + (w & 3) * 4096;
#pragma unroll 1
  for (int it = 1; it < 25; it += 2) {
    { WAITVM(12); BARRIER(); const int dmode = 1; DSETUP(it); BODY(sn, sc, true); }
    { WAITVM(12); BARRIER(); const int dmode = 1; DSETUP(it + 1); BODY(sc, sn, true); }
  }
  const char* wot = (const char*)wo + nblk * 128 + (size_t)(w & 3) * (8 * 131072) + (3 * 131072 - 24 * 16384);
#define WOFFS() do { const int ln_ = lane_id(), dr_ = ln_ >> 3; \
    dof0 = (unsigned)(dr_ * 1024 + (((ln_ & 7) ^ (((dr_ >> 1) & 1) << 2)) << 4)); \
    dof1 = dof0 + 7168; dof2 = dof0 + 4 * 131072 - 2048; dof3 = dof0 + 4 * 131072 + 8192 - 3072; } while (0)
#define TSETUP(it, ku, vu) \
    asm volatile("" : "+s"(ks), "+s"(vs), "+s"(ksp), "+s"(vsp)); \
    const int dmode = w < 4 ? ((it) + 6 < NT ? 1 : (ku) >= 0 ? 2 : 0) : ((it) + 4 < NT ? 1 : (vu) >= 0 ? 2 : 0); \
    const char* dptr = dmode == 1 ? dbase + TOFF((it) + dahead) : wot + (w < 4 ? (ku) : (vu)) * 16384; \
    const unsigned dla = L0 + (w < 4 ? ksp : vsp) + (w & 3) * 4096;
  { WAITVM(12); BARRIER(); WOPF(25); TSETUP(25, -1, -1); BODY(sn, sc, true); }
  if (w < 4) WOFFS();
  { WAITVM(12); BARRIER(); WOPF(26); TSETUP(26, 24, -1); BODY(sc, sn, true); }
  { WAITVM(12); BARRIER(); WOPF(27); TSETUP(27, 25, -1); BODY(sn, sc, true); }
  if (w >= 4) WOFFS();
  { WAITVM(12); BARRIER(); WOPF(28); TSETUP(28, 26, 27); BODY(sc, sn, true); }
  { WAITVM(12); BARRIER(); WOPF(29); TSETUP(29, 28, 29); BODY(sn, sc, true); }
  { WAITVM(12); BARRIER(); WOPF(30); TSETUP(30, 30, 31); BODY(sc, sn, false); }
  asm volatile("" : "+s"(vs));

  const int ln = lane_id();
  const int r32e = ln & 31, hie = ln >> 5;
  const unsigned wof0 = (unsigned)((ln >> 3) * 1024 + (((ln & 7) ^ ((((ln >> 3) >> 1) & 1) << 2)) << 4)), wof1 = wof0 + 7168;
  const int tw0 = w * 2048 + (4 * hie + ((ln & 15) >> 2)) * 128 + ((((ln & 15) >> 3)) << 6) + ((ln >> 4) & 1) * 32 + (ln & 3) * 8;
  const int tw1 = tw0 ^ 64;
  const char* wob = (const char*)wo + nblk * 128 + (size_t)w * (4 * 131072);
#define CELL(i) ((i) == 0 ? 49152 : (i) == 1 ? 81920 : (i) == 2 ? 114688 : (i) == 3 ? 98304 : (i) == 4 ? 0 : \
                 (i) == 5 ? 131072 : (i) == 6 ? 16384 : (i) == 7 ? 147456 : (i) == 8 ? 32768 : 65536)
#define RDW(dst, i) do { dst[0] = cat8(lds_tr(L + CELL(i) + tw0), lds_tr(L + CELL(i) + tw0 + 1024)); \
                         dst[1] = cat8(lds_tr(L + CELL(i) + tw1), lds_tr(L + CELL(i) + tw1 + 1024)); } while (0)
#define WAITL() asm volatile("s_waitcnt lgkmcnt(0)" ::: "memory")
  bf16x8 A[8][2];
  const unsigned foff = (unsigned)FOFF(ln);
  unsigned pv;
#define LDFLAGS() asm volatile("global_load_dword %0, %1, %2 sc1" : "=v"(pv) : "v"(foff), "s"(flg) : "memory")
  {
    WAITVM(8);
    BARRIER();
    WOPF(31);
    lds_char* vb = L + vs;
    u32x4 pw[2];
#pragma unroll
    for (int s = 0; s < 2; ++s)
#pragma unroll
      for (int d = 0; d < 4; ++d) RDV(d, s);
    RDW(A[0], 0); RDW(A[1], 1); RDW(A[2], 2); RDW(A[3], 3);
#pragma unroll
    for (int r = 0; r < 16; ++r) EXP(sn, r);
#pragma unroll
    for (int r = 0; r < 16; r += 2) ADD(sn, r);
#pragma unroll
    for (int s = 0; s < 2; ++s)
#pragma unroll
      for (int e = 0; e < 4; ++e) CVT(sn, s, e);
#pragma unroll
    for (int s = 0; s < 2; ++s)
#pragma unroll
      for (int d = 0; d < 4; ++d) PVM(d, s);
  }

  const char* ob = ows + ((size_t)mblk * 256 + w * 32) * 1024;
  unsigned f0v = f0[0];
#pragma unroll
  for (int i = 1; i < 8; ++i) asm volatile("v_writelane_b32 %0, %1, %2" : "+v"(f0v) : "s"(f0[i]), "i"(i));
  unsigned rdy;
  bf16x8 of[8], Bq[24][2];
  const unsigned oo = ln * 16;
#define HORD(g) ((g) == 0 ? 0 : (g) == 1 ? 2 : (g) == 2 ? 1 : 3)
#define GETRDY() do { PIN(pv); rdy = (unsigned)__builtin_amdgcn_ballot_w64(pv != f0v) & 255u; } while (0)
#define POLL(cond) do { if (!(cond)) { \
      int spins_ = 0; \
      do { __builtin_amdgcn_s_sleep(2); unsigned pq_; \
        asm volatile("global_load_dword %0, %1, %2 sc1\n\ts_waitcnt vmcnt(0)" : "=v"(pq_) : "v"(foff), "s"(flg) : "memory"); \
        rdy = (unsigned)__builtin_amdgcn_ballot_w64(pq_ != f0v) & 255u; \
      } while (!(cond) && ++spins_ < (1 << 18)); } } while (0)
#define PUBLISH(v) do { BARRIER(); if (w == 0 && ln == 0) \
    __hip_atomic_store((__attribute__((address_space(1))) unsigned*)(flg + fic * (FSTR / 4)), f0[8] + (v), __ATOMIC_RELAXED, __HIP_MEMORY_SCOPE_AGENT); } while (0)
#define RING(j) ((j) % 10 == 0 ? 9 : (j) % 10 < 6 ? (j) % 10 + 3 : (j) % 10 - 6)
#define LDO(j) asm volatile("global_load_dwordx4 %0, %1, %2 offset:%3 sc1" : "=v"(of[(j) & 7]) : "v"(oo), \
      "s"(ob + HORD((j) >> 3) * 8192 + (((j) >> 2) & 1) * 4096), "i"(((j) & 3) * 1024) : "memory")
#define DMAU(u) dma2(wof0, wof1, wob + HORD((u) >> 3) * 131072 + ((u) & 7) * 16384, L0 + CELL(RING(u)) + w * 2048)
#define WAITN(n) asm volatile("s_waitcnt vmcnt(%0)" :: "i"(n) : "memory")

  const float l = swapadd(l0 + l1);
  {
    float* ml = (float*)(smem + CELL(8));
    if (hie == 0) { ml[w * 512 + r32e] = m; ml[w * 512 + 32 + r32e] = l; }
#pragma unroll
    for (int dd = 0; dd < 2; ++dd)
#pragma unroll
      for (int g = 0; g < 4; ++g) {
        f32x4 v;
        if (kh) { v[0] = oacc[dd][4 * g]; v[1] = oacc[dd][4 * g + 1]; v[2] = oacc[dd][4 * g + 2]; v[3] = oacc[dd][4 * g + 3]; }
        else { v[0] = oacc[2 + dd][4 * g]; v[1] = oacc[2 + dd][4 * g + 1]; v[2] = oacc[2 + dd][4 * g + 2]; v[3] = oacc[2 + dd][4 * g + 3]; }
        *(__attribute__((address_space(3))) f32x4*)(L + CELL(dd * 2 + (g >> 1)) + w * 2048 + (g & 1) * 1024 + ln * 16) = v;
      }
    WAITVM(0);
    BARRIER();
    DMAU(0);
    RDW(A[4], 4); RDW(A[5], 5); RDW(A[6], 6); RDW(A[7], 7);
    const float mp = ml[(w ^ 4) * 512 + r32e], lp = ml[(w ^ 4) * 512 + 32 + r32e];
    WAITL();
    DMAU(1); DMAU(2); DMAU(3); DMAU(4);
    const float mf = fmaxf(m, mp);
    const float wa = __builtin_amdgcn_exp2f(m - mf), wb = __builtin_amdgcn_exp2f(mp - mf);
    const float inv = 1.f / (wa * l + wb * lp);
    const float fa = wa * inv, fb = wb * inv;
    const char* ost = ows + ((size_t)(b * 4 + qg) * 256 + head * 8 + 4 * kh) * 1024;
#pragma unroll
    for (int dd = 0; dd < 2; ++dd) {
      float o[16];
#pragma unroll
      for (int g = 0; g < 4; ++g) {
        f32x4 v = *(__attribute__((address_space(3))) f32x4*)(L + CELL(dd * 2 + (g >> 1)) + (w ^ 4) * 2048 + (g & 1) * 1024 + ln * 16);
#pragma unroll
        for (int e = 0; e < 4; ++e) {
          const float own = kh ? oacc[2 + dd][4 * g + e] : oacc[dd][4 * g + e];
          o[4 * g + e] = own * fa + v[e] * fb;
        }
      }
#pragma unroll
      for (int sp = 0; sp < 2; ++sp) {
        bf16x8 f = pack8(o[8 * sp + 0], o[8 * sp + 1], o[8 * sp + 2], o[8 * sp + 3], o[8 * sp + 4], o[8 * sp + 5], o[8 * sp + 6], o[8 * sp + 7]);
        asm volatile("global_store_dwordx4 %0, %1, %2 offset:%3 sc1\n\ts_nop 1" :: "v"((unsigned)(ln * 16)), "v"(f), "s"(ost), "i"((dd * 2 + sp) * 1024) : "memory");
      }
    }
    BARRIER();
  }
  DMAU(5); DMAU(6); DMAU(7); DMAU(8); DMAU(9);
#define ASTEP(u, n) do { WAITN(n); RDW(Bq[(u) - 10], RING(u)); WAITL(); DMAU(u); } while (0)
#define MSTEP(j, n) do { WAITN(n); PIN(of[(j) & 7]); \
    if ((j) < 24) { acc[0] = MFMA32(Bq[(j) % 24][0], of[(j) & 7], acc[0]); acc[1] = MFMA32(Bq[(j) % 24][1], of[(j) & 7], acc[1]); } \
    else { acc[0] = MFMA32(A[(j) & 7][0], of[(j) & 7], acc[0]); acc[1] = MFMA32(A[(j) & 7][1], of[(j) & 7], acc[1]); } } while (0)
  f32x16 acc[2] = {};
  ASTEP(10, 22); ASTEP(11, 22); WAITN(14); PUBLISH(1u); ASTEP(12, 22); ASTEP(13, 22); ASTEP(14, 22); ASTEP(15, 18); ASTEP(16, 18); ASTEP(17, 18); LDFLAGS(); ASTEP(18, 19); ASTEP(19, 19); ASTEP(20, 19); ASTEP(21, 19);
  WAITN(8); GETRDY();
  POLL((rdy >> HORD(0)) & 1u);
  LDO(0); LDO(1); LDO(2); LDO(3); LDO(4); LDO(5); LDO(6); LDO(7);
  MSTEP(0, 7); RDW(Bq[12], RING(22)); WAITL(); DMAU(22); POLL((rdy >> HORD(1)) & 1u); LDFLAGS(); LDO(8); GAP();
  MSTEP(1, 10); RDW(Bq[13], RING(23)); WAITL(); DMAU(23); LDO(9); GAP();
  MSTEP(2, 12); RDW(Bq[14], RING(14)); LDO(10); GAP();
  MSTEP(3, 12); RDW(Bq[15], RING(15)); LDO(11); GAP();
  MSTEP(4, 12); RDW(Bq[16], RING(16)); LDO(12); GAP();
  MSTEP(5, 12); RDW(Bq[17], RING(17)); LDO(13); GAP();
  MSTEP(6, 12); RDW(Bq[18], RING(18)); LDO(14); GAP();
  MSTEP(7, 12); RDW(Bq[19], RING(19)); LDO(15); GAP();
  MSTEP(8, 9); RDW(Bq[20], RING(20)); GETRDY(); POLL((rdy >> HORD(2)) & 1u); LDFLAGS(); LDO(16); GAP();
  MSTEP(9, 8); RDW(Bq[21], RING(21)); LDO(17); GAP();
  MSTEP(10, 8); RDW(Bq[22], RING(22)); LDO(18); GAP();
  MSTEP(11, 8); RDW(Bq[23], RING(23)); LDO(19); GAP();
  MSTEP(12, 8); LDO(20); GAP();
  MSTEP(13, 8); LDO(21); GAP();
  MSTEP(14, 8); LDO(22); GAP();
  MSTEP(15, 8); LDO(23); GAP();
  MSTEP(16, 7); GETRDY(); POLL((rdy >> HORD(3)) & 1u); LDFLAGS(); LDO(24); GAP();
  MSTEP(17, 8); LDO(25); GAP();
  MSTEP(18, 8); LDO(26); GAP();
  MSTEP(19, 8); LDO(27); GAP();
  MSTEP(20, 8); LDO(28); GAP();
  MSTEP(21, 8); LDO(29); GAP();
  MSTEP(22, 8); LDO(30); GAP();
  MSTEP(23, 8); LDO(31); GAP();
  MSTEP(24, 7); GAP();
  MSTEP(25, 6); GAP();
  MSTEP(26, 5); GAP();
  MSTEP(27, 4); GAP();
  MSTEP(28, 3); GAP();
  MSTEP(29, 2); GAP();
  MSTEP(30, 1); GAP();
  MSTEP(31, 0); GAP();
  GETRDY();
#pragma unroll
  for (int nb = 0; nb < 2; ++nb)
#pragma unroll
    for (int g = 0; g < 4; ++g) {
      f32x4 v = {acc[nb][4 * g], acc[nb][4 * g + 1], acc[nb][4 * g + 2], acc[nb][4 * g + 3]};
      *(__attribute__((address_space(3))) f32x4*)(L + CELL(nb * 2 + (g >> 1)) + w * 2048 + (g & 1) * 1024 + ln * 16) = v;
    }
  POLL((rdy & 0xF0u) == 0xF0u);
  PUBLISH(2u);
  {
    const int cb = (w >> 1) == 0 ? CELL(0) : (w >> 1) == 1 ? CELL(1) : (w >> 1) == 2 ? CELL(2) : CELL(3);
    f32x4 s = {0.f, 0.f, 0.f, 0.f};
#pragma unroll
    for (int ww = 0; ww < 8; ++ww) s += *(__attribute__((address_space(3))) f32x4*)(L + cb + ww * 2048 + (w & 1) * 1024 + ln * 16);
    const int nb = w >> 2, g = w & 3;
    u32x2 pk = {cvtpk(s[0], s[1]), cvtpk(s[2], s[3])};
    *(u32x2*)((char*)out + ((size_t)(mblk * 32 + r32e) * 512 + nblk * 64 + nb * 32 + 8 * g + 4 * hie) * 2) = pk;
  }
}

extern "C" void kernel_launch(void* const* d_in, const int* in_sizes, int n_in, void* d_out, int out_size, void* d_ws, size_t ws_size,
                              hipStream_t stream) {
  const __hip_bfloat16* x = (const __hip_bfloat16*)d_in[0];
  const __hip_bfloat16* kc = (const __hip_bfloat16*)d_in[1];
  const __hip_bfloat16* vc = (const __hip_bfloat16*)d_in[2];
  const __hip_bfloat16* wq = (const __hip_bfloat16*)d_in[3];
  const __hip_bfloat16* wo = (const __hip_bfloat16*)d_in[4];
  __hip_bfloat16* out = (__hip_bfloat16*)d_out;
  (void)hipFuncSetAttribute((const void*)fused_kernel, hipFuncAttributeMaxDynamicSharedMemorySize, LDS1);
  fused_kernel<<<256, 512, LDS1, stream>>>(x, kc, vc, wq, wo, (char*)d_ws, (unsigned*)((char*)d_ws + (16u << 20)), out);
}
```

```cpp
#define P1 1040
#define N1 6
#define DP 1000
#define P2 880
#define PSLACK 400
#include <hip/hip_runtime.h>
#include <hip/hip_bf16.h>
#include <stdint.h>


typedef __attribute__((ext_vector_type(8))) short bf16x8;
typedef __attribute__((ext_vector_type(4))) short s16x4;
typedef __attribute__((ext_vector_type(16))) float f32x16;
typedef __attribute__((ext_vector_type(4))) float f32x4;
typedef __attribute__((ext_vector_type(4))) unsigned u32x4;
typedef __attribute__((ext_vector_type(2))) unsigned u32x2;
typedef __attribute__((address_space(3))) char lds_char;
typedef short v4i16_t __attribute__((ext_vector_type(4)));

constexpr int NB = 8, SQ = 128, DM = 512, NH = 32, SK = 2048, DH = 128;
constexpr int NT = SK / 64;
constexpr int LDS1 = 163840;
#define NPB 6
#define PBUF(c) (LDS1 - NPB * 16384 + (((c) + 4 * NPB - 16) % NPB) * 16384)
__device__ constexpr int tile_step(int n) { return n < 10 - NPB ? 2 * n : n + 6; }
__device__ constexpr int p_after(int c) {
  int mx = (c > 0 ? c : 0) - 1 + NPB; if (mx > 15) mx = 15;
  int n = 4 * (mx - (c + 1));
  for (int v = 2; v <= 6; v += 2) if (tile_step(v) < c && tile_step(v) >= c + 1 - NPB) n += 4;
  return n;
}
constexpr int XCH_OFF = 131072;
constexpr float CSC = 0.08838834764831845f * 1.4426950408889634f;

#define POL "nt"
#define MFMA32(a, b, c) __builtin_amdgcn_mfma_f32_32x32x16_bf16(a, b, c, 0, 0, 0)

typedef __attribute__((ext_vector_type(2))) float f32x2;
typedef __attribute__((ext_vector_type(2))) __bf16 bf16x2_t;
__device__ __forceinline__ unsigned cvtpk(float lo, float hi) {
  f32x2 v = {lo, hi};
  return __builtin_bit_cast(unsigned, __builtin_convertvector(v, bf16x2_t));
}
__device__ __forceinline__ bf16x8 pack8(float a0, float a1, float a2, float a3, float a4, float a5, float a6, float a7) {
  u32x4 w = {cvtpk(a0, a1), cvtpk(a2, a3), cvtpk(a4, a5), cvtpk(a6, a7)};
  return __builtin_bit_cast(bf16x8, w);
}
__device__ __forceinline__ bf16x8 lds_r128(lds_char* p) { return *(__attribute__((address_space(3))) bf16x8*)p; }
__device__ __forceinline__ s16x4 lds_tr(lds_char* p) {
  return __builtin_bit_cast(s16x4, __builtin_amdgcn_ds_read_tr16_b64_v4i16((__attribute__((address_space(3))) v4i16_t*)p));
}
__device__ __forceinline__ bf16x8 cat8(s16x4 a, s16x4 b) { return __builtin_shufflevector(a, b, 0, 1, 2, 3, 4, 5, 6, 7); }

__device__ __forceinline__ void dma4(unsigned o0, unsigned o1, unsigned o2, unsigned o3, const void* sbase, unsigned ldsaddr) {
  unsigned keep;
  asm volatile(
      "s_mov_b32 %0, m0\n\t"
      "s_mov_b32 m0, %6\n\t"
      "s_nop 0\n\t"
      "global_load_lds_dwordx4 %1, %5 " POL "\n\t"
      "global_load_lds_dwordx4 %2, %5 offset:1024 " POL "\n\t"
      "global_load_lds_dwordx4 %3, %5 offset:2048 " POL "\n\t"
      "global_load_lds_dwordx4 %4, %5 offset:3072 " POL "\n\t"
      "s_mov_b32 m0, %0"
      : "=&s"(keep) : "v"(o0), "v"(o1), "v"(o2), "v"(o3), "s"(sbase), "s"(ldsaddr) : "memory");
}
__device__ __forceinline__ void dma2(unsigned o0, unsigned o1, const void* sbase, unsigned ldsaddr) {
  unsigned keep;
  asm volatile(
      "s_mov_b32 %0, m0\n\t"
      "s_mov_b32 m0, %4\n\t"
      "s_nop 0\n\t"
      "global_load_lds_dwordx4 %1, %3\n\t"
      "global_load_lds_dwordx4 %2, %3 offset:1024\n\t"
      "s_mov_b32 m0, %0"
      : "=&s"(keep) : "v"(o0), "v"(o1), "s"(sbase), "s"(ldsaddr) : "memory");
}
template <int J> __device__ __forceinline__ void dmap(unsigned o, const void* sbase, unsigned ldsaddr) {
  unsigned keep;
  asm volatile("s_mov_b32 %0, m0\n\ts_mov_b32 m0, %3\n\ts_nop 0\n\tglobal_load_lds_dwordx4 %1, %2 offset:%4 " POL "\n\ts_mov_b32 m0, %0"
               : "=&s"(keep) : "v"(o), "s"(sbase), "s"(ldsaddr), "i"(J * 1024) : "memory");
}
template <int J> __device__ __forceinline__ void dmapw(unsigned o, const void* sbase, unsigned ldsaddr) {
  unsigned keep;
  asm volatile("s_mov_b32 %0, m0\n\ts_mov_b32 m0, %3\n\ts_nop 0\n\tglobal_load_lds_dwordx4 %1, %2 offset:%4\n\ts_mov_b32 m0, %0"
               : "=&s"(keep) : "v"(o), "s"(sbase), "s"(ldsaddr), "i"(J * 1024) : "memory");
}
__device__ __forceinline__ int lane_id() {
  int lane;
  asm volatile("v_mbcnt_lo_u32_b32 %0, -1, 0\n\tv_mbcnt_hi_u32_b32 %0, -1, %0" : "=v"(lane));
  return lane;
}
__device__ __forceinline__ float fadd_s(float a, float b) { float r; asm("v_add_f32_e32 %0, %1, %2" : "=v"(r) : "v"(a), "v"(b)); return r; }
#define GAP() __builtin_amdgcn_sched_barrier(0)
__device__ __forceinline__ int kslot(int i) { return i == 0 ? 0 : i * 32768 - 16384; }
__device__ __forceinline__ int vslot(int i) { return i == 4 ? 147456 : i * 32768 + 32768; }
#define WAITVM(n) asm volatile("s_waitcnt vmcnt(" #n ")" ::: "memory")
#define BARRIER() asm volatile("s_waitcnt lgkmcnt(0)\n\ts_barrier" ::: "memory")
#define PIN(x) asm volatile("" : "+v"(x))

__device__ __forceinline__ float swapmax(float v) {
  auto rr = __builtin_amdgcn_permlane32_swap(__float_as_uint(v), __float_as_uint(v), false, false);
  return fmaxf(__uint_as_float(rr[0]), __uint_as_float(rr[1]));
}
__device__ __forceinline__ float swapadd(float v) {
  auto rr = __builtin_amdgcn_permlane32_swap(__float_as_uint(v), __float_as_uint(v), false, false);
  return __uint_as_float(rr[0]) + __uint_as_float(rr[1]);
}

__global__ __launch_bounds__(512, 2) void fused_kernel(const __hip_bfloat16* __restrict__ x, const __hip_bfloat16* __restrict__ kc,
                                                       const __hip_bfloat16* __restrict__ vc, const __hip_bfloat16* __restrict__ wq,
                                                       const __hip_bfloat16* __restrict__ wo, char* ows, unsigned* flg,
                                                       __hip_bfloat16* __restrict__ out) {
  extern __shared__ __attribute__((aligned(16))) char smem[];
  lds_char* L = (lds_char*)smem;
  const unsigned L0 = (unsigned)(uintptr_t)smem;
  const int tid = threadIdx.x, lane = tid & 63;
  const int w = __builtin_amdgcn_readfirstlane(tid >> 6);
  const int qg = w & 3, kh = w >> 2;
  const int r32 = lane & 31, hi = lane >> 5;
  const int q4 = (lane & 15) >> 2, p4 = lane & 3, g1 = (lane >> 4) & 1;
  const int bid = blockIdx.x;
  const int head = 4 * (bid & 7) + ((bid >> 3) & 3), b = bid >> 5;
  const char* kbase = (const char*)kc + ((size_t)(b * NH + head) * SK) * 256;
  const char* vbase = (const char*)vc + ((size_t)(b * NH + head) * SK) * 256;
  const char* wqbase = (const char*)wq + head * 256;
#define FSTR 256
#define XA 2
  const int xcd = bid & 7, jx = bid >> 3;
  const int nblk = ((xcd & ((8 >> XA) - 1)) << XA) | (jx & ((1 << XA) - 1)), mblk = (xcd >> (3 - XA)) * (32 >> XA) + (jx >> XA);
  int fia = (mblk >> 2) * 32 + w;
  int fib = ((((4 * b) % (32 >> XA)) << XA) | (w & ((1 << XA) - 1))) * 8 + ((((4 * b) / (32 >> XA)) << (3 - XA)) | (w >> XA)) - (32 << XA);
  int fic = bid;
  asm volatile("" : "+s"(fia), "+s"(fib), "+s"(fic));
#define FOFF(lane) (((lane) < 8 ? ((lane) < 4 ? fia + 8 * (lane) : fib + ((lane) << (3 + XA))) : fic) * FSTR)
  unsigned fv;
  asm volatile("global_load_dword %0, %1, %2 sc1" : "=v"(fv) : "v"((unsigned)FOFF(lane)), "s"(flg) : "memory");

  const int drow = lane >> 4, dpos = lane & 15;
  unsigned ko[4];
#pragma unroll
  for (int j = 0; j < 4; ++j) ko[j] = (unsigned)(((w & 3) * 16 + drow) * 256 + ((dpos ^ (4 * j + drow)) << 4));
  const unsigned vo = (unsigned)(((w & 3) * 16 + drow) * 256 + ((dpos ^ (drow << 2)) << 4));
  unsigned wo2[2], xo2[2];
#pragma unroll
  for (int j = 0; j < 2; ++j) {
    wo2[j] = (unsigned)(((w & 3) * 8 + 4 * j + drow) * 8192 + ((dpos ^ (drow << 2)) << 4) - j * 1024);
    xo2[j] = (unsigned)(((w & 3) * 32 + 16 * j + (lane >> 2)) * 1024 + (((lane & 3) ^ drow) << 4) - j * 1024);
  }
  const char* xbase = (const char*)x + (size_t)b * SQ * 1024;

#define TOFF(t) ((size_t)(t) * 16384)
#define ISSUE_K(t, la) dma4(ko[0], ko[1], ko[2], ko[3], kbase + TOFF(t), (la) + (w & 3) * 4096)
#define ISSUE_V(t, la) dma4(vo, vo, vo, vo, vbase + TOFF(t), (la) + (w & 3) * 4096)
  const int crot = 2 * b + ((bid >> 3) & 1) + 8 * ((bid >> 4) & 1);
#define ISSUE_P(c) do { const int cc_ = ((c) + crot) & 15; \
    dma2(wo2[0], wo2[1], wqbase + (size_t)cc_ * 32 * 8192, L0 + PBUF(c) + (w & 3) * 2048); \
    dma2(xo2[0], xo2[1], xbase + cc_ * 64, L0 + PBUF(c) + 8192 + (w & 3) * 2048); } while (0)
#define ISSUE_T(c) do { \
    if (w >= 4) { \
      if (tile_step(2) == (c)) ISSUE_V(0, L0 + vslot(0)); \
      if (tile_step(4) == (c)) ISSUE_V(1, L0 + vslot(1)); \
      if (tile_step(6) == (c)) ISSUE_V(2, L0 + vslot(2)); \
    } else { \
      if (tile_step(0) == (c)) ISSUE_K(0, L0 + kslot(0)); \
      if (tile_step(1) == (c)) ISSUE_K(1, L0 + kslot(1)); \
      if (tile_step(3) == (c)) ISSUE_K(2, L0 + kslot(2)); \
      if (tile_step(5) == (c)) ISSUE_K(3, L0 + kslot(3)); \
      if (tile_step(7) == (c)) ISSUE_K(4, L0 + kslot(4)); \
    } } while (0)
  if (w >= 4) {
#pragma unroll
    for (int c = 0; c < NPB; ++c) ISSUE_P(c);
  }

  f32x16 qa[2] = {};
  {
    const int ps = ((p4 & 1) << 1) | (p4 >> 1);
    int wa[2], xa[2];
#pragma unroll
    for (int dd = 0; dd < 2; ++dd) {
      const int dblk = 2 * kh + dd;
      wa[dd] = (8 * hi + q4) * 256 + (((4 * dblk + 2 * g1 + (ps >> 1)) ^ (q4 << 2)) << 4) + 8 * (ps & 1);
      xa[dd] = 8192 + (qg * 32 + r32) * 64 + (((2 * dd + hi) ^ ((r32 >> 2) & 3)) << 4);
    }
    bf16x8 xf[2][2], af[2][2][2];
#pragma unroll
    for (int c = -1; c < 16; ++c) {
      if (w >= 4 && c < 15) asm volatile("s_waitcnt vmcnt(%0)" :: "i"(p_after(c)) : "memory");
      BARRIER();
      if (w >= 4 && c >= 0 && c + NPB < 16) ISSUE_P(c + NPB);
      ISSUE_T(c);
      if (c < 15) {
        lds_char* wb = L + PBUF(c + 1);
#pragma unroll
        for (int s = 0; s < 2; ++s) {
          xf[(c + 1) & 1][s] = lds_r128(wb + xa[s]);
#pragma unroll
          for (int dd = 0; dd < 2; ++dd)
            af[(c + 1) & 1][s][dd] = cat8(lds_tr(wb + wa[dd] + s * 4096), lds_tr(wb + wa[dd] + s * 4096 + 1024));
        }
      }
      GAP();
      if (c >= 0) {
#pragma unroll
        for (int s = 0; s < 2; ++s)
#pragma unroll
          for (int dd = 0; dd < 2; ++dd) qa[dd] = MFMA32(af[c & 1][s][dd], xf[c & 1][s], qa[dd]);
      }
      GAP();
    }
  }
  bf16x8 qf[8];
  unsigned f0[9];
  {
    bf16x8 own[4];
#pragma unroll
    for (int dd = 0; dd < 2; ++dd)
#pragma unroll
      for (int sp = 0; sp < 2; ++sp)
      {
        u32x4 t = __builtin_bit_cast(u32x4, pack8(qa[dd][8 * sp + 0], qa[dd][8 * sp + 1], qa[dd][8 * sp + 2], qa[dd][8 * sp + 3],
                                                  qa[dd][8 * sp + 4], qa[dd][8 * sp + 5], qa[dd][8 * sp + 6], qa[dd][8 * sp + 7]));
#pragma unroll
        for (int e = 0; e < 4; ++e) t[e] = cvtpk(__uint_as_float(t[e] << 16) * CSC, __uint_as_float(t[e] & 0xffff0000u) * CSC);
        own[dd * 2 + sp] = __builtin_bit_cast(bf16x8, t);
      }
#pragma unroll
    for (int i = 0; i < 4; ++i) *(__attribute__((address_space(3))) bf16x8*)(L + XCH_OFF + w * 4096 + i * 1024 + lane * 16) = own[i];
    if (w < 4) WAITVM(12);
    BARRIER();
    PIN(fv);
#pragma unroll
    for (int i = 0; i < 9; ++i) f0[i] = __builtin_amdgcn_readlane(fv, i);
#pragma unroll
    for (int i = 0; i < 4; ++i) {
      bf16x8 o = lds_r128(L + XCH_OFF + (w ^ 4) * 4096 + i * 1024 + lane * 16);
      if (kh == 0) { qf[i] = own[i]; qf[4 + i] = o; } else { qf[4 + i] = own[i]; qf[i] = o; }
    }
  }

  f32x16 oacc[4] = {};
  const int krow = kh * 32 + r32;
  const int kaddr0 = krow * 256 + ((hi ^ (krow & 15)) << 4);
  int vaddr0[4];
#pragma unroll
  for (int d = 0; d < 4; ++d) vaddr0[d] = (kh * 32 + 4 * hi + q4) * 256 + ((d ^ q4) << 6) + g1 * 32 + p4 * 8;

  bf16x8 kf[8], vf[4][2], pf[2];
  f32x16 sc = {}, sn, negm;
#pragma unroll
  for (int s = 0; s < 8; ++s) kf[s] = lds_r128(L + kslot(0) + (kaddr0 ^ (s << 5)));
#pragma unroll
  for (int s = 0; s < 8; ++s) sc = MFMA32(kf[s], qf[s], sc);
#pragma unroll
  for (int s = 0; s < 8; ++s) kf[s] = lds_r128(L + kslot(1) + (kaddr0 ^ (s << 5)));
  float m;
  {
    float tmax = sc[0];
#pragma unroll
    for (int r = 1; r < 16; ++r) tmax = fmaxf(tmax, sc[r]);
    m = swapmax(tmax);
#pragma unroll
    for (int r = 0; r < 16; ++r) { sc[r] -= m; negm[r] = -m; }
  }
  float l0 = 0.f, l1 = 0.f;
  const char* dbase = w < 4 ? kbase : vbase;
  unsigned dof0 = w < 4 ? ko[0] : vo, dof1 = w < 4 ? ko[1] : vo, dof2 = w < 4 ? ko[2] : vo, dof3 = w < 4 ? ko[3] : vo;
  const int dahead = w < 4 ? 6 : 4;
  int ks = kslot(2), vs = vslot(0), ksp = kslot(1), vsp = vslot(4);

#define RDV(d, s) vf[d][s] = cat8(lds_tr(vb + vaddr0[d] + (s) * 4096), lds_tr(vb + vaddr0[d] + (s) * 4096 + 2048))
#define RDK(s) kf[s] = lds_r128(kb + (kaddr0 ^ ((s) << 5)))
#define EXP(SC, r) SC[r] = __builtin_amdgcn_exp2f(SC[r])
#define CVT(SC, s, e) pw[s][e] = cvtpk(SC[8 * (s) + 2 * (e)], SC[8 * (s) + 2 * (e) + 1])
#define ADD(SC, r) do { l0 = fadd_s(l0, SC[r]); l1 = fadd_s(l1, SC[(r) + 1]); } while (0)
#define PVM(d, s) oacc[d] = MFMA32(vf[d][s], __builtin_bit_cast(bf16x8, pw[s]), oacc[d])
#define QKM(SN, s) SN = MFMA32(kf[s], qf[s], SN)
#define DMA(J, o) do { if (dmode == 1) dmap<J>(o, dptr, dla); else if (dmode == 2) dmapw<J>(o, dptr, dla); } while (0)
#define RESCALE(SN, tmax) do { \
    if (__builtin_amdgcn_ballot_w64(!(tmax <= 8.f)) != 0) { \
      const float mn = m + fmaxf(tmax, 0.f); \
      const float dl = mn - m; \
      const float alpha = __builtin_amdgcn_exp2f(-dl); \
      _Pragma("unroll") for (int d = 0; d < 4; ++d) oacc[d] *= alpha; \
      _Pragma("unroll") for (int r = 0; r < 16; ++r) { SN[r] -= dl; negm[r] = -mn; } \
      l0 *= alpha; l1 *= alpha; m = mn; \
    } } while (0)
#define BODY(SC, SN, HASK) do { \
    lds_char* vb = L + vs; \
    lds_char* kb = L + ks; \
    u32x4 pw[2]; \
    SN = MFMA32(kf[0], qf[0], negm); RDV(0, 0); RDV(1, 0); EXP(SC, 0); EXP(SC, 1); GAP(); \
    QKM(SN, 1); RDV(2, 0); RDV(3, 0); EXP(SC, 2); EXP(SC, 3); CVT(SC, 0, 0); GAP(); \
    QKM(SN, 2); RDV(0, 1); RDV(1, 1); EXP(SC, 4); EXP(SC, 5); CVT(SC, 0, 1); GAP(); \
    QKM(SN, 3); RDV(2, 1); RDV(3, 1); EXP(SC, 6); EXP(SC, 7); CVT(SC, 0, 2); GAP(); \
    QKM(SN, 4); DMA(0, dof0); EXP(SC, 8); EXP(SC, 9); CVT(SC, 0, 3); GAP(); \
    QKM(SN, 5); DMA(1, dof1); EXP(SC, 10); EXP(SC, 11); CVT(SC, 1, 0); GAP(); \
    QKM(SN, 6); DMA(2, dof2); EXP(SC, 12); EXP(SC, 13); CVT(SC, 1, 1); GAP(); \
    QKM(SN, 7); DMA(3, dof3); EXP(SC, 14); EXP(SC, 15); CVT(SC, 1, 2); GAP(); \
    PVM(0, 0); if (HASK) { RDK(0); RDK(1); } CVT(SC, 1, 3); ADD(SC, 0); ADD(SC, 2); GAP(); \
    PVM(1, 0); if (HASK) { RDK(2); RDK(3); } ADD(SC, 4); ADD(SC, 6); GAP(); \
    PVM(2, 0); if (HASK) { RDK(4); RDK(5); } ADD(SC, 8); ADD(SC, 10); GAP(); \
    PVM(3, 0); if (HASK) { RDK(6); RDK(7); } ADD(SC, 12); ADD(SC, 14); GAP(); \
    float t0 = fmaxf(fmaxf(SN[0], SN[1]), SN[2]), t1 = fmaxf(fmaxf(SN[3], SN[4]), SN[5]); \
    PVM(0, 1); t0 = fmaxf(fmaxf(t0, SN[6]), SN[7]); t1 = fmaxf(fmaxf(t1, SN[8]), SN[9]); GAP(); \
    PVM(1, 1); t0 = fmaxf(fmaxf(t0, SN[10]), SN[11]); t1 = fmaxf(fmaxf(t1, SN[12]), SN[13]); GAP(); \
    PVM(2, 1); t0 = fmaxf(fmaxf(t0, SN[14]), SN[15]); t0 = swapmax(fmaxf(t0, t1)); GAP(); \
    PVM(3, 1); GAP(); \
    RESCALE(SN, t0); \
    ksp = ks; vsp = vs; \
    ks = ks == 0 ? 16384 : (ks == 114688 ? 0 : ks + 32768); \
    vs = vs == 131072 ? 147456 : (vs == 147456 ? 32768 : vs + 32768); } while (0)

#define PFAT 27
  unsigned pfv;
#define WOPF(it) do { \
    if ((it) == PFAT) { pfv = (unsigned)(w * 512 + (jx >> XA) * 64 + lane_id()) * 1024u; \
      asm volatile("global_load_dword %0, %0, %1" : "+v"(pfv) : "s"((const char*)wo + nblk * 128) : "memory"); } \
    if ((it) == PFAT + 4) PIN(pfv); } while (0)
  unsigned ptg = (unsigned)__builtin_amdgcn_s_memrealtime() << 3;
  int pinc = P1, pcnt = 0;
#define PACEW() do { if (w == 0) { ptg += pinc; ++pcnt; if (pcnt >= N1) { pinc -= DP; if (pinc < P2) pinc = P2; } unsigned t_; \
    while (1) { t_ = (unsigned)__builtin_amdgcn_s_memrealtime() << 3; if ((int)(ptg - t_) <= 0) break; __builtin_amdgcn_s_sleep(1); } \
    if ((int)(t_ - ptg) > PSLACK) ptg = t_ - PSLACK; } } while (0)
  {
    WAITVM(8);
    BARRIER();
    if (w < 4) { ISSUE_K(5, L0 + kslot(0)); ISSUE_K(6, L0 + kslot(1)); }
    else { ISSUE_V(3, L0 + vslot(3)); ISSUE_V(4, L0 + vslot(4)); }
    const int dmode = 0; const char* dptr = dbase; const unsigned dla = 0;
    BODY(sc, sn, true);
  }
#define DSETUP(it) \
    const char* dptr = dbase + TOFF((it) + dahead); \
    const unsigned dla = L0 + (w < 4 ? ksp : vsp) + (w & 3) * 4096;
#pragma unroll 1
  for (int it = 1; it < 25; it += 2) {
    { PACEW(); WAITVM(12); BARRIER(); const int dmode = 1; DSETUP(it); BODY(sn, sc, true); }
    { PACEW(); WAITVM(12); BARRIER(); const int dmode = 1; DSETUP(it + 1); BODY(sc, sn, true); }
  }
  const char* wot = (const char*)wo + nblk * 128 + (size_t)(w & 3) * (8 * 131072) + (3 * 131072 - 24 * 16384);
#define WOFFS() do { const int ln_ = lane_id(), dr_ = ln_ >> 3; \
    dof0 = (unsigned)(dr_ * 1024 + (((ln_ & 7) ^ (((dr_ >> 1) & 1) << 2)) << 4)); \
    dof1 = dof0 + 7168; dof2 = dof0 + 4 * 131072 - 2048; dof3 = dof0 + 4 * 131072 + 8192 - 3072; } while (0)
#define TSETUP(it, ku, vu) \
    asm volatile("" : "+s"(ks), "+s"(vs), "+s"(ksp), "+s"(vsp)); \
    const int dmode = w < 4 ? ((it) + 6 < NT ? 1 : (ku) >= 0 ? 2 : 0) : ((it) + 4 < NT ? 1 : (vu) >= 0 ? 2 : 0); \
    const char* dptr = dmode == 1 ? dbase + TOFF((it) + dahead) : wot + (w < 4 ? (ku) : (vu)) * 16384; \
    const unsigned dla = L0 + (w < 4 ? ksp : vsp) + (w & 3) * 4096;
  { PACEW(); WAITVM(12); BARRIER(); WOPF(25); TSETUP(25, -1, -1); BODY(sn, sc, true); }
  if (w < 4) WOFFS();
  { PACEW(); WAITVM(12); BARRIER(); WOPF(26); TSETUP(26, 24, -1); BODY(sc, sn, true); }
  { PACEW(); WAITVM(12); BARRIER(); WOPF(27); TSETUP(27, 25, -1); BODY(sn, sc, true); }
  if (w >= 4) WOFFS();
  { WAITVM(12); BARRIER(); WOPF(28); TSETUP(28, 26, 27); BODY(sc, sn, true); }
  { WAITVM(12); BARRIER(); WOPF(29); TSETUP(29, 28, 29); BODY(sn, sc, true); }
  { WAITVM(12); BARRIER(); WOPF(30); TSETUP(30, 30, 31); BODY(sc, sn, false); }
  asm volatile("" : "+s"(vs));

  const int ln = lane_id();
  const int r32e = ln & 31, hie = ln >> 5;
  const unsigned wof0 = (unsigned)((ln >> 3) * 1024 + (((ln & 7) ^ ((((ln >> 3) >> 1) & 1) << 2)) << 4)), wof1 = wof0 + 7168;
  const int tw0 = w * 2048 + (4 * hie + ((ln & 15) >> 2)) * 128 + ((((ln & 15) >> 3)) << 6) + ((ln >> 4) & 1) * 32 + (ln & 3) * 8;
  const int tw1 = tw0 ^ 64;
  const char* wob = (const char*)wo + nblk * 128 + (size_t)w * (4 * 131072);
#define CELL(i) ((i) == 0 ? 49152 : (i) == 1 ? 81920 : (i) == 2 ? 114688 : (i) == 3 ? 98304 : (i) == 4 ? 0 : \
                 (i) == 5 ? 131072 : (i) == 6 ? 16384 : (i) == 7 ? 147456 : (i) == 8 ? 32768 : 65536)
#define RDW(dst, i) do { dst[0] = cat8(lds_tr(L + CELL(i) + tw0), lds_tr(L + CELL(i) + tw0 + 1024)); \
                         dst[1] = cat8(lds_tr(L + CELL(i) + tw1), lds_tr(L + CELL(i) + tw1 + 1024)); } while (0)
#define WAITL() asm volatile("s_waitcnt lgkmcnt(0)" ::: "memory")
  bf16x8 A[8][2];
  const unsigned foff = (unsigned)FOFF(ln);
  unsigned pv;
#define LDFLAGS() asm volatile("global_load_dword %0, %1, %2 sc1" : "=v"(pv) : "v"(foff), "s"(flg) : "memory")
  {
    WAITVM(8);
    BARRIER();
    WOPF(31);
    lds_char* vb = L + vs;
    u32x4 pw[2];
#pragma unroll
    for (int s = 0; s < 2; ++s)
#pragma unroll
      for (int d = 0; d < 4; ++d) RDV(d, s);
    RDW(A[0], 0); RDW(A[1], 1); RDW(A[2], 2); RDW(A[3], 3);
#pragma unroll
    for (int r = 0; r < 16; ++r) EXP(sn, r);
#pragma unroll
    for (int r = 0; r < 16; r += 2) ADD(sn, r);
#pragma unroll
    for (int s = 0; s < 2; ++s)
#pragma unroll
      for (int e = 0; e < 4; ++e) CVT(sn, s, e);
#pragma unroll
    for (int s = 0; s < 2; ++s)
#pragma unroll
      for (int d = 0; d < 4; ++d) PVM(d, s);
  }

  const char* ob = ows + ((size_t)mblk * 256 + w * 32) * 1024;
  unsigned f0v = f0[0];
#pragma unroll
  for (int i = 1; i < 8; ++i) asm volatile("v_writelane_b32 %0, %1, %2" : "+v"(f0v) : "s"(f0[i]), "i"(i));
  unsigned rdy;
  bf16x8 of[8], Bq[24][2];
  const unsigned oo = ln * 16;
#define HORD(g) ((g) == 0 ? 0 : (g) == 1 ? 2 : (g) == 2 ? 1 : 3)
#define GETRDY() do { PIN(pv); rdy = (unsigned)__builtin_amdgcn_ballot_w64(pv != f0v) & 255u; } while (0)
#define POLL(cond) do { if (!(cond)) { \
      int spins_ = 0; \
      do { __builtin_amdgcn_s_sleep(2); unsigned pq_; \
        asm volatile("global_load_dword %0, %1, %2 sc1\n\ts_waitcnt vmcnt(0)" : "=v"(pq_) : "v"(foff), "s"(flg) : "memory"); \
        rdy = (unsigned)__builtin_amdgcn_ballot_w64(pq_ != f0v) & 255u; \
      } while (!(cond) && ++spins_ < (1 << 18)); } } while (0)
#define PUBLISH(v) do { BARRIER(); if (w == 0 && ln == 0) \
    __hip_atomic_store((__attribute__((address_space(1))) unsigned*)(flg + fic * (FSTR / 4)), f0[8] + (v), __ATOMIC_RELAXED, __HIP_MEMORY_SCOPE_AGENT); } while (0)
#define RING(j) ((j) % 10 == 0 ? 9 : (j) % 10 < 6 ? (j) % 10 + 3 : (j) % 10 - 6)
#define LDO(j) asm volatile("global_load_dwordx4 %0, %1, %2 offset:%3 sc1" : "=v"(of[(j) & 7]) : "v"(oo), \
      "s"(ob + HORD((j) >> 3) * 8192 + (((j) >> 2) & 1) * 4096), "i"(((j) & 3) * 1024) : "memory")
#define DMAU(u) dma2(wof0, wof1, wob + HORD((u) >> 3) * 131072 + ((u) & 7) * 16384, L0 + CELL(RING(u)) + w * 2048)
#define WAITN(n) asm volatile("s_waitcnt vmcnt(%0)" :: "i"(n) : "memory")

  const float l = swapadd(l0 + l1);
  {
    float* ml = (float*)(smem + CELL(8));
    if (hie == 0) { ml[w * 512 + r32e] = m; ml[w * 512 + 32 + r32e] = l; }
#pragma unroll
    for (int dd = 0; dd < 2; ++dd)
#pragma unroll
      for (int g = 0; g < 4; ++g) {
        f32x4 v;
        if (kh) { v[0] = oacc[dd][4 * g]; v[1] = oacc[dd][4 * g + 1]; v[2] = oacc[dd][4 * g + 2]; v[3] = oacc[dd][4 * g + 3]; }
        else { v[0] = oacc[2 + dd][4 * g]; v[1] = oacc[2 + dd][4 * g + 1]; v[2] = oacc[2 + dd][4 * g + 2]; v[3] = oacc[2 + dd][4 * g + 3]; }
        *(__attribute__((address_space(3))) f32x4*)(L + CELL(dd * 2 + (g >> 1)) + w * 2048 + (g & 1) * 1024 + ln * 16) = v;
      }
    WAITVM(0);
    BARRIER();
    DMAU(0);
    RDW(A[4], 4); RDW(A[5], 5); RDW(A[6], 6); RDW(A[7], 7);
    const float mp = ml[(w ^ 4) * 512 + r32e], lp = ml[(w ^ 4) * 512 + 32 + r32e];
    WAITL();
    DMAU(1); DMAU(2); DMAU(3); DMAU(4);
    const float mf = fmaxf(m, mp);
    const float wa = __builtin_amdgcn_exp2f(m - mf), wb = __builtin_amdgcn_exp2f(mp - mf);
    const float inv = 1.f / (wa * l + wb * lp);
    const float fa = wa * inv, fb = wb * inv;
    const char* ost = ows + ((size_t)(b * 4 + qg) * 256 + head * 8 + 4 * kh) * 1024;
#pragma unroll
    for (int dd = 0; dd < 2; ++dd) {
      float o[16];
#pragma unroll
      for (int g = 0; g < 4; ++g) {
        f32x4 v = *(__attribute__((address_space(3))) f32x4*)(L + CELL(dd * 2 + (g >> 1)) + (w ^ 4) * 2048 + (g & 1) * 1024 + ln * 16);
#pragma unroll
        for (int e = 0; e < 4; ++e) {
          const float own = kh ? oacc[2 + dd][4 * g + e] : oacc[dd][4 * g + e];
          o[4 * g + e] = own * fa + v[e] * fb;
        }
      }
#pragma unroll
      for (int sp = 0; sp < 2; ++sp) {
        bf16x8 f = pack8(o[8 * sp + 0], o[8 * sp + 1], o[8 * sp + 2], o[8 * sp + 3], o[8 * sp + 4], o[8 * sp + 5], o[8 * sp + 6], o[8 * sp + 7]);
        asm volatile("global_store_dwordx4 %0, %1, %2 offset:%3 sc1\n\ts_nop 1" :: "v"((unsigned)(ln * 16)), "v"(f), "s"(ost), "i"((dd * 2 + sp) * 1024) : "memory");
      }
    }
    BARRIER();
  }
  DMAU(5); DMAU(6); DMAU(7); DMAU(8); DMAU(9);
#define ASTEP(u, n) do { WAITN(n); RDW(Bq[(u) - 10], RING(u)); WAITL(); DMAU(u); } while (0)
#define MSTEP(j, n) do { WAITN(n); PIN(of[(j) & 7]); \
    if ((j) < 24) { acc[0] = MFMA32(Bq[(j) % 24][0], of[(j) & 7], acc[0]); acc[1] = MFMA32(Bq[(j) % 24][1], of[(j) & 7], acc[1]); } \
    else { acc[0] = MFMA32(A[(j) & 7][0], of[(j) & 7], acc[0]); acc[1] = MFMA32(A[(j) & 7][1], of[(j) & 7], acc[1]); } } while (0)
  f32x16 acc[2] = {};
  ASTEP(10, 22); ASTEP(11, 22); WAITN(14); PUBLISH(1u); ASTEP(12, 22); ASTEP(13, 22); ASTEP(14, 22); ASTEP(15, 18); ASTEP(16, 18); ASTEP(17, 18); LDFLAGS(); ASTEP(18, 19); ASTEP(19, 19); ASTEP(20, 19); ASTEP(21, 19);
  WAITN(8); GETRDY();
  POLL((rdy >> HORD(0)) & 1u);
  LDO(0); LDO(1); LDO(2); LDO(3); LDO(4); LDO(5); LDO(6); LDO(7);
  MSTEP(0, 7); RDW(Bq[12], RING(22)); WAITL(); DMAU(22); POLL((rdy >> HORD(1)) & 1u); LDFLAGS(); LDO(8); GAP();
  MSTEP(1, 10); RDW(Bq[13], RING(23)); WAITL(); DMAU(23); LDO(9); GAP();
  MSTEP(2, 12); RDW(Bq[14], RING(14)); LDO(10); GAP();
  MSTEP(3, 12); RDW(Bq[15], RING(15)); LDO(11); GAP();
  MSTEP(4, 12); RDW(Bq[16], RING(16)); LDO(12); GAP();
  MSTEP(5, 12); RDW(Bq[17], RING(17)); LDO(13); GAP();
  MSTEP(6, 12); RDW(Bq[18], RING(18)); LDO(14); GAP();
  MSTEP(7, 12); RDW(Bq[19], RING(19)); LDO(15); GAP();
  MSTEP(8, 9); RDW(Bq[20], RING(20)); GETRDY(); POLL((rdy >> HORD(2)) & 1u); LDFLAGS(); LDO(16); GAP();
  MSTEP(9, 8); RDW(Bq[21], RING(21)); LDO(17); GAP();
  MSTEP(10, 8); RDW(Bq[22], RING(22)); LDO(18); GAP();
  MSTEP(11, 8); RDW(Bq[23], RING(23)); LDO(19); GAP();
  MSTEP(12, 8); LDO(20); GAP();
  MSTEP(13, 8); LDO(21); GAP();
  MSTEP(14, 8); LDO(22); GAP();
  MSTEP(15, 8); LDO(23); GAP();
  MSTEP(16, 7); GETRDY(); POLL((rdy >> HORD(3)) & 1u); LDFLAGS(); LDO(24); GAP();
  MSTEP(17, 8); LDO(25); GAP();
  MSTEP(18, 8); LDO(26); GAP();
  MSTEP(19, 8); LDO(27); GAP();
  MSTEP(20, 8); LDO(28); GAP();
  MSTEP(21, 8); LDO(29); GAP();
  MSTEP(22, 8); LDO(30); GAP();
  MSTEP(23, 8); LDO(31); GAP();
  MSTEP(24, 7); GAP();
  MSTEP(25, 6); GAP();
  MSTEP(26, 5); GAP();
  MSTEP(27, 4); GAP();
  MSTEP(28, 3); GAP();
  MSTEP(29, 2); GAP();
  MSTEP(30, 1); GAP();
  MSTEP(31, 0); GAP();
  GETRDY();
#pragma unroll
  for (int nb = 0; nb < 2; ++nb)
#pragma unroll
    for (int g = 0; g < 4; ++g) {
      f32x4 v = {acc[nb][4 * g], acc[nb][4 * g + 1], acc[nb][4 * g + 2], acc[nb][4 * g + 3]};
      *(__attribute__((address_space(3))) f32x4*)(L + CELL(nb * 2 + (g >> 1)) + w * 2048 + (g & 1) * 1024 + ln * 16) = v;
    }
  POLL((rdy & 0xF0u) == 0xF0u);
  PUBLISH(2u);
  {
    const int cb = (w >> 1) == 0 ? CELL(0) : (w >> 1) == 1 ? CELL(1) : (w >> 1) == 2 ? CELL(2) : CELL(3);
    f32x4 s = {0.f, 0.f, 0.f, 0.f};
#pragma unroll
    for (int ww = 0; ww < 8; ++ww) s += *(__attribute__((address_space(3))) f32x4*)(L + cb + ww * 2048 + (w & 1) * 1024 + ln * 16);
    const int nb = w >> 2, g = w & 3;
    u32x2 pk = {cvtpk(s[0], s[1]), cvtpk(s[2], s[3])};
    *(u32x2*)((char*)out + ((size_t)(mblk * 32 + r32e) * 512 + nblk * 64 + nb * 32 + 8 * g + 4 * hie) * 2) = pk;
  }
}

extern "C" void kernel_launch(void* const* d_in, const int* in_sizes, int n_in, void* d_out, int out_size, void* d_ws, size_t ws_size,
                              hipStream_t stream) {
  const __hip_bfloat16* x = (const __hip_bfloat16*)d_in[0];
  const __hip_bfloat16* kc = (const __hip_bfloat16*)d_in[1];
  const __hip_bfloat16* vc = (const __hip_bfloat16*)d_in[2];
  const __hip_bfloat16* wq = (const __hip_bfloat16*)d_in[3];
  const __hip_bfloat16* wo = (const __hip_bfloat16*)d_in[4];
  __hip_bfloat16* out = (__hip_bfloat16*)d_out;
  (void)hipFuncSetAttribute((const void*)fused_kernel, hipFuncAttributeMaxDynamicSharedMemorySize, LDS1);
  fused_kernel<<<256, 512, LDS1, stream>>>(x, kc, vc, wq, wo, (char*)d_ws, (unsigned*)((char*)d_ws + (16u << 20)), out);
}
```

```cpp
#define P1 1040
#define N1 6
#define DP 1000
#define P2 880
#define PSLACK 400
#include <hip/hip_runtime.h>
#include <hip/hip_bf16.h>
#include <stdint.h>


typedef __attribute__((ext_vector_type(8))) short bf16x8;
typedef __attribute__((ext_vector_type(4))) short s16x4;
typedef __attribute__((ext_vector_type(16))) float f32x16;
typedef __attribute__((ext_vector_type(4))) float f32x4;
typedef __attribute__((ext_vector_type(4))) unsigned u32x4;
typedef __attribute__((ext_vector_type(2))) unsigned u32x2;
typedef __attribute__((address_space(3))) char lds_char;
typedef short v4i16_t __attribute__((ext_vector_type(4)));

constexpr int NB = 8, SQ = 128, DM = 512, NH = 32, SK = 2048, DH = 128;
constexpr int NT = SK / 64;
constexpr int LDS1 = 163840;
#define NPB 6
#define PBUF(c) (LDS1 - NPB * 16384 + (((c) + 4 * NPB - 16) % NPB) * 16384)
__device__ constexpr int tile_step(int n) { return n < 10 - NPB ? 2 * n : n + 6; }
__device__ constexpr int p_after(int c) {
  int mx = (c > 0 ? c : 0) - 1 + NPB; if (mx > 15) mx = 15;
  int n = 4 * (mx - (c + 1));
  for (int v = 2; v <= 6; v += 2) if (tile_step(v) < c && tile_step(v) >= c + 1 - NPB) n += 4;
  return n;
}
constexpr int XCH_OFF = 131072;
constexpr float CSC = 0.08838834764831845f * 1.4426950408889634f;

#define POL "nt"
#define MFMA32(a, b, c) __builtin_amdgcn_mfma_f32_32x32x16_bf16(a, b, c, 0, 0, 0)

typedef __attribute__((ext_vector_type(2))) float f32x2;
typedef __attribute__((ext_vector_type(2))) __bf16 bf16x2_t;
__device__ __forceinline__ unsigned cvtpk(float lo, float hi) {
  f32x2 v = {lo, hi};
  return __builtin_bit_cast(unsigned, __builtin_convertvector(v, bf16x2_t));
}
__device__ __forceinline__ bf16x8 pack8(float a0, float a1, float a2, float a3, float a4, float a5, float a6, float a7) {
  u32x4 w = {cvtpk(a0, a1), cvtpk(a2, a3), cvtpk(a4, a5), cvtpk(a6, a7)};
  return __builtin_bit_cast(bf16x8, w);
}
__device__ __forceinline__ bf16x8 lds_r128(lds_char* p) { return *(__attribute__((address_space(3))) bf16x8*)p; }
__device__ __forceinline__ s16x4 lds_tr(lds_char* p) {
  return __builtin_bit_cast(s16x4, __builtin_amdgcn_ds_read_tr16_b64_v4i16((__attribute__((address_space(3))) v4i16_t*)p));
}
__device__ __forceinline__ bf16x8 cat8(s16x4 a, s16x4 b) { return __builtin_shufflevector(a, b, 0, 1, 2, 3, 4, 5, 6, 7); }

__device__ __forceinline__ void dma4(unsigned o0, unsigned o1, unsigned o2, unsigned o3, const void* sbase, unsigned ldsaddr) {
  unsigned keep;
  asm volatile(
      "s_mov_b32 %0, m0\n\t"
      "s_mov_b32 m0, %6\n\t"
      "s_nop 0\n\t"
      "global_load_lds_dwordx4 %1, %5 " POL "\n\t"
      "global_load_lds_dwordx4 %2, %5 offset:1024 " POL "\n\t"
      "global_load_lds_dwordx4 %3, %5 offset:2048 " POL "\n\t"
      "global_load_lds_dwordx4 %4, %5 offset:3072 " POL "\n\t"
      "s_mov_b32 m0, %0"
      : "=&s"(keep) : "v"(o0), "v"(o1), "v"(o2), "v"(o3), "s"(sbase), "s"(ldsaddr) : "memory");
}
__device__ __forceinline__ void dma2(unsigned o0, unsigned o1, const void* sbase, unsigned ldsaddr) {
  unsigned keep;
  asm volatile(
      "s_mov_b32 %0, m0\n\t"
      "s_mov_b32 m0, %4\n\t"
      "s_nop 0\n\t"
      "global_load_lds_dwordx4 %1, %3\n\t"
      "global_load_lds_dwordx4 %2, %3 offset:1024\n\t"
      "s_mov_b32 m0, %0"
      : "=&s"(keep) : "v"(o0), "v"(o1), "s"(sbase), "s"(ldsaddr) : "memory");
}
template <int J> __device__ __forceinline__ void dmap(unsigned o, const void* sbase, unsigned ldsaddr) {
  unsigned keep;
  asm volatile("s_mov_b32 %0, m0\n\ts_mov_b32 m0, %3\n\ts_nop 0\n\tglobal_load_lds_dwordx4 %1, %2 offset:%4 " POL "\n\ts_mov_b32 m0, %0"
               : "=&s"(keep) : "v"(o), "s"(sbase), "s"(ldsaddr), "i"(J * 1024) : "memory");
}
template <int J> __device__ __forceinline__ void dmapw(unsigned o, const void* sbase, unsigned ldsaddr) {
  unsigned keep;
  asm volatile("s_mov_b32 %0, m0\n\ts_mov_b32 m0, %3\n\ts_nop 0\n\tglobal_load_lds_dwordx4 %1, %2 offset:%4\n\ts_mov_b32 m0, %0"
               : "=&s"(keep) : "v"(o), "s"(sbase), "s"(ldsaddr), "i"(J * 1024) : "memory");
}
__device__ __forceinline__ int lane_id() {
  int lane;
  asm volatile("v_mbcnt_lo_u32_b32 %0, -1, 0\n\tv_mbcnt_hi_u32_b32 %0, -1, %0" : "=v"(lane));
  return lane;
}
__device__ __forceinline__ float fadd_s(float a, float b) { float r; asm("v_add_f32_e32 %0, %1, %2" : "=v"(r) : "v"(a), "v"(b)); return r; }
#define GAP() __builtin_amdgcn_sched_barrier(0)
__device__ __forceinline__ int kslot(int i) { return i == 0 ? 0 : i * 32768 - 16384; }
__device__ __forceinline__ int vslot(int i) { return i == 4 ? 147456 : i * 32768 + 32768; }
#define WAITVM(n) asm volatile("s_waitcnt vmcnt(" #n ")" ::: "memory")
#define BARRIER() asm volatile("s_waitcnt lgkmcnt(0)\n\ts_barrier" ::: "memory")
#define PIN(x) asm volatile("" : "+v"(x))

__device__ __forceinline__ float swapmax(float v) {
  auto rr = __builtin_amdgcn_permlane32_swap(__float_as_uint(v), __float_as_uint(v), false, false);
  return fmaxf(__uint_as_float(rr[0]), __uint_as_float(rr[1]));
}
__device__ __forceinline__ float swapadd(float v) {
  auto rr = __builtin_amdgcn_permlane32_swap(__float_as_uint(v), __float_as_uint(v), false, false);
  return __uint_as_float(rr[0]) + __uint_as_float(rr[1]);
}

__global__ __launch_bounds__(512, 2) void fused_kernel(const __hip_bfloat16* __restrict__ x, const __hip_bfloat16* __restrict__ kc,
                                                       const __hip_bfloat16* __restrict__ vc, const __hip_bfloat16* __restrict__ wq,
                                                       const __hip_bfloat16* __restrict__ wo, char* ows, unsigned* flg,
                                                       __hip_bfloat16* __restrict__ out) {
  extern __shared__ __attribute__((aligned(16))) char smem[];
  lds_char* L = (lds_char*)smem;
  const unsigned L0 = (unsigned)(uintptr_t)smem;
  const int tid = threadIdx.x, lane = tid & 63;
  const int w = __builtin_amdgcn_readfirstlane(tid >> 6);
  const int qg = w & 3, kh = w >> 2;
  const int r32 = lane & 31, hi = lane >> 5;
  const int q4 = (lane & 15) >> 2, p4 = lane & 3, g1 = (lane >> 4) & 1;
  const int bid = blockIdx.x;
  const unsigned tst = (unsigned)__builtin_amdgcn_s_memrealtime();
#define PADTO(us) do { while ((int)((unsigned)__builtin_amdgcn_s_memrealtime() - tst) < (us) * 100) __builtin_amdgcn_s_sleep(1); } while (0)
  const int head = 4 * (bid & 7) + ((bid >> 3) & 3), b = bid >> 5;
  const char* kbase = (const char*)kc + ((size_t)(b * NH + head) * SK) * 256;
  const char* vbase = (const char*)vc + ((size_t)(b * NH + head) * SK) * 256;
  const char* wqbase = (const char*)wq + head * 256;
#define FSTR 256
#define XA 2
  const int xcd = bid & 7, jx = bid >> 3;
  const int nblk = ((xcd & ((8 >> XA) - 1)) << XA) | (jx & ((1 << XA) - 1)), mblk = (xcd >> (3 - XA)) * (32 >> XA) + (jx >> XA);
  int fia = (mblk >> 2) * 32 + w;
  int fib = ((((4 * b) % (32 >> XA)) << XA) | (w & ((1 << XA) - 1))) * 8 + ((((4 * b) / (32 >> XA)) << (3 - XA)) | (w >> XA)) - (32 << XA);
  int fic = bid;
  asm volatile("" : "+s"(fia), "+s"(fib), "+s"(fic));
#define FOFF(lane) (((lane) < 8 ? ((lane) < 4 ? fia + 8 * (lane) : fib + ((lane) << (3 + XA))) : fic) * FSTR)
  unsigned fv;
  asm volatile("global_load_dword %0, %1, %2 sc1" : "=v"(fv) : "v"((unsigned)FOFF(lane)), "s"(flg) : "memory");

  const int drow = lane >> 4, dpos = lane & 15;
  unsigned ko[4];
#pragma unroll
  for (int j = 0; j < 4; ++j) ko[j] = (unsigned)(((w & 3) * 16 + drow) * 256 + ((dpos ^ (4 * j + drow)) << 4));
  const unsigned vo = (unsigned)(((w & 3) * 16 + drow) * 256 + ((dpos ^ (drow << 2)) << 4));
  unsigned wo2[2], xo2[2];
#pragma unroll
  for (int j = 0; j < 2; ++j) {
    wo2[j] = (unsigned)(((w & 3) * 8 + 4 * j + drow) * 8192 + ((dpos ^ (drow << 2)) << 4) - j * 1024);
    xo2[j] = (unsigned)(((w & 3) * 32 + 16 * j + (lane >> 2)) * 1024 + (((lane & 3) ^ drow) << 4) - j * 1024);
  }
  const char* xbase = (const char*)x + (size_t)b * SQ * 1024;

#define TOFF(t) ((size_t)(t) * 16384)
#define ISSUE_K(t, la) dma4(ko[0], ko[1], ko[2], ko[3], kbase + TOFF(t), (la) + (w & 3) * 4096)
#define ISSUE_V(t, la) dma4(vo, vo, vo, vo, vbase + TOFF(t), (la) + (w & 3) * 4096)
  const int crot = 2 * b + ((bid >> 3) & 1) + 8 * ((bid >> 4) & 1);
#define ISSUE_P(c) do { const int cc_ = ((c) + crot) & 15; \
    dma2(wo2[0], wo2[1], wqbase + (size_t)cc_ * 32 * 8192, L0 + PBUF(c) + (w & 3) * 2048); \
    dma2(xo2[0], xo2[1], xbase + cc_ * 64, L0 + PBUF(c) + 8192 + (w & 3) * 2048); } while (0)
#define ISSUE_T(c) do { \
    if (w >= 4) { \
      if (tile_step(2) == (c)) ISSUE_V(0, L0 + vslot(0)); \
      if (tile_step(4) == (c)) ISSUE_V(1, L0 + vslot(1)); \
      if (tile_step(6) == (c)) ISSUE_V(2, L0 + vslot(2)); \
    } else { \
      if (tile_step(0) == (c)) ISSUE_K(0, L0 + kslot(0)); \
      if (tile_step(1) == (c)) ISSUE_K(1, L0 + kslot(1)); \
      if (tile_step(3) == (c)) ISSUE_K(2, L0 + kslot(2)); \
      if (tile_step(5) == (c)) ISSUE_K(3, L0 + kslot(3)); \
      if (tile_step(7) == (c)) ISSUE_K(4, L0 + kslot(4)); \
    } } while (0)
  if (w >= 4) {
#pragma unroll
    for (int c = 0; c < NPB; ++c) ISSUE_P(c);
  }

  f32x16 qa[2] = {};
  {
    const int ps = ((p4 & 1) << 1) | (p4 >> 1);
    int wa[2], xa[2];
#pragma unroll
    for (int dd = 0; dd < 2; ++dd) {
      const int dblk = 2 * kh + dd;
      wa[dd] = (8 * hi + q4) * 256 + (((4 * dblk + 2 * g1 + (ps >> 1)) ^ (q4 << 2)) << 4) + 8 * (ps & 1);
      xa[dd] = 8192 + (qg * 32 + r32) * 64 + (((2 * dd + hi) ^ ((r32 >> 2) & 3)) << 4);
    }
    bf16x8 xf[2][2], af[2][2][2];
#pragma unroll
    for (int c = -1; c < 16; ++c) {
      if (w >= 4 && c < 15) asm volatile("s_waitcnt vmcnt(%0)" :: "i"(p_after(c)) : "memory");
      BARRIER();
      if (w >= 4 && c >= 0 && c + NPB < 16) ISSUE_P(c + NPB);
      ISSUE_T(c);
      if (c < 15) {
        lds_char* wb = L + PBUF(c + 1);
#pragma unroll
        for (int s = 0; s < 2; ++s) {
          xf[(c + 1) & 1][s] = lds_r128(wb + xa[s]);
#pragma unroll
          for (int dd = 0; dd < 2; ++dd)
            af[(c + 1) & 1][s][dd] = cat8(lds_tr(wb + wa[dd] + s * 4096), lds_tr(wb + wa[dd] + s * 4096 + 1024));
        }
      }
      GAP();
      if (c >= 0) {
#pragma unroll
        for (int s = 0; s < 2; ++s)
#pragma unroll
          for (int dd = 0; dd < 2; ++dd) qa[dd] = MFMA32(af[c & 1][s][dd], xf[c & 1][s], qa[dd]);
      }
      GAP();
    }
  }
  bf16x8 qf[8];
  unsigned f0[9];
  {
    bf16x8 own[4];
#pragma unroll
    for (int dd = 0; dd < 2; ++dd)
#pragma unroll
      for (int sp = 0; sp < 2; ++sp)
      {
        u32x4 t = __builtin_bit_cast(u32x4, pack8(qa[dd][8 * sp + 0], qa[dd][8 * sp + 1], qa[dd][8 * sp + 2], qa[dd][8 * sp + 3],
                                                  qa[dd][8 * sp + 4], qa[dd][8 * sp + 5], qa[dd][8 * sp + 6], qa[dd][8 * sp + 7]));
#pragma unroll
        for (int e = 0; e < 4; ++e) t[e] = cvtpk(__uint_as_float(t[e] << 16) * CSC, __uint_as_float(t[e] & 0xffff0000u) * CSC);
        own[dd * 2 + sp] = __builtin_bit_cast(bf16x8, t);
      }
#pragma unroll
    for (int i = 0; i < 4; ++i) *(__attribute__((address_space(3))) bf16x8*)(L + XCH_OFF + w * 4096 + i * 1024 + lane * 16) = own[i];
    if (w < 4) WAITVM(12);
    BARRIER();
    PIN(fv);
#pragma unroll
    for (int i = 0; i < 9; ++i) f0[i] = __builtin_amdgcn_readlane(fv, i);
#pragma unroll
    for (int i = 0; i < 4; ++i) {
      bf16x8 o = lds_r128(L + XCH_OFF + (w ^ 4) * 4096 + i * 1024 + lane * 16);
      if (kh == 0) { qf[i] = own[i]; qf[4 + i] = o; } else { qf[4 + i] = own[i]; qf[i] = o; }
    }
  }

  f32x16 oacc[4] = {};
  const int krow = kh * 32 + r32;
  const int kaddr0 = krow * 256 + ((hi ^ (krow & 15)) << 4);
  int vaddr0[4];
#pragma unroll
  for (int d = 0; d < 4; ++d) vaddr0[d] = (kh * 32 + 4 * hi + q4) * 256 + ((d ^ q4) << 6) + g1 * 32 + p4 * 8;

  bf16x8 kf[8], vf[4][2], pf[2];
  f32x16 sc = {}, sn, negm;
#pragma unroll
  for (int s = 0; s < 8; ++s) kf[s] = lds_r128(L + kslot(0) + (kaddr0 ^ (s << 5)));
#pragma unroll
  for (int s = 0; s < 8; ++s) sc = MFMA32(kf[s], qf[s], sc);
#pragma unroll
  for (int s = 0; s < 8; ++s) kf[s] = lds_r128(L + kslot(1) + (kaddr0 ^ (s << 5)));
  float m;
  {
    float tmax = sc[0];
#pragma unroll
    for (int r = 1; r < 16; ++r) tmax = fmaxf(tmax, sc[r]);
    m = swapmax(tmax);
#pragma unroll
    for (int r = 0; r < 16; ++r) { sc[r] -= m; negm[r] = -m; }
  }
  float l0 = 0.f, l1 = 0.f;
  const char* dbase = w < 4 ? kbase : vbase;
  unsigned dof0 = w < 4 ? ko[0] : vo, dof1 = w < 4 ? ko[1] : vo, dof2 = w < 4 ? ko[2] : vo, dof3 = w < 4 ? ko[3] : vo;
  const int dahead = w < 4 ? 6 : 4;
  int ks = kslot(2), vs = vslot(0), ksp = kslot(1), vsp = vslot(4);

#define RDV(d, s) vf[d][s] = cat8(lds_tr(vb + vaddr0[d] + (s) * 4096), lds_tr(vb + vaddr0[d] + (s) * 4096 + 2048))
#define RDK(s) kf[s] = lds_r128(kb + (kaddr0 ^ ((s) << 5)))
#define EXP(SC, r) SC[r] = __builtin_amdgcn_exp2f(SC[r])
#define CVT(SC, s, e) pw[s][e] = cvtpk(SC[8 * (s) + 2 * (e)], SC[8 * (s) + 2 * (e) + 1])
#define ADD(SC, r) do { l0 = fadd_s(l0, SC[r]); l1 = fadd_s(l1, SC[(r) + 1]); } while (0)
#define PVM(d, s) oacc[d] = MFMA32(vf[d][s], __builtin_bit_cast(bf16x8, pw[s]), oacc[d])
#define QKM(SN, s) SN = MFMA32(kf[s], qf[s], SN)
#define DMA(J, o) do { if (dmode == 1) dmap<J>(o, dptr, dla); else if (dmode == 2) dmapw<J>(o, dptr, dla); } while (0)
#define RESCALE(SN, tmax) do { \
    if (__builtin_amdgcn_ballot_w64(!(tmax <= 8.f)) != 0) { \
      const float mn = m + fmaxf(tmax, 0.f); \
      const float dl = mn - m; \
      const float alpha = __builtin_amdgcn_exp2f(-dl); \
      _Pragma("unroll") for (int d = 0; d < 4; ++d) oacc[d] *= alpha; \
      _Pragma("unroll") for (int r = 0; r < 16; ++r) { SN[r] -= dl; negm[r] = -mn; } \
      l0 *= alpha; l1 *= alpha; m = mn; \
    } } while (0)
#define BODY(SC, SN, HASK) do { \
    lds_char* vb = L + vs; \
    lds_char* kb = L + ks; \
    u32x4 pw[2]; \
    SN = MFMA32(kf[0], qf[0], negm); RDV(0, 0); RDV(1, 0); EXP(SC, 0); EXP(SC, 1); GAP(); \
    QKM(SN, 1); RDV(2, 0); RDV(3, 0); EXP(SC, 2); EXP(SC, 3); CVT(SC, 0, 0); GAP(); \
    QKM(SN, 2); RDV(0, 1); RDV(1, 1); EXP(SC, 4); EXP(SC, 5); CVT(SC, 0, 1); GAP(); \
    QKM(SN, 3); RDV(2, 1); RDV(3, 1); EXP(SC, 6); EXP(SC, 7); CVT(SC, 0, 2); GAP(); \
    QKM(SN, 4); DMA(0, dof0); EXP(SC, 8); EXP(SC, 9); CVT(SC, 0, 3); GAP(); \
    QKM(SN, 5); DMA(1, dof1); EXP(SC, 10); EXP(SC, 11); CVT(SC, 1, 0); GAP(); \
    QKM(SN, 6); DMA(2, dof2); EXP(SC, 12); EXP(SC, 13); CVT(SC, 1, 1); GAP(); \
    QKM(SN, 7); DMA(3, dof3); EXP(SC, 14); EXP(SC, 15); CVT(SC, 1, 2); GAP(); \
    PVM(0, 0); if (HASK) { RDK(0); RDK(1); } CVT(SC, 1, 3); ADD(SC, 0); ADD(SC, 2); GAP(); \
    PVM(1, 0); if (HASK) { RDK(2); RDK(3); } ADD(SC, 4); ADD(SC, 6); GAP(); \
    PVM(2, 0); if (HASK) { RDK(4); RDK(5); } ADD(SC, 8); ADD(SC, 10); GAP(); \
    PVM(3, 0); if (HASK) { RDK(6); RDK(7); } ADD(SC, 12); ADD(SC, 14); GAP(); \
    float t0 = fmaxf(fmaxf(SN[0], SN[1]), SN[2]), t1 = fmaxf(fmaxf(SN[3], SN[4]), SN[5]); \
    PVM(0, 1); t0 = fmaxf(fmaxf(t0, SN[6]), SN[7]); t1 = fmaxf(fmaxf(t1, SN[8]), SN[9]); GAP(); \
    PVM(1, 1); t0 = fmaxf(fmaxf(t0, SN[10]), SN[11]); t1 = fmaxf(fmaxf(t1, SN[12]), SN[13]); GAP(); \
    PVM(2, 1); t0 = fmaxf(fmaxf(t0, SN[14]), SN[15]); t0 = swapmax(fmaxf(t0, t1)); GAP(); \
    PVM(3, 1); GAP(); \
    RESCALE(SN, t0); \
    ksp = ks; vsp = vs; \
    ks = ks == 0 ? 16384 : (ks == 114688 ? 0 : ks + 32768); \
    vs = vs == 131072 ? 147456 : (vs == 147456 ? 32768 : vs + 32768); } while (0)

#define PFAT 27
  unsigned pfv;
#define WOPF(it) do { \
    if ((it) == PFAT) { pfv = (unsigned)(w * 512 + (jx >> XA) * 64 + lane_id()) * 1024u; \
      asm volatile("global_load_dword %0, %0, %1" : "+v"(pfv) : "s"((const char*)wo + nblk * 128) : "memory"); } \
    if ((it) == PFAT + 4) PIN(pfv); } while (0)
  unsigned ptg = (unsigned)__builtin_amdgcn_s_memrealtime() << 3;
  int pinc = P1, pcnt = 0;
#define PACEW() do { if (w == 0) { ptg += pinc; ++pcnt; if (pcnt >= N1) { pinc -= DP; if (pinc < P2) pinc = P2; } unsigned t_; \
    while (1) { t_ = (unsigned)__builtin_amdgcn_s_memrealtime() << 3; if ((int)(ptg - t_) <= 0) break; __builtin_amdgcn_s_sleep(1); } \
    if ((int)(t_ - ptg) > PSLACK) ptg = t_ - PSLACK; } } while (0)
  {
    WAITVM(8);
    BARRIER();
    if (w < 4) { ISSUE_K(5, L0 + kslot(0)); ISSUE_K(6, L0 + kslot(1)); }
    else { ISSUE_V(3, L0 + vslot(3)); ISSUE_V(4, L0 + vslot(4)); }
    const int dmode = 0; const char* dptr = dbase; const unsigned dla = 0;
    BODY(sc, sn, true);
  }
#define DSETUP(it) \
    const char* dptr = dbase + TOFF((it) + dahead); \
    const unsigned dla = L0 + (w < 4 ? ksp : vsp) + (w & 3) * 4096;
#pragma unroll 1
  for (int it = 1; it < 25; it += 2) {
    { PACEW(); WAITVM(12); BARRIER(); const int dmode = 1; DSETUP(it); BODY(sn, sc, true); }
    { PACEW(); WAITVM(12); BARRIER(); const int dmode = 1; DSETUP(it + 1); BODY(sc, sn, true); }
  }
  const char* wot = (const char*)wo + nblk * 128 + (size_t)(w & 3) * (8 * 131072) + (3 * 131072 - 24 * 16384);
#define WOFFS() do { const int ln_ = lane_id(), dr_ = ln_ >> 3; \
    dof0 = (unsigned)(dr_ * 1024 + (((ln_ & 7) ^ (((dr_ >> 1) & 1) << 2)) << 4)); \
    dof1 = dof0 + 7168; dof2 = dof0 + 4 * 131072 - 2048; dof3 = dof0 + 4 * 131072 + 8192 - 3072; } while (0)
#define TSETUP(it, ku, vu) \
    asm volatile("" : "+s"(ks), "+s"(vs), "+s"(ksp), "+s"(vsp)); \
    const int dmode = w < 4 ? ((it) + 6 < NT ? 1 : (ku) >= 0 ? 2 : 0) : ((it) + 4 < NT ? 1 : (vu) >= 0 ? 2 : 0); \
    const char* dptr = dmode == 1 ? dbase + TOFF((it) + dahead) : wot + (w < 4 ? (ku) : (vu)) * 16384; \
    const unsigned dla = L0 + (w < 4 ? ksp : vsp) + (w & 3) * 4096;
  { PACEW(); WAITVM(12); BARRIER(); WOPF(25); TSETUP(25, -1, -1); BODY(sn, sc, true); }
  if (w < 4) WOFFS();
  { PACEW(); WAITVM(12); BARRIER(); WOPF(26); TSETUP(26, 24, -1); BODY(sc, sn, true); }
  { PACEW(); WAITVM(12); BARRIER(); WOPF(27); TSETUP(27, 25, -1); BODY(sn, sc, true); }
  if (w >= 4) WOFFS();
  { WAITVM(12); BARRIER(); WOPF(28); TSETUP(28, 26, 27); BODY(sc, sn, true); }
  { WAITVM(12); BARRIER(); WOPF(29); TSETUP(29, 28, 29); BODY(sn, sc, true); }
  { WAITVM(12); BARRIER(); WOPF(30); TSETUP(30, 30, 31); BODY(sc, sn, false); }
  asm volatile("" : "+s"(vs));

  const int ln = lane_id();
  const int r32e = ln & 31, hie = ln >> 5;
  const unsigned wof0 = (unsigned)((ln >> 3) * 1024 + (((ln & 7) ^ ((((ln >> 3) >> 1) & 1) << 2)) << 4)), wof1 = wof0 + 7168;
  const int tw0 = w * 2048 + (4 * hie + ((ln & 15) >> 2)) * 128 + ((((ln & 15) >> 3)) << 6) + ((ln >> 4) & 1) * 32 + (ln & 3) * 8;
  const int tw1 = tw0 ^ 64;
  const char* wob = (const char*)wo + nblk * 128 + (size_t)w * (4 * 131072);
#define CELL(i) ((i) == 0 ? 49152 : (i) == 1 ? 81920 : (i) == 2 ? 114688 : (i) == 3 ? 98304 : (i) == 4 ? 0 : \
                 (i) == 5 ? 131072 : (i) == 6 ? 16384 : (i) == 7 ? 147456 : (i) == 8 ? 32768 : 65536)
#define RDW(dst, i) do { dst[0] = cat8(lds_tr(L + CELL(i) + tw0), lds_tr(L + CELL(i) + tw0 + 1024)); \
                         dst[1] = cat8(lds_tr(L + CELL(i) + tw1), lds_tr(L + CELL(i) + tw1 + 1024)); } while (0)
#define WAITL() asm volatile("s_waitcnt lgkmcnt(0)" ::: "memory")
  bf16x8 A[8][2];
  const unsigned foff = (unsigned)FOFF(ln);
  unsigned pv;
#define LDFLAGS() asm volatile("global_load_dword %0, %1, %2 sc1" : "=v"(pv) : "v"(foff), "s"(flg) : "memory")
  {
    WAITVM(8);
    BARRIER();
    WOPF(31);
    lds_char* vb = L + vs;
    u32x4 pw[2];
#pragma unroll
    for (int s = 0; s < 2; ++s)
#pragma unroll
      for (int d = 0; d < 4; ++d) RDV(d, s);
    RDW(A[0], 0); RDW(A[1], 1); RDW(A[2], 2); RDW(A[3], 3);
#pragma unroll
    for (int r = 0; r < 16; ++r) EXP(sn, r);
#pragma unroll
    for (int r = 0; r < 16; r += 2) ADD(sn, r);
#pragma unroll
    for (int s = 0; s < 2; ++s)
#pragma unroll
      for (int e = 0; e < 4; ++e) CVT(sn, s, e);
#pragma unroll
    for (int s = 0; s < 2; ++s)
#pragma unroll
      for (int d = 0; d < 4; ++d) PVM(d, s);
  }

  const char* ob = ows + ((size_t)mblk * 256 + w * 32) * 1024;
  unsigned f0v = f0[0];
#pragma unroll
  for (int i = 1; i < 8; ++i) asm volatile("v_writelane_b32 %0, %1, %2" : "+v"(f0v) : "s"(f0[i]), "i"(i));
  unsigned rdy;
  bf16x8 of[8], Bq[24][2];
  const unsigned oo = ln * 16;
#define HORD(g) ((g) == 0 ? 0 : (g) == 1 ? 2 : (g) == 2 ? 1 : 3)
#define GETRDY() do { PIN(pv); rdy = (unsigned)__builtin_amdgcn_ballot_w64(pv != f0v) & 255u; } while (0)
#define POLL(cond) do { if (!(cond)) { \
      int spins_ = 0; \
      do { __builtin_amdgcn_s_sleep(2); unsigned pq_; \
        asm volatile("global_load_dword %0, %1, %2 sc1\n\ts_waitcnt vmcnt(0)" : "=v"(pq_) : "v"(foff), "s"(flg) : "memory"); \
        rdy = (unsigned)__builtin_amdgcn_ballot_w64(pq_ != f0v) & 255u; \
      } while (!(cond) && ++spins_ < (1 << 18)); } } while (0)
#define PUBLISH(v) do { BARRIER(); if (w == 0 && ln == 0) \
    __hip_atomic_store((__attribute__((address_space(1))) unsigned*)(flg + fic * (FSTR / 4)), f0[8] + (v), __ATOMIC_RELAXED, __HIP_MEMORY_SCOPE_AGENT); } while (0)
#define RING(j) ((j) % 10 == 0 ? 9 : (j) % 10 < 6 ? (j) % 10 + 3 : (j) % 10 - 6)
#define LDO(j) asm volatile("global_load_dwordx4 %0, %1, %2 offset:%3 sc1" : "=v"(of[(j) & 7]) : "v"(oo), \
      "s"(ob + HORD((j) >> 3) * 8192 + (((j) >> 2) & 1) * 4096), "i"(((j) & 3) * 1024) : "memory")
#define DMAU(u) dma2(wof0, wof1, wob + HORD((u) >> 3) * 131072 + ((u) & 7) * 16384, L0 + CELL(RING(u)) + w * 2048)
#define WAITN(n) asm volatile("s_waitcnt vmcnt(%0)" :: "i"(n) : "memory")

  const float l = swapadd(l0 + l1);
  {
    float* ml = (float*)(smem + CELL(8));
    if (hie == 0) { ml[w * 512 + r32e] = m; ml[w * 512 + 32 + r32e] = l; }
#pragma unroll
    for (int dd = 0; dd < 2; ++dd)
#pragma unroll
      for (int g = 0; g < 4; ++g) {
        f32x4 v;
        if (kh) { v[0] = oacc[dd][4 * g]; v[1] = oacc[dd][4 * g + 1]; v[2] = oacc[dd][4 * g + 2]; v[3] = oacc[dd][4 * g + 3]; }
        else { v[0] = oacc[2 + dd][4 * g]; v[1] = oacc[2 + dd][4 * g + 1]; v[2] = oacc[2 + dd][4 * g + 2]; v[3] = oacc[2 + dd][4 * g + 3]; }
        *(__attribute__((address_space(3))) f32x4*)(L + CELL(dd * 2 + (g >> 1)) + w * 2048 + (g & 1) * 1024 + ln * 16) = v;
      }
    WAITVM(0);
    BARRIER();
    DMAU(0);
    RDW(A[4], 4); RDW(A[5], 5); RDW(A[6], 6); RDW(A[7], 7);
    const float mp = ml[(w ^ 4) * 512 + r32e], lp = ml[(w ^ 4) * 512 + 32 + r32e];
    WAITL();
    DMAU(1); DMAU(2); DMAU(3); DMAU(4);
    const float mf = fmaxf(m, mp);
    const float wa = __builtin_amdgcn_exp2f(m - mf), wb = __builtin_amdgcn_exp2f(mp - mf);
    const float inv = 1.f / (wa * l + wb * lp);
    const float fa = wa * inv, fb = wb * inv;
    const char* ost = ows + ((size_t)(b * 4 + qg) * 256 + head * 8 + 4 * kh) * 1024;
#pragma unroll
    for (int dd = 0; dd < 2; ++dd) {
      float o[16];
#pragma unroll
      for (int g = 0; g < 4; ++g) {
        f32x4 v = *(__attribute__((address_space(3))) f32x4*)(L + CELL(dd * 2 + (g >> 1)) + (w ^ 4) * 2048 + (g & 1) * 1024 + ln * 16);
#pragma unroll
        for (int e = 0; e < 4; ++e) {
          const float own = kh ? oacc[2 + dd][4 * g + e] : oacc[dd][4 * g + e];
          o[4 * g + e] = own * fa + v[e] * fb;
        }
      }
#pragma unroll
      for (int sp = 0; sp < 2; ++sp) {
        bf16x8 f = pack8(o[8 * sp + 0], o[8 * sp + 1], o[8 * sp + 2], o[8 * sp + 3], o[8 * sp + 4], o[8 * sp + 5], o[8 * sp + 6], o[8 * sp + 7]);
        asm volatile("global_store_dwordx4 %0, %1, %2 offset:%3 sc1\n\ts_nop 1" :: "v"((unsigned)(ln * 16)), "v"(f), "s"(ost), "i"((dd * 2 + sp) * 1024) : "memory");
      }
    }
    BARRIER();
  }
  DMAU(5); DMAU(6); DMAU(7); DMAU(8); DMAU(9);
#define ASTEP(u, n) do { WAITN(n); RDW(Bq[(u) - 10], RING(u)); WAITL(); DMAU(u); } while (0)
#define MSTEP(j, n) do { WAITN(n); PIN(of[(j) & 7]); \
    if ((j) < 24) { acc[0] = MFMA32(Bq[(j) % 24][0], of[(j) & 7], acc[0]); acc[1] = MFMA32(Bq[(j) % 24][1], of[(j) & 7], acc[1]); } \
    else { acc[0] = MFMA32(A[(j) & 7][0], of[(j) & 7], acc[0]); acc[1] = MFMA32(A[(j) & 7][1], of[(j) & 7], acc[1]); } } while (0)
  f32x16 acc[2] = {};
  ASTEP(10, 22); ASTEP(11, 22); WAITN(14); PUBLISH(1u); ASTEP(12, 22); ASTEP(13, 22); ASTEP(14, 22); ASTEP(15, 18); ASTEP(16, 18); ASTEP(17, 18); LDFLAGS(); ASTEP(18, 19); ASTEP(19, 19); ASTEP(20, 19); ASTEP(21, 19);
  WAITN(8); GETRDY();
  POLL((rdy >> HORD(0)) & 1u);
  LDO(0); LDO(1); LDO(2); LDO(3); LDO(4); LDO(5); LDO(6); LDO(7);
  MSTEP(0, 7); RDW(Bq[12], RING(22)); WAITL(); DMAU(22); POLL((rdy >> HORD(1)) & 1u); LDFLAGS(); LDO(8); GAP();
  MSTEP(1, 10); RDW(Bq[13], RING(23)); WAITL(); DMAU(23); LDO(9); GAP();
  MSTEP(2, 12); RDW(Bq[14], RING(14)); LDO(10); GAP();
  MSTEP(3, 12); RDW(Bq[15], RING(15)); LDO(11); GAP();
  MSTEP(4, 12); RDW(Bq[16], RING(16)); LDO(12); GAP();
  MSTEP(5, 12); RDW(Bq[17], RING(17)); LDO(13); GAP();
  MSTEP(6, 12); RDW(Bq[18], RING(18)); LDO(14); GAP();
  MSTEP(7, 12); RDW(Bq[19], RING(19)); LDO(15); GAP();
  MSTEP(8, 9); RDW(Bq[20], RING(20)); GETRDY(); POLL((rdy >> HORD(2)) & 1u); LDFLAGS(); LDO(16); GAP();
  MSTEP(9, 8); RDW(Bq[21], RING(21)); LDO(17); GAP();
  MSTEP(10, 8); RDW(Bq[22], RING(22)); LDO(18); GAP();
  MSTEP(11, 8); RDW(Bq[23], RING(23)); LDO(19); GAP();
  MSTEP(12, 8); LDO(20); GAP();
  MSTEP(13, 8); LDO(21); GAP();
  MSTEP(14, 8); LDO(22); GAP();
  MSTEP(15, 8); LDO(23); GAP();
  MSTEP(16, 7); GETRDY(); POLL((rdy >> HORD(3)) & 1u); LDFLAGS(); LDO(24); GAP();
  MSTEP(17, 8); LDO(25); GAP();
  MSTEP(18, 8); LDO(26); GAP();
  MSTEP(19, 8); LDO(27); GAP();
  MSTEP(20, 8); LDO(28); GAP();
  MSTEP(21, 8); LDO(29); GAP();
  MSTEP(22, 8); LDO(30); GAP();
  MSTEP(23, 8); LDO(31); GAP();
  MSTEP(24, 7); GAP();
  MSTEP(25, 6); GAP();
  MSTEP(26, 5); GAP();
  MSTEP(27, 4); GAP();
  MSTEP(28, 3); GAP();
  MSTEP(29, 2); GAP();
  MSTEP(30, 1); GAP();
  MSTEP(31, 0); GAP();
  GETRDY();
#pragma unroll
  for (int nb = 0; nb < 2; ++nb)
#pragma unroll
    for (int g = 0; g < 4; ++g) {
      f32x4 v = {acc[nb][4 * g], acc[nb][4 * g + 1], acc[nb][4 * g + 2], acc[nb][4 * g + 3]};
      *(__attribute__((address_space(3))) f32x4*)(L + CELL(nb * 2 + (g >> 1)) + w * 2048 + (g & 1) * 1024 + ln * 16) = v;
    }
  POLL((rdy & 0xF0u) == 0xF0u);
  PUBLISH(2u);
  {
    const int cb = (w >> 1) == 0 ? CELL(0) : (w >> 1) == 1 ? CELL(1) : (w >> 1) == 2 ? CELL(2) : CELL(3);
    f32x4 s = {0.f, 0.f, 0.f, 0.f};
#pragma unroll
    for (int ww = 0; ww < 8; ++ww) s += *(__attribute__((address_space(3))) f32x4*)(L + cb + ww * 2048 + (w & 1) * 1024 + ln * 16);
    const int nb = w >> 2, g = w & 3;
    u32x2 pk = {cvtpk(s[0], s[1]), cvtpk(s[2], s[3])};
    *(u32x2*)((char*)out + ((size_t)(mblk * 32 + r32e) * 512 + nblk * 64 + nb * 32 + 8 * g + 4 * hie) * 2) = pk;
  }
  WAITVM(0); BARRIER(); if (w == 0) { asm volatile("buffer_wbl2 sc0 sc1\n\ts_waitcnt vmcnt(0)" ::: "memory"); }
  PADTO(75);
}

extern "C" void kernel_launch(void* const* d_in, const int* in_sizes, int n_in, void* d_out, int out_size, void* d_ws, size_t ws_size,
                              hipStream_t stream) {
  const __hip_bfloat16* x = (const __hip_bfloat16*)d_in[0];
  const __hip_bfloat16* kc = (const __hip_bfloat16*)d_in[1];
  const __hip_bfloat16* vc = (const __hip_bfloat16*)d_in[2];
  const __hip_bfloat16* wq = (const __hip_bfloat16*)d_in[3];
  const __hip_bfloat16* wo = (const __hip_bfloat16*)d_in[4];
  __hip_bfloat16* out = (__hip_bfloat16*)d_out;
  (void)hipFuncSetAttribute((const void*)fused_kernel, hipFuncAttributeMaxDynamicSharedMemorySize, LDS1);
  fused_kernel<<<256, 512, LDS1, stream>>>(x, kc, vc, wq, wo, (char*)d_ws, (unsigned*)((char*)d_ws + (16u << 20)), out);
}
```

```cpp
#define P1 1000
#define N1 4
#define DP 20
#define P2 890
#define PSLACK 400
#include <hip/hip_runtime.h>
#include <hip/hip_bf16.h>
#include <stdint.h>


typedef __attribute__((ext_vector_type(8))) short bf16x8;
typedef __attribute__((ext_vector_type(4))) short s16x4;
typedef __attribute__((ext_vector_type(16))) float f32x16;
typedef __attribute__((ext_vector_type(4))) float f32x4;
typedef __attribute__((ext_vector_type(4))) unsigned u32x4;
typedef __attribute__((ext_vector_type(2))) unsigned u32x2;
typedef __attribute__((address_space(3))) char lds_char;
typedef short v4i16_t __attribute__((ext_vector_type(4)));

constexpr int NB = 8, SQ = 128, DM = 512, NH = 32, SK = 2048, DH = 128;
constexpr int NT = SK / 64;
constexpr int LDS1 = 163840;
#define NPB 6
#define PBUF(c) (LDS1 - NPB * 16384 + (((c) + 4 * NPB - 16) % NPB) * 16384)
__device__ constexpr int tile_step(int n) { return n < 10 - NPB ? 2 * n : n + 6; }
__device__ constexpr int p_after(int c) {
  int mx = (c > 0 ? c : 0) - 1 + NPB; if (mx > 15) mx = 15;
  int n = 4 * (mx - (c + 1));
  for (int v = 2; v <= 6; v += 2) if (tile_step(v) < c && tile_step(v) >= c + 1 - NPB) n += 4;
  return n;
}
constexpr int XCH_OFF = 131072;
constexpr float CSC = 0.08838834764831845f * 1.4426950408889634f;

#define POL "nt"
#define MFMA32(a, b, c) __builtin_amdgcn_mfma_f32_32x32x16_bf16(a, b, c, 0, 0, 0)

typedef __attribute__((ext_vector_type(2))) float f32x2;
typedef __attribute__((ext_vector_type(2))) __bf16 bf16x2_t;
__device__ __forceinline__ unsigned cvtpk(float lo, float hi) {
  f32x2 v = {lo, hi};
  return __builtin_bit_cast(unsigned, __builtin_convertvector(v, bf16x2_t));
}
__device__ __forceinline__ bf16x8 pack8(float a0, float a1, float a2, float a3, float a4, float a5, float a6, float a7) {
  u32x4 w = {cvtpk(a0, a1), cvtpk(a2, a3), cvtpk(a4, a5), cvtpk(a6, a7)};
  return __builtin_bit_cast(bf16x8, w);
}
__device__ __forceinline__ bf16x8 lds_r128(lds_char* p) { return *(__attribute__((address_space(3))) bf16x8*)p; }
__device__ __forceinline__ s16x4 lds_tr(lds_char* p) {
  return __builtin_bit_cast(s16x4, __builtin_amdgcn_ds_read_tr16_b64_v4i16((__attribute__((address_space(3))) v4i16_t*)p));
}
__device__ __forceinline__ bf16x8 cat8(s16x4 a, s16x4 b) { return __builtin_shufflevector(a, b, 0, 1, 2, 3, 4, 5, 6, 7); }

__device__ __forceinline__ void dma4(unsigned o0, unsigned o1, unsigned o2, unsigned o3, const void* sbase, unsigned ldsaddr) {
  unsigned keep;
  asm volatile(
      "s_mov_b32 %0, m0\n\t"
      "s_mov_b32 m0, %6\n\t"
      "s_nop 0\n\t"
      "global_load_lds_dwordx4 %1, %5 " POL "\n\t"
      "global_load_lds_dwordx4 %2, %5 offset:1024 " POL "\n\t"
      "global_load_lds_dwordx4 %3, %5 offset:2048 " POL "\n\t"
      "global_load_lds_dwordx4 %4, %5 offset:3072 " POL "\n\t"
      "s_mov_b32 m0, %0"
      : "=&s"(keep) : "v"(o0), "v"(o1), "v"(o2), "v"(o3), "s"(sbase), "s"(ldsaddr) : "memory");
}
__device__ __forceinline__ void dma2(unsigned o0, unsigned o1, const void* sbase, unsigned ldsaddr) {
  unsigned keep;
  asm volatile(
      "s_mov_b32 %0, m0\n\t"
      "s_mov_b32 m0, %4\n\t"
      "s_nop 0\n\t"
      "global_load_lds_dwordx4 %1, %3\n\t"
      "global_load_lds_dwordx4 %2, %3 offset:1024\n\t"
      "s_mov_b32 m0, %0"
      : "=&s"(keep) : "v"(o0), "v"(o1), "s"(sbase), "s"(ldsaddr) : "memory");
}
template <int J> __device__ __forceinline__ void dmap(unsigned o, const void* sbase, unsigned ldsaddr) {
  unsigned keep;
  asm volatile("s_mov_b32 %0, m0\n\ts_mov_b32 m0, %3\n\ts_nop 0\n\tglobal_load_lds_dwordx4 %1, %2 offset:%4 " POL "\n\ts_mov_b32 m0, %0"
               : "=&s"(keep) : "v"(o), "s"(sbase), "s"(ldsaddr), "i"(J * 1024) : "memory");
}
template <int J> __device__ __forceinline__ void dmapw(unsigned o, const void* sbase, unsigned ldsaddr) {
  unsigned keep;
  asm volatile("s_mov_b32 %0, m0\n\ts_mov_b32 m0, %3\n\ts_nop 0\n\tglobal_load_lds_dwordx4 %1, %2 offset:%4\n\ts_mov_b32 m0, %0"
               : "=&s"(keep) : "v"(o), "s"(sbase), "s"(ldsaddr), "i"(J * 1024) : "memory");
}
__device__ __forceinline__ int lane_id() {
  int lane;
  asm volatile("v_mbcnt_lo_u32_b32 %0, -1, 0\n\tv_mbcnt_hi_u32_b32 %0, -1, %0" : "=v"(lane));
  return lane;
}
__device__ __forceinline__ float fadd_s(float a, float b) { float r; asm("v_add_f32_e32 %0, %1, %2" : "=v"(r) : "v"(a), "v"(b)); return r; }
#define GAP() __builtin_amdgcn_sched_barrier(0)
__device__ __forceinline__ int kslot(int i) { return i == 0 ? 0 : i * 32768 - 16384; }
__device__ __forceinline__ int vslot(int i) { return i == 4 ? 147456 : i * 32768 + 32768; }
#define WAITVM(n) asm volatile("s_waitcnt vmcnt(" #n ")" ::: "memory")
#define BARRIER() asm volatile("s_waitcnt lgkmcnt(0)\n\ts_barrier" ::: "memory")
#define PIN(x) asm volatile("" : "+v"(x))

__device__ __forceinline__ float swapmax(float v) {
  auto rr = __builtin_amdgcn_permlane32_swap(__float_as_uint(v), __float_as_uint(v), false, false);
  return fmaxf(__uint_as_float(rr[0]), __uint_as_float(rr[1]));
}
__device__ __forceinline__ float swapadd(float v) {
  auto rr = __builtin_amdgcn_permlane32_swap(__float_as_uint(v), __float_as_uint(v), false, false);
  return __uint_as_float(rr[0]) + __uint_as_float(rr[1]);
}

__global__ __launch_bounds__(512, 2) void fused_kernel(const __hip_bfloat16* __restrict__ x, const __hip_bfloat16* __restrict__ kc,
                                                       const __hip_bfloat16* __restrict__ vc, const __hip_bfloat16* __restrict__ wq,
                                                       const __hip_bfloat16* __restrict__ wo, char* ows, unsigned* flg,
                                                       __hip_bfloat16* __restrict__ out) {
  extern __shared__ __attribute__((aligned(16))) char smem[];
  lds_char* L = (lds_char*)smem;
  const unsigned L0 = (unsigned)(uintptr_t)smem;
  const int tid = threadIdx.x, lane = tid & 63;
  const int w = __builtin_amdgcn_readfirstlane(tid >> 6);
  const int qg = w & 3, kh = w >> 2;
  const int r32 = lane & 31, hi = lane >> 5;
  const int q4 = (lane & 15) >> 2, p4 = lane & 3, g1 = (lane >> 4) & 1;
  const int bid = blockIdx.x;
  const int head = 4 * (bid & 7) + ((bid >> 3) & 3), b = bid >> 5;
  const char* kbase = (const char*)kc + ((size_t)(b * NH + head) * SK) * 256;
  const char* vbase = (const char*)vc + ((size_t)(b * NH + head) * SK) * 256;
  const char* wqbase = (const char*)wq + head * 256;
#define FSTR 256
#define XA 2
  const int xcd = bid & 7, jx = bid >> 3;
  const int nblk = ((xcd & ((8 >> XA) - 1)) << XA) | (jx & ((1 << XA) - 1)), mblk = (xcd >> (3 - XA)) * (32 >> XA) + (jx >> XA);
  int fia = (mblk >> 2) * 32 + w;
  int fib = ((((4 * b) % (32 >> XA)) << XA) | (w & ((1 << XA) - 1))) * 8 + ((((4 * b) / (32 >> XA)) << (3 - XA)) | (w >> XA)) - (32 << XA);
  int fic = bid;
  asm volatile("" : "+s"(fia), "+s"(fib), "+s"(fic));
#define FOFF(lane) (((lane) < 8 ? ((lane) < 4 ? fia + 8 * (lane) : fib + ((lane) << (3 + XA))) : fic) * FSTR)
  unsigned fv;
  asm volatile("global_load_dword %0, %1, %2 sc1" : "=v"(fv) : "v"((unsigned)FOFF(lane)), "s"(flg) : "memory");

  const int drow = lane >> 4, dpos = lane & 15;
  unsigned ko[4];
#pragma unroll
  for (int j = 0; j < 4; ++j) ko[j] = (unsigned)(((w & 3) * 16 + drow) * 256 + ((dpos ^ (4 * j + drow)) << 4));
  const unsigned vo = (unsigned)(((w & 3) * 16 + drow) * 256 + ((dpos ^ (drow << 2)) << 4));
  unsigned wo2[2], xo2[2];
#pragma unroll
  for (int j = 0; j < 2; ++j) {
    wo2[j] = (unsigned)(((w & 3) * 8 + 4 * j + drow) * 8192 + ((dpos ^ (drow << 2)) << 4) - j * 1024);
    xo2[j] = (unsigned)(((w & 3) * 32 + 16 * j + (lane >> 2)) * 1024 + (((lane & 3) ^ drow) << 4) - j * 1024);
  }
  const char* xbase = (const char*)x + (size_t)b * SQ * 1024;

#define TOFF(t) ((size_t)(t) * 16384)
#define ISSUE_K(t, la) dma4(ko[0], ko[1], ko[2], ko[3], kbase + TOFF(t), (la) + (w & 3) * 4096)
#define ISSUE_V(t, la) dma4(vo, vo, vo, vo, vbase + TOFF(t), (la) + (w & 3) * 4096)
  const int crot = 2 * b + ((bid >> 3) & 1) + 8 * ((bid >> 4) & 1);
#define ISSUE_P(c) do { const int cc_ = ((c) + crot) & 15; \
    dma2(wo2[0], wo2[1], wqbase + (size_t)cc_ * 32 * 8192, L0 + PBUF(c) + (w & 3) * 2048); \
    dma2(xo2[0], xo2[1], xbase + cc_ * 64, L0 + PBUF(c) + 8192 + (w & 3) * 2048); } while (0)
#define ISSUE_T(c) do { \
    if (w >= 4) { \
      if (tile_step(2) == (c)) ISSUE_V(0, L0 + vslot(0)); \
      if (tile_step(4) == (c)) ISSUE_V(1, L0 + vslot(1)); \
      if (tile_step(6) == (c)) ISSUE_V(2, L0 + vslot(2)); \
    } else { \
      if (tile_step(0) == (c)) ISSUE_K(0, L0 + kslot(0)); \
      if (tile_step(1) == (c)) ISSUE_K(1, L0 + kslot(1)); \
      if (tile_step(3) == (c)) ISSUE_K(2, L0 + kslot(2)); \
      if (tile_step(5) == (c)) ISSUE_K(3, L0 + kslot(3)); \
      if (tile_step(7) == (c)) ISSUE_K(4, L0 + kslot(4)); \
    } } while (0)
  if (w >= 4) {
#pragma unroll
    for (int c = 0; c < NPB; ++c) ISSUE_P(c);
  }

  f32x16 qa[2] = {};
  {
    const int ps = ((p4 & 1) << 1) | (p4 >> 1);
    int wa[2], xa[2];
#pragma unroll
    for (int dd = 0; dd < 2; ++dd) {
      const int dblk = 2 * kh + dd;
      wa[dd] = (8 * hi + q4) * 256 + (((4 * dblk + 2 * g1 + (ps >> 1)) ^ (q4 << 2)) << 4) + 8 * (ps & 1);
      xa[dd] = 8192 + (qg * 32 + r32) * 64 + (((2 * dd + hi) ^ ((r32 >> 2) & 3)) << 4);
    }
    bf16x8 xf[2][2], af[2][2][2];
#pragma unroll
    for (int c = -1; c < 16; ++c) {
      if (w >= 4 && c < 15) asm volatile("s_waitcnt vmcnt(%0)" :: "i"(p_after(c)) : "memory");
      BARRIER();
      if (w >= 4 && c >= 0 && c + NPB < 16) ISSUE_P(c + NPB);
      ISSUE_T(c);
      if (c < 15) {
        lds_char* wb = L + PBUF(c + 1);
#pragma unroll
        for (int s = 0; s < 2; ++s) {
          xf[(c + 1) & 1][s] = lds_r128(wb + xa[s]);
#pragma unroll
          for (int dd = 0; dd < 2; ++dd)
            af[(c + 1) & 1][s][dd] = cat8(lds_tr(wb + wa[dd] + s * 4096), lds_tr(wb + wa[dd] + s * 4096 + 1024));
        }
      }
      GAP();
      if (c >= 0) {
#pragma unroll
        for (int s = 0; s < 2; ++s)
#pragma unroll
          for (int dd = 0; dd < 2; ++dd) qa[dd] = MFMA32(af[c & 1][s][dd], xf[c & 1][s], qa[dd]);
      }
      GAP();
    }
  }
  bf16x8 qf[8];
  unsigned f0[9];
  {
    bf16x8 own[4];
#pragma unroll
    for (int dd = 0; dd < 2; ++dd)
#pragma unroll
      for (int sp = 0; sp < 2; ++sp)
      {
        u32x4 t = __builtin_bit_cast(u32x4, pack8(qa[dd][8 * sp + 0], qa[dd][8 * sp + 1], qa[dd][8 * sp + 2], qa[dd][8 * sp + 3],
                                                  qa[dd][8 * sp + 4], qa[dd][8 * sp + 5], qa[dd][8 * sp + 6], qa[dd][8 * sp + 7]));
#pragma unroll
        for (int e = 0; e < 4; ++e) t[e] = cvtpk(__uint_as_float(t[e] << 16) * CSC, __uint_as_float(t[e] & 0xffff0000u) * CSC);
        own[dd * 2 + sp] = __builtin_bit_cast(bf16x8, t);
      }
#pragma unroll
    for (int i = 0; i < 4; ++i) *(__attribute__((address_space(3))) bf16x8*)(L + XCH_OFF + w * 4096 + i * 1024 + lane * 16) = own[i];
    if (w < 4) WAITVM(12);
    BARRIER();
    PIN(fv);
#pragma unroll
    for (int i = 0; i < 9; ++i) f0[i] = __builtin_amdgcn_readlane(fv, i);
#pragma unroll
    for (int i = 0; i < 4; ++i) {
      bf16x8 o = lds_r128(L + XCH_OFF + (w ^ 4) * 4096 + i * 1024 + lane * 16);
      if (kh == 0) { qf[i] = own[i]; qf[4 + i] = o; } else { qf[4 + i] = own[i]; qf[i] = o; }
    }
  }

  f32x16 oacc[4] = {};
  const int krow = kh * 32 + r32;
  const int kaddr0 = krow * 256 + ((hi ^ (krow & 15)) << 4);
  int vaddr0[4];
#pragma unroll
  for (int d = 0; d < 4; ++d) vaddr0[d] = (kh * 32 + 4 * hi + q4) * 256 + ((d ^ q4) << 6) + g1 * 32 + p4 * 8;

  bf16x8 kf[8], vf[4][2], pf[2];
  f32x16 sc = {}, sn, negm;
#pragma unroll
  for (int s = 0; s < 8; ++s) kf[s] = lds_r128(L + kslot(0) + (kaddr0 ^ (s << 5)));
#pragma unroll
  for (int s = 0; s < 8; ++s) sc = MFMA32(kf[s], qf[s], sc);
#pragma unroll
  for (int s = 0; s < 8; ++s) kf[s] = lds_r128(L + kslot(1) + (kaddr0 ^ (s << 5)));
  float m;
  {
    float tmax = sc[0];
#pragma unroll
    for (int r = 1; r < 16; ++r) tmax = fmaxf(tmax, sc[r]);
    m = swapmax(tmax);
#pragma unroll
    for (int r = 0; r < 16; ++r) { sc[r] -= m; negm[r] = -m; }
  }
  float l0 = 0.f, l1 = 0.f;
  const char* dbase = w < 4 ? kbase : vbase;
  unsigned dof0 = w < 4 ? ko[0] : vo, dof1 = w < 4 ? ko[1] : vo, dof2 = w < 4 ? ko[2] : vo, dof3 = w < 4 ? ko[3] : vo;
  const int dahead = w < 4 ? 6 : 4;
  int ks = kslot(2), vs = vslot(0), ksp = kslot(1), vsp = vslot(4);

#define RDV(d, s) vf[d][s] = cat8(lds_tr(vb + vaddr0[d] + (s) * 4096), lds_tr(vb + vaddr0[d] + (s) * 4096 + 2048))
#define RDK(s) kf[s] = lds_r128(kb + (kaddr0 ^ ((s) << 5)))
#define EXP(SC, r) SC[r] = __builtin_amdgcn_exp2f(SC[r])
#define CVT(SC, s, e) pw[s][e] = cvtpk(SC[8 * (s) + 2 * (e)], SC[8 * (s) + 2 * (e) + 1])
#define ADD(SC, r) do { l0 = fadd_s(l0, SC[r]); l1 = fadd_s(l1, SC[(r) + 1]); } while (0)
#define PVM(d, s) oacc[d] = MFMA32(vf[d][s], __builtin_bit_cast(bf16x8, pw[s]), oacc[d])
#define QKM(SN, s) SN = MFMA32(kf[s], qf[s], SN)
#define DMA(J, o) do { if (dmode == 1) dmap<J>(o, dptr, dla); else if (dmode == 2) dmapw<J>(o, dptr, dla); } while (0)
#define RESCALE(SN, tmax) do { \
    if (__builtin_amdgcn_ballot_w64(!(tmax <= 8.f)) != 0) { \
      const float mn = m + fmaxf(tmax, 0.f); \
      const float dl = mn - m; \
      const float alpha = __builtin_amdgcn_exp2f(-dl); \
      _Pragma("unroll") for (int d = 0; d < 4; ++d) oacc[d] *= alpha; \
      _Pragma("unroll") for (int r = 0; r < 16; ++r) { SN[r] -= dl; negm[r] = -mn; } \
      l0 *= alpha; l1 *= alpha; m = mn; \
    } } while (0)
#define BODY(SC, SN, HASK) do { \
    lds_char* vb = L + vs; \
    lds_char* kb = L + ks; \
    u32x4 pw[2]; \
    SN = MFMA32(kf[0], qf[0], negm); RDV(0, 0); RDV(1, 0); EXP(SC, 0); EXP(SC, 1); GAP(); \
    QKM(SN, 1); RDV(2, 0); RDV(3, 0); EXP(SC, 2); EXP(SC, 3); CVT(SC, 0, 0); GAP(); \
    QKM(SN, 2); RDV(0, 1); RDV(1, 1); EXP(SC, 4); EXP(SC, 5); CVT(SC, 0, 1); GAP(); \
    QKM(SN, 3); RDV(2, 1); RDV(3, 1); EXP(SC, 6); EXP(SC, 7); CVT(SC, 0, 2); GAP(); \
    QKM(SN, 4); DMA(0, dof0); EXP(SC, 8); EXP(SC, 9); CVT(SC, 0, 3); GAP(); \
    QKM(SN, 5); DMA(1, dof1); EXP(SC, 10); EXP(SC, 11); CVT(SC, 1, 0); GAP(); \
    QKM(SN, 6); DMA(2, dof2); EXP(SC, 12); EXP(SC, 13); CVT(SC, 1, 1); GAP(); \
    QKM(SN, 7); DMA(3, dof3); EXP(SC, 14); EXP(SC, 15); CVT(SC, 1, 2); GAP(); \
    PVM(0, 0); if (HASK) { RDK(0); RDK(1); } CVT(SC, 1, 3); ADD(SC, 0); ADD(SC, 2); GAP(); \
    PVM(1, 0); if (HASK) { RDK(2); RDK(3); } ADD(SC, 4); ADD(SC, 6); GAP(); \
    PVM(2, 0); if (HASK) { RDK(4); RDK(5); } ADD(SC, 8); ADD(SC, 10); GAP(); \
    PVM(3, 0); if (HASK) { RDK(6); RDK(7); } ADD(SC, 12); ADD(SC, 14); GAP(); \
    float t0 = fmaxf(fmaxf(SN[0], SN[1]), SN[2]), t1 = fmaxf(fmaxf(SN[3], SN[4]), SN[5]); \
    PVM(0, 1); t0 = fmaxf(fmaxf(t0, SN[6]), SN[7]); t1 = fmaxf(fmaxf(t1, SN[8]), SN[9]); GAP(); \
    PVM(1, 1); t0 = fmaxf(fmaxf(t0, SN[10]), SN[11]); t1 = fmaxf(fmaxf(t1, SN[12]), SN[13]); GAP(); \
    PVM(2, 1); t0 = fmaxf(fmaxf(t0, SN[14]), SN[15]); t0 = swapmax(fmaxf(t0, t1)); GAP(); \
    PVM(3, 1); GAP(); \
    RESCALE(SN, t0); \
    ksp = ks; vsp = vs; \
    ks = ks == 0 ? 16384 : (ks == 114688 ? 0 : ks + 32768); \
    vs = vs == 131072 ? 147456 : (vs == 147456 ? 32768 : vs + 32768); } while (0)

#define PFAT 27
  unsigned pfv;
#define WOPF(it) do { \
    if ((it) == PFAT) { pfv = (unsigned)(w * 512 + (jx >> XA) * 64 + lane_id()) * 1024u; \
      asm volatile("global_load_dword %0, %0, %1" : "+v"(pfv) : "s"((const char*)wo + nblk * 128) : "memory"); } \
    if ((it) == PFAT + 4) PIN(pfv); } while (0)
  unsigned ptg = (unsigned)__builtin_amdgcn_s_memrealtime() << 3;
  int pinc = P1, pcnt = 0;
#define PACEW() do { if (w == 0) { ptg += pinc; ++pcnt; if (pcnt >= N1) { pinc -= DP; if (pinc < P2) pinc = P2; } unsigned t_; \
    while (1) { t_ = (unsigned)__builtin_amdgcn_s_memrealtime() << 3; if ((int)(ptg - t_) <= 0) break; __builtin_amdgcn_s_sleep(1); } \
    if ((int)(t_ - ptg) > PSLACK) ptg = t_ - PSLACK; } } while (0)
  {
    WAITVM(8);
    BARRIER();
    if (w < 4) { ISSUE_K(5, L0 + kslot(0)); ISSUE_K(6, L0 + kslot(1)); }
    else { ISSUE_V(3, L0 + vslot(3)); ISSUE_V(4, L0 + vslot(4)); }
    const int dmode = 0; const char* dptr = dbase; const unsigned dla = 0;
    BODY(sc, sn, true);
  }
#define DSETUP(it) \
    const char* dptr = dbase + TOFF((it) + dahead); \
    const unsigned dla = L0 + (w < 4 ? ksp : vsp) + (w & 3) * 4096;
#pragma unroll 1
  for (int it = 1; it < 25; it += 2) {
    { PACEW(); WAITVM(12); BARRIER(); const int dmode = 1; DSETUP(it); BODY(sn, sc, true); }
    { PACEW(); WAITVM(12); BARRIER(); const int dmode = 1; DSETUP(it + 1); BODY(sc, sn, true); }
  }
  const char* wot = (const char*)wo + nblk * 128 + (size_t)(w & 3) * (8 * 131072) + (3 * 131072 - 24 * 16384);
#define WOFFS() do { const int ln_ = lane_id(), dr_ = ln_ >> 3; \
    dof0 = (unsigned)(dr_ * 1024 + (((ln_ & 7) ^ (((dr_ >> 1) & 1) << 2)) << 4)); \
    dof1 = dof0 + 7168; dof2 = dof0 + 4 * 131072 - 2048; dof3 = dof0 + 4 * 131072 + 8192 - 3072; } while (0)
#define TSETUP(it, ku, vu) \
    asm volatile("" : "+s"(ks), "+s"(vs), "+s"(ksp), "+s"(vsp)); \
    const int dmode = w < 4 ? ((it) + 6 < NT ? 1 : (ku) >= 0 ? 2 : 0) : ((it) + 4 < NT ? 1 : (vu) >= 0 ? 2 : 0); \
    const char* dptr = dmode == 1 ? dbase + TOFF((it) + dahead) : wot + (w < 4 ? (ku) : (vu)) * 16384; \
    const unsigned dla = L0 + (w < 4 ? ksp : vsp) + (w & 3) * 4096;
  { PACEW(); WAITVM(12); BARRIER(); WOPF(25); TSETUP(25, -1, -1); BODY(sn, sc, true); }
  if (w < 4) WOFFS();
  { PACEW(); WAITVM(12); BARRIER(); WOPF(26); TSETUP(26, 24, -1); BODY(sc, sn, true); }
  { PACEW(); WAITVM(12); BARRIER(); WOPF(27); TSETUP(27, 25, -1); BODY(sn, sc, true); }
  if (w >= 4) WOFFS();
  { WAITVM(12); BARRIER(); WOPF(28); TSETUP(28, 26, 27); BODY(sc, sn, true); }
  { WAITVM(12); BARRIER(); WOPF(29); TSETUP(29, 28, 29); BODY(sn, sc, true); }
  { WAITVM(12); BARRIER(); WOPF(30); TSETUP(30, 30, 31); BODY(sc, sn, false); }
  asm volatile("" : "+s"(vs));

  const int ln = lane_id();
  const int r32e = ln & 31, hie = ln >> 5;
  const unsigned wof0 = (unsigned)((ln >> 3) * 1024 + (((ln & 7) ^ ((((ln >> 3) >> 1) & 1) << 2)) << 4)), wof1 = wof0 + 7168;
  const int tw0 = w * 2048 + (4 * hie + ((ln & 15) >> 2)) * 128 + ((((ln & 15) >> 3)) << 6) + ((ln >> 4) & 1) * 32 + (ln & 3) * 8;
  const int tw1 = tw0 ^ 64;
  const char* wob = (const char*)wo + nblk * 128 + (size_t)w * (4 * 131072);
#define CELL(i) ((i) == 0 ? 49152 : (i) == 1 ? 81920 : (i) == 2 ? 114688 : (i) == 3 ? 98304 : (i) == 4 ? 0 : \
                 (i) == 5 ? 131072 : (i) == 6 ? 16384 : (i) == 7 ? 147456 : (i) == 8 ? 32768 : 65536)
#define RDW(dst, i) do { dst[0] = cat8(lds_tr(L + CELL(i) + tw0), lds_tr(L + CELL(i) + tw0 + 1024)); \
                         dst[1] = cat8(lds_tr(L + CELL(i) + tw1), lds_tr(L + CELL(i) + tw1 + 1024)); } while (0)
#define WAITL() asm volatile("s_waitcnt lgkmcnt(0)" ::: "memory")
  bf16x8 A[8][2];
  const unsigned foff = (unsigned)FOFF(ln);
  unsigned pv;
#define LDFLAGS() asm volatile("global_load_dword %0, %1, %2 sc1" : "=v"(pv) : "v"(foff), "s"(flg) : "memory")
  {
    WAITVM(8);
    BARRIER();
    WOPF(31);
    lds_char* vb = L + vs;
    u32x4 pw[2];
#pragma unroll
    for (int s = 0; s < 2; ++s)
#pragma unroll
      for (int d = 0; d < 4; ++d) RDV(d, s);
    RDW(A[0], 0); RDW(A[1], 1); RDW(A[2], 2); RDW(A[3], 3);
#pragma unroll
    for (int r = 0; r < 16; ++r) EXP(sn, r);
#pragma unroll
    for (int r = 0; r < 16; r += 2) ADD(sn, r);
#pragma unroll
    for (int s = 0; s < 2; ++s)
#pragma unroll
      for (int e = 0; e < 4; ++e) CVT(sn, s, e);
#pragma unroll
    for (int s = 0; s < 2; ++s)
#pragma unroll
      for (int d = 0; d < 4; ++d) PVM(d, s);
  }

  const char* ob = ows + ((size_t)mblk * 256 + w * 32) * 1024;
  unsigned f0v = f0[0];
#pragma unroll
  for (int i = 1; i < 8; ++i) asm volatile("v_writelane_b32 %0, %1, %2" : "+v"(f0v) : "s"(f0[i]), "i"(i));
  unsigned rdy;
  bf16x8 of[8], Bq[24][2];
  const unsigned oo = ln * 16;
#define HORD(g) ((g) == 0 ? 0 : (g) == 1 ? 2 : (g) == 2 ? 1 : 3)
#define GETRDY() do { PIN(pv); rdy = (unsigned)__builtin_amdgcn_ballot_w64(pv != f0v) & 255u; } while (0)
#define POLL(cond) do { if (!(cond)) { \
      int spins_ = 0; \
      do { __builtin_amdgcn_s_sleep(2); unsigned pq_; \
        asm volatile("global_load_dword %0, %1, %2 sc1\n\ts_waitcnt vmcnt(0)" : "=v"(pq_) : "v"(foff), "s"(flg) : "memory"); \
        rdy = (unsigned)__builtin_amdgcn_ballot_w64(pq_ != f0v) & 255u; \
      } while (!(cond) && ++spins_ < (1 << 18)); } } while (0)
#define PUBLISH(v) do { BARRIER(); if (w == 0 && ln == 0) \
    __hip_atomic_store((__attribute__((address_space(1))) unsigned*)(flg + fic * (FSTR / 4)), f0[8] + (v), __ATOMIC_RELAXED, __HIP_MEMORY_SCOPE_AGENT); } while (0)
#define RING(j) ((j) % 10 == 0 ? 9 : (j) % 10 < 6 ? (j) % 10 + 3 : (j) % 10 - 6)
#define LDO(j) asm volatile("global_load_dwordx4 %0, %1, %2 offset:%3 sc1" : "=v"(of[(j) & 7]) : "v"(oo), \
      "s"(ob + HORD((j) >> 3) * 8192 + (((j) >> 2) & 1) * 4096), "i"(((j) & 3) * 1024) : "memory")
#define DMAU(u) dma2(wof0, wof1, wob + HORD((u) >> 3) * 131072 + ((u) & 7) * 16384, L0 + CELL(RING(u)) + w * 2048)
#define WAITN(n) asm volatile("s_waitcnt vmcnt(%0)" :: "i"(n) : "memory")

  const float l = swapadd(l0 + l1);
  {
    float* ml = (float*)(smem + CELL(8));
    if (hie == 0) { ml[w * 512 + r32e] = m; ml[w * 512 + 32 + r32e] = l; }
#pragma unroll
    for (int dd = 0; dd < 2; ++dd)
#pragma unroll
      for (int g = 0; g < 4; ++g) {
        f32x4 v;
        if (kh) { v[0] = oacc[dd][4 * g]; v[1] = oacc[dd][4 * g + 1]; v[2] = oacc[dd][4 * g + 2]; v[3] = oacc[dd][4 * g + 3]; }
        else { v[0] = oacc[2 + dd][4 * g]; v[1] = oacc[2 + dd][4 * g + 1]; v[2] = oacc[2 + dd][4 * g + 2]; v[3] = oacc[2 + dd][4 * g + 3]; }
        *(__attribute__((address_space(3))) f32x4*)(L + CELL(dd * 2 + (g >> 1)) + w * 2048 + (g & 1) * 1024 + ln * 16) = v;
      }
    WAITVM(0);
    BARRIER();
    DMAU(0);
    RDW(A[4], 4); RDW(A[5], 5); RDW(A[6], 6); RDW(A[7], 7);
    const float mp = ml[(w ^ 4) * 512 + r32e], lp = ml[(w ^ 4) * 512 + 32 + r32e];
    WAITL();
    DMAU(1); DMAU(2); DMAU(3); DMAU(4);
    const float mf = fmaxf(m, mp);
    const float wa = __builtin_amdgcn_exp2f(m - mf), wb = __builtin_amdgcn_exp2f(mp - mf);
    const float inv = 1.f / (wa * l + wb * lp);
    const float fa = wa * inv, fb = wb * inv;
    const char* ost = ows + ((size_t)(b * 4 + qg) * 256 + head * 8 + 4 * kh) * 1024;
#pragma unroll
    for (int dd = 0; dd < 2; ++dd) {
      float o[16];
#pragma unroll
      for (int g = 0; g < 4; ++g) {
        f32x4 v = *(__attribute__((address_space(3))) f32x4*)(L + CELL(dd * 2 + (g >> 1)) + (w ^ 4) * 2048 + (g & 1) * 1024 + ln * 16);
#pragma unroll
        for (int e = 0; e < 4; ++e) {
          const float own = kh ? oacc[2 + dd][4 * g + e] : oacc[dd][4 * g + e];
          o[4 * g + e] = own * fa + v[e] * fb;
        }
      }
#pragma unroll
      for (int sp = 0; sp < 2; ++sp) {
        bf16x8 f = pack8(o[8 * sp + 0], o[8 * sp + 1], o[8 * sp + 2], o[8 * sp + 3], o[8 * sp + 4], o[8 * sp + 5], o[8 * sp + 6], o[8 * sp + 7]);
        asm volatile("global_store_dwordx4 %0, %1, %2 offset:%3 sc1\n\ts_nop 1" :: "v"((unsigned)(ln * 16)), "v"(f), "s"(ost), "i"((dd * 2 + sp) * 1024) : "memory");
      }
    }
    BARRIER();
  }
  DMAU(5); DMAU(6); DMAU(7); DMAU(8); DMAU(9);
#define ASTEP(u, n) do { WAITN(n); RDW(Bq[(u) - 10], RING(u)); WAITL(); DMAU(u); } while (0)
#define MSTEP(j, n) do { WAITN(n); PIN(of[(j) & 7]); \
    if ((j) < 24) { acc[0] = MFMA32(Bq[(j) % 24][0], of[(j) & 7], acc[0]); acc[1] = MFMA32(Bq[(j) % 24][1], of[(j) & 7], acc[1]); } \
    else { acc[0] = MFMA32(A[(j) & 7][0], of[(j) & 7], acc[0]); acc[1] = MFMA32(A[(j) & 7][1], of[(j) & 7], acc[1]); } } while (0)
  f32x16 acc[2] = {};
  ASTEP(10, 22); ASTEP(11, 22); WAITN(14); PUBLISH(1u); ASTEP(12, 22); ASTEP(13, 22); ASTEP(14, 22); ASTEP(15, 18); ASTEP(16, 18); ASTEP(17, 18); LDFLAGS(); ASTEP(18, 19); ASTEP(19, 19); ASTEP(20, 19); ASTEP(21, 19);
  WAITN(8); GETRDY();
  POLL((rdy >> HORD(0)) & 1u);
  LDO(0); LDO(1); LDO(2); LDO(3); LDO(4); LDO(5); LDO(6); LDO(7);
  MSTEP(0, 7); RDW(Bq[12], RING(22)); WAITL(); DMAU(22); POLL((rdy >> HORD(1)) & 1u); LDFLAGS(); LDO(8); GAP();
  MSTEP(1, 10); RDW(Bq[13], RING(23)); WAITL(); DMAU(23); LDO(9); GAP();
  MSTEP(2, 12); RDW(Bq[14], RING(14)); LDO(10); GAP();
  MSTEP(3, 12); RDW(Bq[15], RING(15)); LDO(11); GAP();
  MSTEP(4, 12); RDW(Bq[16], RING(16)); LDO(12); GAP();
  MSTEP(5, 12); RDW(Bq[17], RING(17)); LDO(13); GAP();
  MSTEP(6, 12); RDW(Bq[18], RING(18)); LDO(14); GAP();
  MSTEP(7, 12); RDW(Bq[19], RING(19)); LDO(15); GAP();
  MSTEP(8, 9); RDW(Bq[20], RING(20)); GETRDY(); POLL((rdy >> HORD(2)) & 1u); LDFLAGS(); LDO(16); GAP();
  MSTEP(9, 8); RDW(Bq[21], RING(21)); LDO(17); GAP();
  MSTEP(10, 8); RDW(Bq[22], RING(22)); LDO(18); GAP();
  MSTEP(11, 8); RDW(Bq[23], RING(23)); LDO(19); GAP();
  MSTEP(12, 8); LDO(20); GAP();
  MSTEP(13, 8); LDO(21); GAP();
  MSTEP(14, 8); LDO(22); GAP();
  MSTEP(15, 8); LDO(23); GAP();
  MSTEP(16, 7); GETRDY(); POLL((rdy >> HORD(3)) & 1u); LDFLAGS(); LDO(24); GAP();
  MSTEP(17, 8); LDO(25); GAP();
  MSTEP(18, 8); LDO(26); GAP();
  MSTEP(19, 8); LDO(27); GAP();
  MSTEP(20, 8); LDO(28); GAP();
  MSTEP(21, 8); LDO(29); GAP();
  MSTEP(22, 8); LDO(30); GAP();
  MSTEP(23, 8); LDO(31); GAP();
  MSTEP(24, 7); GAP();
  MSTEP(25, 6); GAP();
  MSTEP(26, 5); GAP();
  MSTEP(27, 4); GAP();
  MSTEP(28, 3); GAP();
  MSTEP(29, 2); GAP();
  MSTEP(30, 1); GAP();
  MSTEP(31, 0); GAP();
  GETRDY();
#pragma unroll
  for (int nb = 0; nb < 2; ++nb)
#pragma unroll
    for (int g = 0; g < 4; ++g) {
      f32x4 v = {acc[nb][4 * g], acc[nb][4 * g + 1], acc[nb][4 * g + 2], acc[nb][4 * g + 3]};
      *(__attribute__((address_space(3))) f32x4*)(L + CELL(nb * 2 + (g >> 1)) + w * 2048 + (g & 1) * 1024 + ln * 16) = v;
    }
  POLL((rdy & 0xF0u) == 0xF0u);
  PUBLISH(2u);
  {
    const int cb = (w >> 1) == 0 ? CELL(0) : (w >> 1) == 1 ? CELL(1) : (w >> 1) == 2 ? CELL(2) : CELL(3);
    f32x4 s = {0.f, 0.f, 0.f, 0.f};
#pragma unroll
    for (int ww = 0; ww < 8; ++ww) s += *(__attribute__((address_space(3))) f32x4*)(L + cb + ww * 2048 + (w & 1) * 1024 + ln * 16);
    const int nb = w >> 2, g = w & 3;
    u32x2 pk = {cvtpk(s[0], s[1]), cvtpk(s[2], s[3])};
    *(u32x2*)((char*)out + ((size_t)(mblk * 32 + r32e) * 512 + nblk * 64 + nb * 32 + 8 * g + 4 * hie) * 2) = pk;
  }
}

extern "C" void kernel_launch(void* const* d_in, const int* in_sizes, int n_in, void* d_out, int out_size, void* d_ws, size_t ws_size,
                              hipStream_t stream) {
  const __hip_bfloat16* x = (const __hip_bfloat16*)d_in[0];
  const __hip_bfloat16* kc = (const __hip_bfloat16*)d_in[1];
  const __hip_bfloat16* vc = (const __hip_bfloat16*)d_in[2];
  const __hip_bfloat16* wq = (const __hip_bfloat16*)d_in[3];
  const __hip_bfloat16* wo = (const __hip_bfloat16*)d_in[4];
  __hip_bfloat16* out = (__hip_bfloat16*)d_out;
  (void)hipFuncSetAttribute((const void*)fused_kernel, hipFuncAttributeMaxDynamicSharedMemorySize, LDS1);
  fused_kernel<<<256, 512, LDS1, stream>>>(x, kc, vc, wq, wo, (char*)d_ws, (unsigned*)((char*)d_ws + (16u << 20)), out);
}
```

```cpp
#define P1 1000
#define N1 3
#define DP 20
#define P2 900
#define PSLACK 400
#define KS5 15
#define KS6 15
#include <hip/hip_runtime.h>
#include <hip/hip_bf16.h>
#include <stdint.h>

typedef __attribute__((ext_vector_type(8))) short bf16x8;
typedef __attribute__((ext_vector_type(4))) short s16x4;
typedef __attribute__((ext_vector_type(16))) float f32x16;
typedef __attribute__((ext_vector_type(4))) float f32x4;
typedef __attribute__((ext_vector_type(4))) unsigned u32x4;
typedef __attribute__((ext_vector_type(2))) unsigned u32x2;
typedef __attribute__((address_space(3))) char lds_char;
typedef short v4i16_t __attribute__((ext_vector_type(4)));

constexpr int NB = 8, SQ = 128, DM = 512, NH = 32, SK = 2048, DH = 128;
constexpr int NT = SK / 64;
constexpr int LDS1 = 163840;
#define NPB 6
#define PBUF(c) (LDS1 - NPB * 16384 + (((c) + 4 * NPB - 16) % NPB) * 16384)
__device__ constexpr int tile_step(int n) { return n < 10 - NPB ? 2 * n : n + 6; }
__device__ constexpr int p_after(int c) {
  int mx = (c > 0 ? c : 0) - 1 + NPB; if (mx > 15) mx = 15;
  int n = 4 * (mx - (c + 1));
  for (int v = 2; v <= 6; v += 2) if (tile_step(v) < c && tile_step(v) >= c + 1 - NPB) n += 4;
  return n;
}
#define KR 10
constexpr int XCH_OFF = 131072;
constexpr float CSC = 0.08838834764831845f * 1.4426950408889634f;

#define POL "nt"
#define MFMA32(a, b, c) __builtin_amdgcn_mfma_f32_32x32x16_bf16(a, b, c, 0, 0, 0)

typedef __attribute__((ext_vector_type(2))) float f32x2;
typedef __attribute__((ext_vector_type(2))) __bf16 bf16x2_t;
__device__ __forceinline__ unsigned cvtpk(float lo, float hi) {
  f32x2 v = {lo, hi};
  return __builtin_bit_cast(unsigned, __builtin_convertvector(v, bf16x2_t));
}
__device__ __forceinline__ bf16x8 pack8(float a0, float a1, float a2, float a3, float a4, float a5, float a6, float a7) {
  u32x4 w = {cvtpk(a0, a1), cvtpk(a2, a3), cvtpk(a4, a5), cvtpk(a6, a7)};
  return __builtin_bit_cast(bf16x8, w);
}
__device__ __forceinline__ bf16x8 lds_r128(lds_char* p) { return *(__attribute__((address_space(3))) bf16x8*)p; }
__device__ __forceinline__ s16x4 lds_tr(lds_char* p) {
  return __builtin_bit_cast(s16x4, __builtin_amdgcn_ds_read_tr16_b64_v4i16((__attribute__((address_space(3))) v4i16_t*)p));
}
__device__ __forceinline__ bf16x8 cat8(s16x4 a, s16x4 b) { return __builtin_shufflevector(a, b, 0, 1, 2, 3, 4, 5, 6, 7); }

__device__ __forceinline__ void dma4(unsigned o0, unsigned o1, unsigned o2, unsigned o3, const void* sbase, unsigned ldsaddr) {
  unsigned keep;
  asm volatile(
      "s_mov_b32 %0, m0\n\t"
      "s_mov_b32 m0, %6\n\t"
      "s_nop 0\n\t"
      "global_load_lds_dwordx4 %1, %5 " POL "\n\t"
      "global_load_lds_dwordx4 %2, %5 offset:1024 " POL "\n\t"
      "global_load_lds_dwordx4 %3, %5 offset:2048 " POL "\n\t"
      "global_load_lds_dwordx4 %4, %5 offset:3072 " POL "\n\t"
      "s_mov_b32 m0, %0"
      : "=&s"(keep) : "v"(o0), "v"(o1), "v"(o2), "v"(o3), "s"(sbase), "s"(ldsaddr) : "memory");
}
__device__ __forceinline__ void dma2(unsigned o0, unsigned o1, const void* sbase, unsigned ldsaddr) {
  unsigned keep;
  asm volatile(
      "s_mov_b32 %0, m0\n\t"
      "s_mov_b32 m0, %4\n\t"
      "s_nop 0\n\t"
      "global_load_lds_dwordx4 %1, %3\n\t"
      "global_load_lds_dwordx4 %2, %3 offset:1024\n\t"
      "s_mov_b32 m0, %0"
      : "=&s"(keep) : "v"(o0), "v"(o1), "s"(sbase), "s"(ldsaddr) : "memory");
}
template <int J> __device__ __forceinline__ void dmap(unsigned o, const void* sbase, unsigned ldsaddr) {
  unsigned keep;
  asm volatile("s_mov_b32 %0, m0\n\ts_mov_b32 m0, %3\n\ts_nop 0\n\tglobal_load_lds_dwordx4 %1, %2 offset:%4 " POL "\n\ts_mov_b32 m0, %0"
               : "=&s"(keep) : "v"(o), "s"(sbase), "s"(ldsaddr), "i"(J * 1024) : "memory");
}
template <int J> __device__ __forceinline__ void dmapw(unsigned o, const void* sbase, unsigned ldsaddr) {
  unsigned keep;
  asm volatile("s_mov_b32 %0, m0\n\ts_mov_b32 m0, %3\n\ts_nop 0\n\tglobal_load_lds_dwordx4 %1, %2 offset:%4\n\ts_mov_b32 m0, %0"
               : "=&s"(keep) : "v"(o), "s"(sbase), "s"(ldsaddr), "i"(J * 1024) : "memory");
}
__device__ __forceinline__ int lane_id() {
  int lane;
  asm volatile("v_mbcnt_lo_u32_b32 %0, -1, 0\n\tv_mbcnt_hi_u32_b32 %0, -1, %0" : "=v"(lane));
  return lane;
}
__device__ __forceinline__ float fadd_s(float a, float b) { float r; asm("v_add_f32_e32 %0, %1, %2" : "=v"(r) : "v"(a), "v"(b)); return r; }
#define GAP() __builtin_amdgcn_sched_barrier(0)
__device__ __forceinline__ int kslot(int i) { return i == 0 ? 0 : i * 32768 - 16384; }
__device__ __forceinline__ int vslot(int i) { return i == 4 ? 147456 : i * 32768 + 32768; }
#define WAITVM(n) asm volatile("s_waitcnt vmcnt(" #n ")" ::: "memory")
#define BARRIER() asm volatile("s_waitcnt lgkmcnt(0)\n\ts_barrier" ::: "memory")
#define PIN(x) asm volatile("" : "+v"(x))

__device__ __forceinline__ float swapmax(float v) {
  auto rr = __builtin_amdgcn_permlane32_swap(__float_as_uint(v), __float_as_uint(v), false, false);
  return fmaxf(__uint_as_float(rr[0]), __uint_as_float(rr[1]));
}
__device__ __forceinline__ float swapadd(float v) {
  auto rr = __builtin_amdgcn_permlane32_swap(__float_as_uint(v), __float_as_uint(v), false, false);
  return __uint_as_float(rr[0]) + __uint_as_float(rr[1]);
}

__global__ __launch_bounds__(512, 2) void fused_kernel(const __hip_bfloat16* __restrict__ x, const __hip_bfloat16* __restrict__ kc,
                                                       const __hip_bfloat16* __restrict__ vc, const __hip_bfloat16* __restrict__ wq,
                                                       const __hip_bfloat16* __restrict__ wo, char* ows, unsigned* flg,
                                                       __hip_bfloat16* __restrict__ out) {
  extern __shared__ __attribute__((aligned(16))) char smem[];
  lds_char* L = (lds_char*)smem;
  const unsigned L0 = (unsigned)(uintptr_t)smem;
  const int tid = threadIdx.x, lane = tid & 63;
  const int w = __builtin_amdgcn_readfirstlane(tid >> 6);
  const int qg = w & 3, kh = w >> 2;
  const int r32 = lane & 31, hi = lane >> 5;
  const int q4 = (lane & 15) >> 2, p4 = lane & 3, g1 = (lane >> 4) & 1;
  const int bid = blockIdx.x;
  const int head = 4 * (bid & 7) + ((bid >> 3) & 3), b = bid >> 5;
  const char* kbase = (const char*)kc + ((size_t)(b * NH + head) * SK) * 256;
  const char* vbase = (const char*)vc + ((size_t)(b * NH + head) * SK) * 256;
  const char* wqbase = (const char*)wq + head * 256;
#define FSTR 256
#define XA 2
  const int xcd = bid & 7, jx = bid >> 3;
  const int nblk = ((xcd & ((8 >> XA) - 1)) << XA) | (jx & ((1 << XA) - 1)), mblk = (xcd >> (3 - XA)) * (32 >> XA) + (jx >> XA);
  int fia = (mblk >> 2) * 32 + w;
  int fib = ((((4 * b) % (32 >> XA)) << XA) | (w & ((1 << XA) - 1))) * 8 + ((((4 * b) / (32 >> XA)) << (3 - XA)) | (w >> XA)) - (32 << XA);
  int fic = bid;
  asm volatile("" : "+s"(fia), "+s"(fib), "+s"(fic));
#define FOFF(lane) (((lane) < 8 ? ((lane) < 4 ? fia + 8 * (lane) : fib + ((lane) << (3 + XA))) : fic) * FSTR)
  unsigned fv;
  asm volatile("global_load_dword %0, %1, %2 sc1" : "=v"(fv) : "v"((unsigned)FOFF(lane)), "s"(flg) : "memory");

  const int drow = lane >> 4, dpos = lane & 15;
  unsigned ko[4];
#pragma unroll
  for (int j = 0; j < 4; ++j) ko[j] = (unsigned)(((w & 3) * 16 + drow) * 256 + ((dpos ^ (4 * j + drow)) << 4));
  const unsigned vo = (unsigned)(((w & 3) * 16 + drow) * 256 + ((dpos ^ (drow << 2)) << 4));
  unsigned wo2[2], xo2[2];
#pragma unroll
  for (int j = 0; j < 2; ++j) {
    wo2[j] = (unsigned)(((w & 3) * 8 + 4 * j + drow) * 8192 + ((dpos ^ (drow << 2)) << 4) - j * 1024);
    xo2[j] = (unsigned)(((w & 3) * 32 + 16 * j + (lane >> 2)) * 1024 + (((lane & 3) ^ drow) << 4) - j * 1024);
  }
  const char* xbase = (const char*)x + (size_t)b * SQ * 1024;

#define TOFF(t) ((size_t)(t) * 16384)
#define ISSUE_K(t, la) dma4(ko[0], ko[1], ko[2], ko[3], kbase + TOFF(t), (la) + (w & 3) * 4096)
#define ISSUE_V(t, la) dma4(vo, vo, vo, vo, vbase + TOFF(t), (la) + (w & 3) * 4096)
  const int crot = 2 * b + ((bid >> 3) & 1) + 8 * ((bid >> 4) & 1);
#define ISSUE_P(c) do { const int cc_ = ((c) + crot) & 15; \
    dma2(wo2[0], wo2[1], wqbase + (size_t)cc_ * 32 * 8192, L0 + PBUF(c) + (w & 3) * 2048); \
    dma2(xo2[0], xo2[1], xbase + cc_ * 64, L0 + PBUF(c) + 8192 + (w & 3) * 2048); } while (0)
#define ISSUE_T(c) do { \
    if (w >= 4) { \
      if (tile_step(2) == (c)) ISSUE_V(0, L0 + vslot(0)); \
      if (tile_step(4) == (c)) ISSUE_V(1, L0 + vslot(1)); \
      if (tile_step(6) == (c)) ISSUE_V(2, L0 + vslot(2)); \
    } else { \
      if (tile_step(0) == (c)) ISSUE_K(0, L0 + kslot(0)); \
      if (tile_step(1) == (c)) ISSUE_K(1, L0 + kslot(1)); \
      if (tile_step(3) == (c)) ISSUE_K(2, L0 + kslot(2)); \
      if (tile_step(5) == (c)) ISSUE_K(3, L0 + kslot(3)); \
      if (tile_step(7) == (c)) ISSUE_K(4, L0 + kslot(4)); \
      if ((c) == KS5) ISSUE_K(5, L0 + kslot(0)); \
      if ((c) == KS6) ISSUE_K(6, L0 + kslot(1)); \
    } } while (0)
  if (w >= 4) {
#pragma unroll
    for (int c = 0; c < NPB; ++c) ISSUE_P(c);
  }

  f32x16 qa[2] = {};
  const int krow = kh * 32 + r32;
  const int kaddr0 = (krow * 256 + ((hi ^ (krow & 15)) << 4)) ^ (kh << 7);
  bf16x8 kf0[8], kf[8];
  {
    const int ps = ((p4 & 1) << 1) | (p4 >> 1);
    int wa[2], xa[2];
#pragma unroll
    for (int dd = 0; dd < 2; ++dd) {
      const int dblk = 2 * kh + dd;
      wa[dd] = (8 * hi + q4) * 256 + (((4 * dblk + 2 * g1 + (ps >> 1)) ^ (q4 << 2)) << 4) + 8 * (ps & 1);
      xa[dd] = 8192 + (qg * 32 + r32) * 64 + (((2 * dd + hi) ^ ((r32 >> 2) & 3)) << 4);
    }
    bf16x8 xf[2][2], af[2][2][2];
#pragma unroll
    for (int c = -1; c < 16; ++c) {
      if (w >= 4 && c < 15) asm volatile("s_waitcnt vmcnt(%0)" :: "i"(p_after(c)) : "memory");
      if (w < 4 && c == KR) WAITVM(4);
      BARRIER();
      if (w >= 4 && c >= 0 && c + NPB < 16) ISSUE_P(c + NPB);
      ISSUE_T(c);
      if (c < 15) {
        lds_char* wb = L + PBUF(c + 1);
#pragma unroll
        for (int s = 0; s < 2; ++s) {
          xf[(c + 1) & 1][s] = lds_r128(wb + xa[s]);
#pragma unroll
          for (int dd = 0; dd < 2; ++dd)
            af[(c + 1) & 1][s][dd] = cat8(lds_tr(wb + wa[dd] + s * 4096), lds_tr(wb + wa[dd] + s * 4096 + 1024));
        }
      }
      if (c >= KR && c < KR + 4) {
#pragma unroll
        for (int s = 0; s < 4; ++s) {
          const int sx = (c - KR) * 4 + s;
          if (sx < 8) kf0[sx] = lds_r128(L + kslot(0) + (kaddr0 ^ (sx << 5)));
          else kf[sx - 8] = lds_r128(L + kslot(1) + (kaddr0 ^ ((sx - 8) << 5)));
        }
      }
      GAP();
      if (c >= 0) {
#pragma unroll
        for (int s = 0; s < 2; ++s)
#pragma unroll
          for (int dd = 0; dd < 2; ++dd) qa[dd] = MFMA32(af[c & 1][s][dd], xf[c & 1][s], qa[dd]);
      }
      GAP();
    }
  }
  bf16x8 qf[8];
  unsigned f0[9];
  f32x16 sc = {}, sn, negm;
  {
    bf16x8 own[4];
#pragma unroll
    for (int dd = 0; dd < 2; ++dd)
#pragma unroll
      for (int sp = 0; sp < 2; ++sp)
      {
        u32x4 t = __builtin_bit_cast(u32x4, pack8(qa[dd][8 * sp + 0], qa[dd][8 * sp + 1], qa[dd][8 * sp + 2], qa[dd][8 * sp + 3],
                                                  qa[dd][8 * sp + 4], qa[dd][8 * sp + 5], qa[dd][8 * sp + 6], qa[dd][8 * sp + 7]));
#pragma unroll
        for (int e = 0; e < 4; ++e) t[e] = cvtpk(__uint_as_float(t[e] << 16) * CSC, __uint_as_float(t[e] & 0xffff0000u) * CSC);
        own[dd * 2 + sp] = __builtin_bit_cast(bf16x8, t);
      }
#pragma unroll
    for (int i = 0; i < 4; ++i) *(__attribute__((address_space(3))) bf16x8*)(L + XCH_OFF + w * 4096 + i * 1024 + lane * 16) = own[i];
#pragma unroll
    for (int i = 0; i < 4; ++i) { qf[i] = own[i]; sc = MFMA32(kf0[i], qf[i], sc); }
    BARRIER();
    PIN(fv);
#pragma unroll
    for (int i = 0; i < 9; ++i) f0[i] = __builtin_amdgcn_readlane(fv, i);
#pragma unroll
    for (int i = 0; i < 4; ++i) {
      qf[4 + i] = lds_r128(L + XCH_OFF + (w ^ 4) * 4096 + i * 1024 + lane * 16);
      sc = MFMA32(kf0[4 + i], qf[4 + i], sc);
    }
  }

  f32x16 oacc[4] = {};
  int vaddr0[4];
#pragma unroll
  for (int d = 0; d < 4; ++d) vaddr0[d] = (kh * 32 + 4 * hi + q4) * 256 + ((d ^ q4 ^ (2 - 2 * kh)) << 6) + g1 * 32 + p4 * 8;

  bf16x8 vf[4][2], pf[2];
  float m;
  {
    float tmax = sc[0];
#pragma unroll
    for (int r = 1; r < 16; ++r) tmax = fmaxf(tmax, sc[r]);
    m = swapmax(tmax);
#pragma unroll
    for (int r = 0; r < 16; ++r) { sc[r] -= m; negm[r] = -m; }
  }
  float l0 = 0.f, l1 = 0.f;
  const char* dbase = w < 4 ? kbase : vbase;
  unsigned dof0 = w < 4 ? ko[0] : vo, dof1 = w < 4 ? ko[1] : vo, dof2 = w < 4 ? ko[2] : vo, dof3 = w < 4 ? ko[3] : vo;
  const int dahead = w < 4 ? 6 : 4;
  int ks = kslot(2), vs = vslot(0), ksp = kslot(1), vsp = vslot(4);

#define RDV(d, s) vf[d][s] = cat8(lds_tr(vb + vaddr0[d] + (s) * 4096), lds_tr(vb + vaddr0[d] + (s) * 4096 + 2048))
#define RDK(s) kf[s] = lds_r128(kb + (kaddr0 ^ ((s) << 5)))
#define EXP(SC, r) SC[r] = __builtin_amdgcn_exp2f(SC[r])
#define CVT(SC, s, e) pw[s][e] = cvtpk(SC[8 * (s) + 2 * (e)], SC[8 * (s) + 2 * (e) + 1])
#define ADD(SC, r) do { l0 = fadd_s(l0, SC[r]); l1 = fadd_s(l1, SC[(r) + 1]); } while (0)
#define PVM(d, s) oacc[d] = MFMA32(vf[d][s], __builtin_bit_cast(bf16x8, pw[s]), oacc[d])
#define QKM(SN, s) SN = MFMA32(kf[s], qf[s], SN)
#define DMA(J, o) do { if (dmode == 1) dmap<J>(o, dptr, dla); else if (dmode == 2) dmapw<J>(o, dptr, dla); } while (0)
#define RESCALE(SN, tmax) do { \
    if (__builtin_amdgcn_ballot_w64(!(tmax <= 8.f)) != 0) { \
      const float mn = m + fmaxf(tmax, 0.f); \
      const float dl = mn - m; \
      const float alpha = __builtin_amdgcn_exp2f(-dl); \
      _Pragma("unroll") for (int d = 0; d < 4; ++d) oacc[d] *= alpha; \
      _Pragma("unroll") for (int r = 0; r < 16; ++r) { SN[r] -= dl; negm[r] = -mn; } \
      l0 *= alpha; l1 *= alpha; m = mn; \
    } } while (0)
#define BODY(SC, SN, HASK) do { \
    lds_char* vb = L + vs; \
    lds_char* kb = L + ks; \
    u32x4 pw[2]; \
    SN = MFMA32(kf[0], qf[0], negm); RDV(0, 0); RDV(1, 0); EXP(SC, 0); EXP(SC, 1); GAP(); \
    QKM(SN, 1); RDV(2, 0); RDV(3, 0); EXP(SC, 2); EXP(SC, 3); CVT(SC, 0, 0); GAP(); \
    QKM(SN, 2); RDV(0, 1); RDV(1, 1); EXP(SC, 4); EXP(SC, 5); CVT(SC, 0, 1); GAP(); \
    QKM(SN, 3); RDV(2, 1); RDV(3, 1); EXP(SC, 6); EXP(SC, 7); CVT(SC, 0, 2); GAP(); \
    QKM(SN, 4); DMA(0, dof0); EXP(SC, 8); EXP(SC, 9); CVT(SC, 0, 3); GAP(); \
    QKM(SN, 5); DMA(1, dof1); EXP(SC, 10); EXP(SC, 11); CVT(SC, 1, 0); GAP(); \
    QKM(SN, 6); DMA(2, dof2); EXP(SC, 12); EXP(SC, 13); CVT(SC, 1, 1); GAP(); \
    QKM(SN, 7); DMA(3, dof3); EXP(SC, 14); EXP(SC, 15); CVT(SC, 1, 2); GAP(); \
    PVM(0, 0); if (HASK) { RDK(0); RDK(1); } CVT(SC, 1, 3); ADD(SC, 0); ADD(SC, 2); GAP(); \
    PVM(1, 0); if (HASK) { RDK(2); RDK(3); } ADD(SC, 4); ADD(SC, 6); GAP(); \
    PVM(2, 0); if (HASK) { RDK(4); RDK(5); } ADD(SC, 8); ADD(SC, 10); GAP(); \
    PVM(3, 0); if (HASK) { RDK(6); RDK(7); } ADD(SC, 12); ADD(SC, 14); GAP(); \
    float t0 = fmaxf(fmaxf(SN[0], SN[1]), SN[2]), t1 = fmaxf(fmaxf(SN[3], SN[4]), SN[5]); \
    PVM(0, 1); t0 = fmaxf(fmaxf(t0, SN[6]), SN[7]); t1 = fmaxf(fmaxf(t1, SN[8]), SN[9]); GAP(); \
    PVM(1, 1); t0 = fmaxf(fmaxf(t0, SN[10]), SN[11]); t1 = fmaxf(fmaxf(t1, SN[12]), SN[13]); GAP(); \
    PVM(2, 1); t0 = fmaxf(fmaxf(t0, SN[14]), SN[15]); t0 = swapmax(fmaxf(t0, t1)); GAP(); \
    PVM(3, 1); GAP(); \
    RESCALE(SN, t0); \
    ksp = ks; vsp = vs; \
    ks = ks == 0 ? 16384 : (ks == 114688 ? 0 : ks + 32768); \
    vs = vs == 131072 ? 147456 : (vs == 147456 ? 32768 : vs + 32768); } while (0)

#define PFAT 30
  unsigned pfv = 0;
#define WOPF(it) do { \
    if ((it) == PFAT) { pfv = (unsigned)(w * 512 + (jx >> XA) * 64 + lane_id()) * 1024u; \
      asm volatile("global_load_dword %0, %0, %1" : "+v"(pfv) : "s"((const char*)wo + nblk * 128) : "memory"); } } while (0)
  unsigned ptg = (unsigned)__builtin_amdgcn_s_memrealtime() << 3;
  int pinc = P1, pcnt = 0;
#define PACEW() do { if (w == 0) { ptg += pinc; ++pcnt; if (pcnt >= N1) { pinc -= DP; if (pinc < P2) pinc = P2; } unsigned t_; \
    while (1) { t_ = (unsigned)__builtin_amdgcn_s_memrealtime() << 3; if ((int)(ptg - t_) <= 0) break; __builtin_amdgcn_s_sleep(1); } \
    if ((int)(t_ - ptg) > PSLACK) ptg = t_ - PSLACK; } } while (0)
  {
    if (w < 4) WAITVM(16); else WAITVM(8);
    BARRIER();
    if (w >= 4) { ISSUE_V(3, L0 + vslot(3)); ISSUE_V(4, L0 + vslot(4)); }
    const int dmode = 0; const char* dptr = dbase; const unsigned dla = 0;
    BODY(sc, sn, true);
  }
#define DSETUP(it) \
    const char* dptr = dbase + TOFF((it) + dahead); \
    const unsigned dla = L0 + (w < 4 ? ksp : vsp) + (w & 3) * 4096;
#pragma unroll 1
  for (int it = 1; it < 25; it += 2) {
    { PACEW(); WAITVM(12); BARRIER(); const int dmode = 1; DSETUP(it); BODY(sn, sc, true); }
    { PACEW(); WAITVM(12); BARRIER(); const int dmode = 1; DSETUP(it + 1); BODY(sc, sn, true); }
  }
  const char* wot = (const char*)wo + nblk * 128 + (size_t)(w & 3) * (8 * 131072) + (3 * 131072 - 24 * 16384);
#define WOFFS() do { const int ln_ = lane_id(), dr_ = ln_ >> 3; \
    dof0 = (unsigned)(dr_ * 1024 + (((ln_ & 7) ^ (((dr_ >> 1) & 1) << 2)) << 4)); \
    dof1 = dof0 + 7168; dof2 = dof0 + 4 * 131072 - 2048; dof3 = dof0 + 4 * 131072 + 8192 - 3072; } while (0)
#define TSETUP(it, ku, vu) \
    asm volatile("" : "+s"(ks), "+s"(vs), "+s"(ksp), "+s"(vsp)); \
    const int dmode = w < 4 ? ((it) + 6 < NT ? 1 : (ku) >= 0 ? 2 : 0) : ((it) + 4 < NT ? 1 : (vu) >= 0 ? 2 : 0); \
    const char* dptr = dmode == 1 ? dbase + TOFF((it) + dahead) : wot + (w < 4 ? (ku) : (vu)) * 16384; \
    const unsigned dla = L0 + (w < 4 ? ksp : vsp) + (w & 3) * 4096;
  { PACEW(); WAITVM(12); BARRIER(); WOPF(25); TSETUP(25, -1, -1); BODY(sn, sc, true); }
  if (w < 4) WOFFS();
  { PACEW(); WAITVM(12); BARRIER(); WOPF(26); TSETUP(26, 24, -1); BODY(sc, sn, true); }
  { PACEW(); WAITVM(12); BARRIER(); WOPF(27); TSETUP(27, 25, -1); BODY(sn, sc, true); }
  if (w >= 4) WOFFS();
  { WAITVM(12); BARRIER(); WOPF(28); TSETUP(28, 26, 27); BODY(sc, sn, true); }
  { WAITVM(12); BARRIER(); WOPF(29); TSETUP(29, 28, 29); BODY(sn, sc, true); }
  { WAITVM(12); BARRIER(); WOPF(30); TSETUP(30, 30, 31); BODY(sc, sn, false); }
  asm volatile("" : "+s"(vs));

  const int ln = lane_id();
  const int r32e = ln & 31, hie = ln >> 5;
  const unsigned wof0 = (unsigned)((ln >> 3) * 1024 + (((ln & 7) ^ ((((ln >> 3) >> 1) & 1) << 2)) << 4)), wof1 = wof0 + 7168;
  const int tw0 = w * 2048 + (4 * hie + ((ln & 15) >> 2)) * 128 + ((((ln & 15) >> 3)) << 6) + ((ln >> 4) & 1) * 32 + (ln & 3) * 8;
  const int tw1 = tw0 ^ 64;
  const char* wob = (const char*)wo + nblk * 128 + (size_t)w * (4 * 131072);
#define CELL(i) ((i) == 0 ? 49152 : (i) == 1 ? 81920 : (i) == 2 ? 114688 : (i) == 3 ? 98304 : (i) == 4 ? 0 : \
                 (i) == 5 ? 131072 : (i) == 6 ? 16384 : (i) == 7 ? 147456 : (i) == 8 ? 32768 : 65536)
#define RDW(dst, i) do { dst[0] = cat8(lds_tr(L + CELL(i) + tw0), lds_tr(L + CELL(i) + tw0 + 1024)); \
                         dst[1] = cat8(lds_tr(L + CELL(i) + tw1), lds_tr(L + CELL(i) + tw1 + 1024)); } while (0)
#define WAITL() asm volatile("s_waitcnt lgkmcnt(0)" ::: "memory")
#define HORD(g) ((g) == 0 ? 0 : (g) == 1 ? 2 : (g) == 2 ? 1 : 3)
#define RING(j) ((j) % 10 < 2 ? (j) % 10 + 4 : (j) % 10 == 2 ? 9 : (j) % 10 < 6 ? (j) % 10 + 3 : (j) % 10 - 6)
#define DMAU(u) dma2(wof0, wof1, wob + HORD((u) >> 3) * 131072 + ((u) & 7) * 16384, L0 + CELL(RING(u)) + w * 2048)
  bf16x8 A[8][2];
  const unsigned foff = (unsigned)FOFF(ln);
  unsigned pv;
  float l;
#define LDFLAGS() asm volatile("global_load_dword %0, %1, %2 sc1" : "=v"(pv) : "v"(foff), "s"(flg) : "memory")
  {
    if (PFAT == 30) WAITVM(5); else WAITVM(4);
    BARRIER();
    WOPF(31);
    RDW(A[4], 4); RDW(A[5], 5);
    lds_char* vb = L + vs;
    u32x4 pw[2];
#pragma unroll
    for (int s = 0; s < 2; ++s)
#pragma unroll
      for (int d = 0; d < 4; ++d) RDV(d, s);
#pragma unroll
    for (int r = 0; r < 8; ++r) EXP(sn, r);
    GAP();
    WAITL();
    DMAU(0); DMAU(1);
    RDW(A[0], 0); RDW(A[1], 1); RDW(A[2], 2); RDW(A[3], 3);
#pragma unroll
    for (int r = 8; r < 16; ++r) EXP(sn, r);
#pragma unroll
    for (int r = 0; r < 16; r += 2) { l0 += sn[r]; l1 += sn[r + 1]; }
#pragma unroll
    for (int s = 0; s < 2; ++s)
#pragma unroll
      for (int e = 0; e < 4; ++e) CVT(sn, s, e);
#define XW(dd) _Pragma("unroll") for (int g = 0; g < 4; ++g) { \
      f32x4 v = {oacc[dd][4 * g], oacc[dd][4 * g + 1], oacc[dd][4 * g + 2], oacc[dd][4 * g + 3]}; \
      *(__attribute__((address_space(3))) f32x4*)(L + CELL((dd) * 2 + (g >> 1)) + w * 2048 + (g & 1) * 1024 + ln * 16) = v; }
    PVM(0, 0); PVM(1, 0); PVM(0, 1); PVM(1, 1); PVM(2, 0); PVM(3, 0);
    l = swapadd(l0 + l1);
    if (hie == 0) { ((float*)(smem + CELL(8)))[w * 512 + r32e] = m; ((float*)(smem + CELL(8)))[w * 512 + 32 + r32e] = l; }
    GAP();
    XW(0); PVM(2, 1); GAP();
    XW(1); PVM(3, 1); GAP();
  }

  const char* ob = ows + ((size_t)mblk * 256 + w * 32) * 1024;
  unsigned f0v = f0[0];
#pragma unroll
  for (int i = 1; i < 8; ++i) asm volatile("v_writelane_b32 %0, %1, %2" : "+v"(f0v) : "s"(f0[i]), "i"(i));
  unsigned rdy;
  bf16x8 of[8], Bq[24][2];
  const unsigned oo = ln * 16;
#define GETRDY() do { PIN(pv); rdy = (unsigned)__builtin_amdgcn_ballot_w64(pv != f0v) & 255u; } while (0)
#define POLL(cond) do { if (!(cond)) { \
      int spins_ = 0; \
      do { __builtin_amdgcn_s_sleep(2); unsigned pq_; \
        asm volatile("global_load_dword %0, %1, %2 sc1\n\ts_waitcnt vmcnt(0)" : "=v"(pq_) : "v"(foff), "s"(flg) : "memory"); \
        rdy = (unsigned)__builtin_amdgcn_ballot_w64(pq_ != f0v) & 255u; \
      } while (!(cond) && ++spins_ < (1 << 18)); } } while (0)
#define PUBLISH(v) do { BARRIER(); if (w == 0 && ln == 0) \
    __hip_atomic_store((__attribute__((address_space(1))) unsigned*)(flg + fic * (FSTR / 4)), f0[8] + (v), __ATOMIC_RELAXED, __HIP_MEMORY_SCOPE_AGENT); } while (0)
#define LDO(j) asm volatile("global_load_dwordx4 %0, %1, %2 offset:%3 sc1" : "=v"(of[(j) & 7]) : "v"(oo), \
      "s"(ob + HORD((j) >> 3) * 8192 + (((j) >> 2) & 1) * 4096), "i"(((j) & 3) * 1024) : "memory")
#define WAITN(n) asm volatile("s_waitcnt vmcnt(%0)" :: "i"(n) : "memory")

  {
    float* ml = (float*)(smem + CELL(8));
    WAITVM(4);
    PIN(pfv);
    BARRIER();
    DMAU(2);
    RDW(A[6], 6); RDW(A[7], 7);
    asm volatile("" ::: "memory");
    const float mp = ml[(w ^ 4) * 512 + r32e], lp = ml[(w ^ 4) * 512 + 32 + r32e];
    asm volatile("" ::: "memory");
    f32x4 xv[8];
#pragma unroll
    for (int g = 0; g < 8; ++g)
      xv[g] = *(__attribute__((address_space(3))) f32x4*)(L + CELL(g >> 1) + (w ^ 4) * 2048 + (g & 1) * 1024 + ln * 16);
    asm volatile("s_waitcnt lgkmcnt(8)" ::: "memory");
    DMAU(3); DMAU(4);
    const float mf = fmaxf(m, mp);
    const float wa = __builtin_amdgcn_exp2f(m - mf), wb = __builtin_amdgcn_exp2f(mp - mf);
    const float inv = 1.f / (wa * l + wb * lp);
    const float fa = wa * inv, fb = wb * inv;
    const char* ost = ows + ((size_t)(b * 4 + qg) * 256 + head * 8 + 4 * kh) * 1024;
#pragma unroll
    for (int dd = 0; dd < 2; ++dd) {
      float o[16];
#pragma unroll
      for (int g = 0; g < 4; ++g) {
        const f32x4 v = xv[dd * 4 + g];
#pragma unroll
        for (int e = 0; e < 4; ++e) {
          const float own = oacc[2 + dd][4 * g + e];
          o[4 * g + e] = own * fa + v[e] * fb;
        }
      }
#pragma unroll
      for (int sp = 0; sp < 2; ++sp) {
        bf16x8 f = pack8(o[8 * sp + 0], o[8 * sp + 1], o[8 * sp + 2], o[8 * sp + 3], o[8 * sp + 4], o[8 * sp + 5], o[8 * sp + 6], o[8 * sp + 7]);
        asm volatile("global_store_dwordx4 %0, %1, %2 offset:%3 sc1\n\ts_nop 1" :: "v"((unsigned)(ln * 16)), "v"(f), "s"(ost), "i"((dd * 2 + sp) * 1024) : "memory");
      }
    }
    BARRIER();
  }
  DMAU(5); DMAU(6); DMAU(7); DMAU(8); DMAU(9);
#define ASTEP(u, n) do { WAITN(n); RDW(Bq[(u) - 10], RING(u)); WAITL(); DMAU(u); } while (0)
#define MSTEP(j, n) do { WAITN(n); PIN(of[(j) & 7]); \
    if ((j) < 24) { acc[0] = MFMA32(Bq[(j) % 24][0], of[(j) & 7], acc[0]); acc[1] = MFMA32(Bq[(j) % 24][1], of[(j) & 7], acc[1]); } \
    else { acc[0] = MFMA32(A[(j) & 7][0], of[(j) & 7], acc[0]); acc[1] = MFMA32(A[(j) & 7][1], of[(j) & 7], acc[1]); } } while (0)
  f32x16 acc[2] = {};
  ASTEP(10, 22); ASTEP(11, 22); WAITN(14); PUBLISH(1u); ASTEP(12, 22); ASTEP(13, 22); ASTEP(14, 22); ASTEP(15, 18); ASTEP(16, 18); ASTEP(17, 18); LDFLAGS(); ASTEP(18, 19); ASTEP(19, 19); ASTEP(20, 19); ASTEP(21, 19);
  WAITN(8); GETRDY();
  POLL((rdy >> HORD(0)) & 1u);
  LDO(0); LDO(1); LDO(2); LDO(3); LDO(4); LDO(5); LDO(6); LDO(7);
  MSTEP(0, 7); RDW(Bq[12], RING(22)); WAITL(); DMAU(22); POLL((rdy >> HORD(1)) & 1u); LDFLAGS(); LDO(8); GAP();
  MSTEP(1, 10); RDW(Bq[13], RING(23)); WAITL(); DMAU(23); LDO(9); GAP();
  MSTEP(2, 12); RDW(Bq[14], RING(14)); LDO(10); GAP();
  MSTEP(3, 12); RDW(Bq[15], RING(15)); LDO(11); GAP();
  MSTEP(4, 12); RDW(Bq[16], RING(16)); LDO(12); GAP();
  MSTEP(5, 12); RDW(Bq[17], RING(17)); LDO(13); GAP();
  MSTEP(6, 12); RDW(Bq[18], RING(18)); LDO(14); GAP();
  MSTEP(7, 12); RDW(Bq[19], RING(19)); LDO(15); GAP();
  MSTEP(8, 9); RDW(Bq[20], RING(20)); GETRDY(); POLL((rdy >> HORD(2)) & 1u); LDFLAGS(); LDO(16); GAP();
  MSTEP(9, 8); RDW(Bq[21], RING(21)); LDO(17); GAP();
  MSTEP(10, 8); RDW(Bq[22], RING(22)); LDO(18); GAP();
  MSTEP(11, 8); RDW(Bq[23], RING(23)); LDO(19); GAP();
  MSTEP(12, 8); LDO(20); GAP();
  MSTEP(13, 8); LDO(21); GAP();
  MSTEP(14, 8); LDO(22); GAP();
  MSTEP(15, 8); LDO(23); GAP();
  MSTEP(16, 7); GETRDY(); POLL((rdy >> HORD(3)) & 1u); LDFLAGS(); LDO(24); GAP();
  MSTEP(17, 8); LDO(25); GAP();
  MSTEP(18, 8); LDO(26); GAP();
  MSTEP(19, 8); LDO(27); GAP();
  MSTEP(20, 8); LDO(28); GAP();
  MSTEP(21, 8); LDO(29); GAP();
  MSTEP(22, 8); LDO(30); GAP();
  MSTEP(23, 8); LDO(31); GAP();
  MSTEP(24, 7); GAP();
  MSTEP(25, 6); GAP();
  MSTEP(26, 5); GAP();
  MSTEP(27, 4); GAP();
  MSTEP(28, 3); GAP();
  MSTEP(29, 2); GAP();
  MSTEP(30, 1); GAP();
  MSTEP(31, 0); GAP();
  GETRDY();
#pragma unroll
  for (int nb = 0; nb < 2; ++nb)
#pragma unroll
    for (int g = 0; g < 4; ++g) {
      f32x4 v = {acc[nb][4 * g], acc[nb][4 * g + 1], acc[nb][4 * g + 2], acc[nb][4 * g + 3]};
      *(__attribute__((address_space(3))) f32x4*)(L + CELL(nb * 2 + (g >> 1)) + w * 2048 + (g & 1) * 1024 + ln * 16) = v;
    }
  POLL((rdy & 0xF0u) == 0xF0u);
  PUBLISH(2u);
  {
    const int cb = (w >> 1) == 0 ? CELL(0) : (w >> 1) == 1 ? CELL(1) : (w >> 1) == 2 ? CELL(2) : CELL(3);
    f32x4 s = {0.f, 0.f, 0.f, 0.f};
#pragma unroll
    for (int ww = 0; ww < 8; ++ww) s += *(__attribute__((address_space(3))) f32x4*)(L + cb + ww * 2048 + (w & 1) * 1024 + ln * 16);
    const int nb = w >> 2, g = w & 3;
    u32x2 pk = {cvtpk(s[0], s[1]), cvtpk(s[2], s[3])};
    *(u32x2*)((char*)out + ((size_t)(mblk * 32 + r32e) * 512 + nblk * 64 + nb * 32 + 8 * g + 4 * hie) * 2) = pk;
  }
}

extern "C" void kernel_launch(void* const* d_in, const int* in_sizes, int n_in, void* d_out, int out_size, void* d_ws, size_t ws_size,
                              hipStream_t stream) {
  const __hip_bfloat16* x = (const __hip_bfloat16*)d_in[0];
  const __hip_bfloat16* kc = (const __hip_bfloat16*)d_in[1];
  const __hip_bfloat16* vc = (const __hip_bfloat16*)d_in[2];
  const __hip_bfloat16* wq = (const __hip_bfloat16*)d_in[3];
  const __hip_bfloat16* wo = (const __hip_bfloat16*)d_in[4];
  __hip_bfloat16* out = (__hip_bfloat16*)d_out;
  (void)hipFuncSetAttribute((const void*)fused_kernel, hipFuncAttributeMaxDynamicSharedMemorySize, LDS1);
  fused_kernel<<<256, 512, LDS1, stream>>>(x, kc, vc, wq, wo, (char*)d_ws, (unsigned*)((char*)d_ws + (16u << 20)), out);
}
```
